```python
import torch

def _softmax(x, axis=-1):
    return torch.softmax(x, dim=axis)

def _gelu(x):
    return torch.nn.functional.gelu(x, approximate='tanh')

def _silu(x):
    return torch.nn.functional.silu(x)

def _sigmoid(x):
    return torch.sigmoid(x)

def _layernorm(x, axis=-1, eps=1e-05):
    m = x.mean(dim=axis, keepdim=True)
    v = x.var(dim=axis, keepdim=True, unbiased=False)
    return (x - m) / torch.sqrt(v + eps)

def _scatter_add(tgt, idx, src, axis=0):
    return tgt.clone().index_add_(axis, idx, src)

def _segment_sum(src, seg, n):
    out = torch.zeros((n,) + src.shape[1:], dtype=src.dtype)
    return out.index_add_(0, seg, src)

def _pad(x, pw):
    flat = tuple((v for pair in reversed(pw) for v in pair))
    return torch.nn.functional.pad(x, flat)
kernel_name = 'parallel_sum__softmax_full_mha__mixer_gmlp__prompt'

def setup_inputs():

    _w = lambda a: (lambda t: t.to(torch.bfloat16) if t.is_floating_point() else t)(torch.as_tensor(a))

    def _A_setup():
        B, S, D = (1, 2048, 512)
        return {'x': _w((lambda *s: torch.randn(*s, dtype=torch.bfloat16) * 0.05)(B, S, D)), 'wqkv': _w((lambda *s: torch.randn(*s, dtype=torch.bfloat16) * 0.2)(D, 3 * D)), 'wo': _w((lambda *s: torch.randn(*s, dtype=torch.bfloat16) * 0.2)(D, D))}
    _A_in = _A_setup()

    def _B_setup():
        B, S, D, F = (1, 2048, 512, 1792)
        Fh = F // 2
        return {'x': _w((lambda *s: torch.randn(*s, dtype=torch.bfloat16) * 0.05)(B, S, D)), 'w_in': _w((lambda *s: torch.randn(*s, dtype=torch.bfloat16) * 0.8)(D, 2 * Fh)), 'w_sp': _w(0.01 * (lambda *s: torch.randn(*s, dtype=torch.bfloat16) * 0.8)(S, S)), 'b_sp': _w(1.0 + 0.01 * (lambda *s: torch.randn(*s, dtype=torch.bfloat16) * 0.2)(S)), 'w_out': _w((lambda *s: torch.randn(*s, dtype=torch.bfloat16) * 0.2)(Fh, D))}
    _B_in = _B_setup()
    return {'A_x': _A_in['x'], 'A_wqkv': _A_in['wqkv'], 'A_wo': _A_in['wo'], 'B_x': _B_in['x'], 'B_w_in': _B_in['w_in'], 'B_w_sp': _B_in['w_sp'], 'B_b_sp': _B_in['b_sp'], 'B_w_out': _B_in['w_out']}

def reference(A_x, A_wqkv, A_wo, B_x, B_w_in, B_w_sp, B_b_sp, B_w_out):

    def _A_ref(x, wqkv, wo):
        B, S, D = x.shape
        H = 32
        Dh = D // H
        qkv = (lambda x, *d: x.permute(*d))((x @ wqkv).reshape(B, S, 3, H, Dh), 2, 0, 3, 1, 4)
        q, k, v = (qkv[0], qkv[1], qkv[2])
        scale = Dh ** (-0.5)
        scores = torch.einsum('bhqd,bhkd->bhqk', q, k) * scale
        p = _softmax(scores, axis=-1)
        o = torch.einsum('bhqk,bhkd->bhqd', p, v)
        return (lambda x, *d: x.permute(*d))(o, 0, 2, 1, 3).reshape(B, S, D) @ wo

    def _B_ref(x, w_in, w_sp, b_sp, w_out):
        B, S, D = x.shape
        F = w_in.shape[-1]
        Fh = F // 2
        z = _gelu(torch.einsum('bsd,df->bsf', x, w_in))
        z1 = z[..., :Fh].clone()
        z2 = _layernorm(z[..., Fh:], axis=-1)
        g = torch.einsum('ts,bsf->btf', w_sp, z2) + b_sp[None, :, None]
        return torch.einsum('bsf,fd->bsd', z1 * g, w_out)
    x = A_x
    xn = _layernorm(x, axis=-1)
    return x + _A_ref(xn, A_wqkv, A_wo) + _B_ref(xn, B_w_in, B_w_sp, B_b_sp, B_w_out)
```

```cpp
#include <hip/hip_runtime.h>
#include <hip/hip_bf16.h>
#include <stdint.h>

typedef __attribute__((ext_vector_type(2))) __bf16 bf16x2;
typedef __attribute__((ext_vector_type(8))) __bf16 bf16x8;
typedef __attribute__((ext_vector_type(16))) __bf16 bf16x16;
typedef __attribute__((ext_vector_type(2))) float f32x2;
typedef __attribute__((ext_vector_type(4))) float f32x4;
typedef __attribute__((ext_vector_type(8))) float f32x8;
typedef __attribute__((ext_vector_type(16))) float f32x16;
typedef __attribute__((ext_vector_type(2))) unsigned u32x2;
typedef __attribute__((ext_vector_type(4))) unsigned u32x4;
typedef __attribute__((ext_vector_type(4))) short s16x4;
#define LAS __attribute__((address_space(3)))
#define DI __device__ __forceinline__
#define SB() __builtin_amdgcn_sched_barrier(0)
#define PIN(x) asm volatile("" : "+v"(x))
#define WAITV(n) asm volatile("s_waitcnt vmcnt(" #n ")" ::: "memory")
#define MFMA16(a, b, c) __builtin_amdgcn_mfma_f32_16x16x32_bf16(a, b, c, 0, 0, 0)
#define MFMA32(a, b, c) __builtin_amdgcn_mfma_f32_32x32x16_bf16(a, b, c, 0, 0, 0)

constexpr int S_ = 2048, D_ = 512, FH = 896;
constexpr size_t WS_Q = 0, WS_K = 2u << 20, WS_V = 4u << 20, WS_Z1 = 6u << 20, WS_Z2 = 10u << 20, WS_Z2N = 14u << 20,
                 WS_CAT = 18u << 20, WS_FLAG = 24u << 20;
constexpr int CAT_KS = 88;

DI void glds16(const void* sbase, unsigned voff, unsigned ldsdst) {
    unsigned keep;
    asm volatile("s_mov_b32 %0, m0\n\ts_mov_b32 m0, %3\n\ts_nop 0\n\tglobal_load_lds_dwordx4 %1, %2\n\ts_mov_b32 m0, %0"
                 : "=&s"(keep) : "v"(voff), "s"(sbase), "s"(ldsdst) : "memory");
}
DI unsigned pk2(float a, float b) { f32x2 v = {a, b}; return __builtin_bit_cast(unsigned, __builtin_convertvector(v, bf16x2)); }
DI float bflo(unsigned u) { return __uint_as_float(u << 16); }
DI float bfhi(unsigned u) { return __uint_as_float(u & 0xffff0000u); }
DI s16x4 trread(unsigned addr) { return __builtin_amdgcn_ds_read_tr16_b64_v4i16((LAS s16x4*)(uintptr_t)addr); }
DI bf16x8 trfrag(unsigned addr0, unsigned addr1) {
    s16x4 lo = trread(addr0), hi = trread(addr1);
    return __builtin_bit_cast(bf16x8, __builtin_shufflevector(lo, hi, 0, 1, 2, 3, 4, 5, 6, 7));
}
DI bf16x8 ldsfrag(unsigned addr) { return *(const LAS bf16x8*)(uintptr_t)addr; }
DI float gelu_t(float u) {
    const float k1 = -2.3022081985f;
    float w = u * __builtin_fmaf(u * u, k1 * 0.044715f, k1);
    float e = __builtin_amdgcn_exp2f(w);
    return u * __builtin_amdgcn_rcpf(1.0f + e);
}

constexpr int K1_A = 0, K1_B = 3 * 16384, K1_ST = K1_B + 3 * 32768, K1_LDS = K1_ST + 1024 + 2048;
enum { EPI_Q = 0, EPI_K = 1, EPI_V = 2, EPI_Z1 = 3, EPI_Z2 = 4 };

template <int NGW, int EPI>
DI void k1_body(const char* __restrict__ x, const char* __restrict__ W, int ldw  , int ncol0  , int ocol0  ,
                int rp, char* __restrict__ ws, unsigned lds0) {
    constexpr bool ORI2 = (EPI == EPI_V || EPI == EPI_Z2);
    constexpr int NB = NGW * 2;
    constexpr int LB = NGW;
    constexpr int L = 2 + LB;
    const int tid = threadIdx.x, lane = tid & 63;
    const int wid = __builtin_amdgcn_readfirstlane(tid >> 6);
    const int wm = wid & 1, wn = wid >> 1;
    const char* xa = x + (size_t)rp * 128 * 1024;
    const unsigned avoff = (unsigned)((wid * 16 + (lane >> 3)) * 1024 + (((lane & 7) ^ (lane >> 3)) << 4));
    const char* wb = W + (size_t)ncol0 * 2;
    const unsigned bvoff = (unsigned)((wid * 8 + ((lane >> 2) & 7)) * ldw + (lane >> 5) * 64 + ((((lane & 3) ^ ((wid & 1) << 1))) << 4));
    auto issue = [&](int c, int slot) {
        glds16(xa + c * 128, avoff, lds0 + K1_A + slot * 16384 + wid * 2048);
        glds16(xa + c * 128 + 8 * 1024, avoff, lds0 + K1_A + slot * 16384 + wid * 2048 + 1024);
#pragma unroll
        for (int i = 0; i < LB; ++i)
            glds16(wb + (size_t)c * 64 * ldw + i * 128, bvoff, lds0 + K1_B + slot * 32768 + (wid * NB + 2 * i) * 512);
    };
    issue(0, 0); issue(1, 1); issue(2, 2);
    const int r = lane & 15, g = lane >> 4;
    unsigned aaddr[2][4];
#pragma unroll
    for (int kk = 0; kk < 2; ++kk)
#pragma unroll
        for (int j = 0; j < 4; ++j) {
            int row = wm * 64 + (j ^ wn) * 16 + r;
            int ch = (((kk ^ wm) * 4 + g) ^ (r & 7));
            aaddr[kk][j] = lds0 + K1_A + row * 128 + ch * 16;
        }
    const int nt0 = wn * NGW;
    unsigned baddr[2][NGW];
#pragma unroll
    for (int kk = 0; kk < 2; ++kk)
#pragma unroll
        for (int j = 0; j < NGW; ++j) {
            const int kb = (kk ^ wm) * 4 + g, nt = nt0 + j;
            baddr[kk][j] = lds0 + K1_B + (kb * NB + (nt >> 1)) * 512 + ((lane & 15) >> 2) * 64 + (((nt ^ g) & 1) * 32) + (lane & 3) * 8;
        }
    f32x4 acc[4][NGW];
    f32x4 rsum = {0, 0, 0, 0}, gram = {0, 0, 0, 0}, csum[NGW];
#pragma unroll
    for (int j = 0; j < NGW; ++j) { csum[j] = f32x4{0, 0, 0, 0};
#pragma unroll
        for (int i = 0; i < 4; ++i) acc[i][j] = f32x4{0, 0, 0, 0}; }
    u32x4 onesu = {0x3f803f80u, 0x3f803f80u, 0x3f803f80u, 0x3f803f80u};
    const bf16x8 ones = __builtin_bit_cast(bf16x8, onesu);
#pragma unroll
    for (int c = 0; c < 8; ++c) {
        const int slot = c % 3;
        if (c == 0) { if (L == 6) WAITV(12); else WAITV(10); }
        else if (c < 7) { if (L == 6) WAITV(6); else WAITV(5); }
        else WAITV(0);
        asm volatile("s_waitcnt lgkmcnt(0)\n\ts_barrier" ::: "memory");
        if (c >= 1 && c + 2 < 8) issue(c + 2, (c + 2) % 3);
#pragma unroll
        for (int kk = 0; kk < 2; ++kk) {
            bf16x8 af[4], bfr[NGW];
#pragma unroll
            for (int j = 0; j < 4; ++j) af[j] = ldsfrag(aaddr[kk][j] + slot * 16384);
#pragma unroll
            for (int j = 0; j < NGW; ++j) {
                const unsigned a = baddr[kk][j] + slot * 32768;
                bfr[j] = trfrag(a, a + 256);
            }
#pragma unroll
            for (int i = 0; i < 4; ++i)
#pragma unroll
                for (int j = 0; j < NGW; ++j)
                    acc[i][j] = ORI2 ? MFMA16(af[i], bfr[j], acc[i][j]) : MFMA16(bfr[j], af[i], acc[i][j]);
            rsum = MFMA16(ones, af[0], rsum);
            gram = MFMA16(af[0], af[0], gram);
            if (kk == 0) {
#pragma unroll
                for (int j = 0; j < NGW; ++j) csum[j] = MFMA16(ones, bfr[j], csum[j]);
            }
        }
    }
    LAS float* st_r = (LAS float*)(uintptr_t)(lds0 + K1_ST);
    LAS float* st_m = st_r + 128;
    LAS float* st_c = st_r + 256;
    {
        float ss = (lane & 2) ? ((lane & 1) ? gram[3] : gram[2]) : ((lane & 1) ? gram[1] : gram[0]);
        float mean = rsum[0] * (1.0f / 512.0f);
        float var = fmaxf(ss * (1.0f / 512.0f) - mean * mean, 0.0f);
        float rs = __builtin_amdgcn_rsqf(var + 1e-5f);
        if (EPI == EPI_Q) rs *= 0.25f * 1.4426950408889634f;
        if (g == (r >> 2)) { int row = wm * 64 + wn * 16 + r; st_r[row] = rs; st_m[row] = -mean * rs; }
        if (g == 0) {
#pragma unroll
            for (int j = 0; j < NGW; ++j) st_c[wm * 256 + (nt0 + j) * 16 + r] = csum[j][0];
        }
    }
    __syncthreads();
    const int row0 = rp * 128 + wm * 64;
    if (!ORI2) {
        float rs[4], nm[4];
#pragma unroll
        for (int i = 0; i < 4; ++i) { int row = wm * 64 + (i ^ wn) * 16 + r; rs[i] = st_r[row]; nm[i] = st_m[row]; }
#pragma unroll
        for (int j = 0; j < NGW; ++j) {
            f32x4 c0 = *(LAS f32x4*)(st_c + (nt0 + j) * 16 + 4 * g), c1 = *(LAS f32x4*)(st_c + 256 + (nt0 + j) * 16 + 4 * g);
            f32x4 cs = c0 + c1;
            const int ocol = ocol0 + (nt0 + j) * 16;
#pragma unroll
            for (int ip = 0; ip < 2; ++ip) {
                unsigned p[2][2];
#pragma unroll
                for (int s = 0; s < 2; ++s) {
                    const int i = ip * 2 + s;
                    float y[4];
#pragma unroll
                    for (int e = 0; e < 4; ++e) { y[e] = __builtin_fmaf(nm[i], cs[e], acc[i][j][e] * rs[i]); if (EPI == EPI_Z1) y[e] = gelu_t(y[e]); }
                    p[s][0] = pk2(y[0], y[1]); p[s][1] = pk2(y[2], y[3]);
                }
                auto s0 = __builtin_amdgcn_permlane16_swap(p[0][0], p[1][0], false, false);
                auto s1 = __builtin_amdgcn_permlane16_swap(p[0][1], p[1][1], false, false);
                u32x4 v = {s0[0], s1[0], s0[1], s1[1]};
                const int mt = (ip * 2 + (g & 1)) ^ wn;
                const int rt = (row0 >> 5) + (mt >> 1);
                const int key = (mt & 1) * 16 + r;
                size_t off;
                if (EPI == EPI_Z1) off = WS_Z1 + ((size_t)rt * 56 + (ocol >> 4)) * 1024;
                else off = (EPI == EPI_Q ? WS_Q : WS_K) + ((size_t)(ocol >> 4) * 64 + rt) * 1024;
                *(u32x4*)(ws + off + (g >> 1) * 512 + key * 16) = v;
            }
        }
    } else {
        float cs[NGW];
#pragma unroll
        for (int j = 0; j < NGW; ++j) cs[j] = st_c[(nt0 + j) * 16 + r] + st_c[256 + (nt0 + j) * 16 + r];
#pragma unroll
        for (int ip = 0; ip < 2; ++ip) {
            f32x4 rs[2], nm[2];
#pragma unroll
            for (int s = 0; s < 2; ++s) { int row = wm * 64 + ((ip * 2 + s) ^ wn) * 16 + 4 * g; rs[s] = *(LAS f32x4*)(st_r + row); nm[s] = *(LAS f32x4*)(st_m + row); }
#pragma unroll
            for (int j = 0; j < NGW; ++j) {
                const int ocol = ocol0 + (nt0 + j) * 16;
                unsigned p[2][2];
#pragma unroll
                for (int s = 0; s < 2; ++s) {
                    const int i = ip * 2 + s;
                    float y[4];
#pragma unroll
                    for (int e = 0; e < 4; ++e) { y[e] = __builtin_fmaf(nm[s][e], cs[j], acc[i][j][e] * rs[s][e]); if (EPI == EPI_Z2) y[e] = gelu_t(y[e]); }
                    p[s][0] = pk2(y[0], y[1]); p[s][1] = pk2(y[2], y[3]);
                }
                auto s0 = __builtin_amdgcn_permlane16_swap(p[0][0], p[1][0], false, false);
                auto s1 = __builtin_amdgcn_permlane16_swap(p[0][1], p[1][1], false, false);
                u32x4 v = {s0[0], s1[0], s0[1], s1[1]};
                const int mt = (ip * 2 + (g & 1)) ^ wn;
                const int step = (row0 >> 4) + mt;
                size_t off;
                if (EPI == EPI_V) off = WS_V + ((size_t)(ocol >> 4) * 128 + step) * 512 + (g >> 1) * 256 + r * 16;
                else off = WS_Z2 + ((size_t)(step * 2 + (g >> 1)) * FH + ocol + r) * 16;
                *(u32x4*)(ws + off) = v;
            }
        }
    }
}

__global__ __launch_bounds__(512, 2) void k1_kernel(const char* __restrict__ x, const char* __restrict__ wqkv, const char* __restrict__ win, char* __restrict__ ws) {
    extern __shared__ __attribute__((aligned(16))) char smem[];
    const unsigned lds0 = (unsigned)(uintptr_t)(LAS char*)smem;
    const int b = blockIdx.x, xc = b & 7, rr = b >> 3;
    const int rp = (xc & 1) * 8 + (rr & 7), ct = (xc >> 1) * 4 + (rr >> 3);
    if (threadIdx.x == 0) *(unsigned*)(ws + WS_FLAG + b * 4) = 0u;
    if (ct < 2) k1_body<4, EPI_Q>(x, wqkv, 3072, ct * 256, ct * 256, rp, ws, lds0);
    else if (ct < 4) k1_body<4, EPI_K>(x, wqkv, 3072, ct * 256, (ct - 2) * 256, rp, ws, lds0);
    else if (ct < 6) k1_body<4, EPI_V>(x, wqkv, 3072, ct * 256, (ct - 4) * 256, rp, ws, lds0);
    else if (ct < 11) { int c0 = min((ct - 6) * 192, 704); k1_body<3, EPI_Z1>(x, win, 3584, c0, c0, rp, ws, lds0); }
    else { int c0 = min((ct - 11) * 192, 704); k1_body<3, EPI_Z2>(x, win, 3584, FH + c0, c0, rp, ws, lds0); }
}

constexpr int K2_K = 0, K2_V = 63 * 1024, K2_W = 126 * 1024, K2_ONES = K2_W + 32768, K2_MISC = K2_ONES + 1024, K2_LDS = K2_MISC + 1024;
constexpr int SPIN_MAX = 1 << 20;
constexpr unsigned FLAG_TAG = 0x5eed0001u;
constexpr float REF_OFF = 64.0f;

DI void lds_inc(unsigned addr) { __hip_atomic_fetch_add((LAS unsigned*)(uintptr_t)addr, 1u, __ATOMIC_RELAXED, __HIP_MEMORY_SCOPE_WORKGROUP); }
DI void lds_wait(unsigned addr, unsigned n) {
    for (int it = 0; it < SPIN_MAX && __builtin_amdgcn_readfirstlane(__hip_atomic_load((LAS unsigned*)(uintptr_t)addr, __ATOMIC_RELAXED, __HIP_MEMORY_SCOPE_WORKGROUP)) < n; ++it)
        __builtin_amdgcn_s_sleep(1);
    asm volatile("" ::: "memory");
}
DI bf16x16 lo8(const f32x16& x) { f32x8 t = __builtin_shufflevector(x, x, 0, 1, 2, 3, 4, 5, 6, 7); return __builtin_bit_cast(bf16x16, t); }
DI bf16x16 hi8(const f32x16& x) { f32x8 t = __builtin_shufflevector(x, x, 8, 9, 10, 11, 12, 13, 14, 15); return __builtin_bit_cast(bf16x16, t); }
#define SMF(a, b, c) __builtin_amdgcn_smfmac_f32_32x32x32_bf16(a, b, c, sidx, 0, 0)
#define GLD16(dst, voff, sbase, IMM) asm volatile("global_load_dwordx4 %0, %1, %2 offset:" #IMM : "=v"(dst) : "v"(voff), "s"(sbase) : "memory")
#define GLD16S(dst, voff, sbase) asm volatile("global_load_dwordx4 %0, %1, %2 sc1" : "=v"(dst) : "v"(voff), "s"(sbase) : "memory")

DI float max16(const f32x16& s) {
    float a = fmaxf(fmaxf(s[0], s[1]), s[2]), b = fmaxf(fmaxf(s[3], s[4]), s[5]);
    a = fmaxf(fmaxf(a, s[6]), s[7]); b = fmaxf(fmaxf(b, s[8]), s[9]);
    a = fmaxf(fmaxf(a, s[10]), s[11]); b = fmaxf(fmaxf(b, s[12]), s[13]);
    a = fmaxf(fmaxf(a, s[14]), s[15]);
    return fmaxf(a, b);
}
DI float xhalf_max(float m) {
    auto rr = __builtin_amdgcn_permlane32_swap(__float_as_uint(m), __float_as_uint(m), false, false);
    return fmaxf(__uint_as_float(rr[0]), __uint_as_float(rr[1]));
}
DI f32x16 splat16(float v) { f32x16 r; for (int i = 0; i < 16; ++i) r[i] = v; return r; }

DI void att_exact(const char* kg, const char* vg, bf16x8 q, f32x16& o_out) {
    const int lane = threadIdx.x & 63;
    const int sidx = (int)0xDDDDDDDDu;
    f32x16 z = splat16(0.f);
    float m = -INFINITY;
    for (int t = 0; t < 64; ++t) {
        bf16x8 kf = *(const bf16x8*)(kg + t * 1024 + lane * 16);
        f32x16 s = MFMA32(kf, q, z);
        m = fmaxf(m, max16(s));
    }
    m = xhalf_max(m);
    f32x16 c = splat16(-m), o = z;
    const bool isv = (lane & 16) == 0;
    u32x4 onesu = {0x3f803f80u, 0x3f803f80u, 0x3f803f80u, 0x3f803f80u};
    const unsigned vo = (lane >> 5) * 256 + (lane & 15) * 16;
    for (int t = 0; t < 64; ++t) {
        bf16x8 kf = *(const bf16x8*)(kg + t * 1024 + lane * 16);
        u32x4 v0 = *(const u32x4*)(vg + t * 1024 + vo), v1 = *(const u32x4*)(vg + t * 1024 + 512 + vo);
        if (!isv) { v0 = onesu; v1 = onesu; }
        f32x16 s = MFMA32(kf, q, c);
#pragma unroll
        for (int i = 0; i < 16; ++i) s[i] = __builtin_amdgcn_exp2f(s[i]);
        o = SMF(__builtin_bit_cast(bf16x8, v0), lo8(s), o);
        o = SMF(__builtin_bit_cast(bf16x8, v1), hi8(s), o);
    }
    o_out = o;
}

DI void att_store(const f32x16& o, char* dst, int lane) {
    const float rl = 1.0f / o[8];
    unsigned p0 = pk2(o[0] * rl, o[1] * rl), p1 = pk2(o[2] * rl, o[3] * rl), p2 = pk2(o[4] * rl, o[5] * rl), p3 = pk2(o[6] * rl, o[7] * rl);
    auto s0 = __builtin_amdgcn_permlane32_swap(p0, p2, false, false);
    auto s1 = __builtin_amdgcn_permlane32_swap(p1, p3, false, false);
    u32x4 v = {s0[0], s1[0], s0[1], s1[1]};
    *(u32x4*)(dst + lane * 16) = v;
}
DI bool att_bad(const f32x16& o) {
    float a = fmaxf(fmaxf(fabsf(o[0]), fabsf(o[1])), fmaxf(fabsf(o[2]), fabsf(o[3])));
    float b = fmaxf(fmaxf(fabsf(o[4]), fabsf(o[5])), fmaxf(fabsf(o[6]), fabsf(o[7])));
    float s = (o[0] + o[1] + o[2] + o[3]) + (o[4] + o[5] + o[6] + o[7]);
    bool ok = (o[8] < 0x1p120f) && (o[8] > 0x1p-80f) && (fmaxf(a, b) < 0x1p124f) && (s == s);
    return __any(!ok);
}

DI void att_role(int head, int qb, int w, char* __restrict__ ws, unsigned lds0) {
    const int lane = threadIdx.x & 63;
    const char* kg = ws + WS_K + (size_t)head * 65536;
    const char* vg = ws + WS_V + (size_t)head * 65536;
    const char* qg = ws + WS_Q + (size_t)head * 65536 + (size_t)(qb * 8 + w * 2) * 1024;
    const unsigned l16 = lane * 16;
    const unsigned vo = (lane >> 5) * 256 + (lane & 15) * 16;
    u32x4 qa, qb_, k0, k1, v00, v01;
    GLD16(qa, l16, qg, 0); GLD16(qb_, l16, qg, 1024);
    GLD16(k0, l16, kg, 0); GLD16(k1, l16, kg, 1024);
    GLD16(v00, vo, vg, 0); GLD16(v01, vo, vg, 512);
#pragma unroll
    for (int i = 0; i < 32; ++i) {
        int t = 1 + 2 * i + (w >> 1); if (t > 63) t = 63;
        const char* src = ((w & 1) ? vg : kg) + t * 1024;
        glds16(src, l16, lds0 + ((w & 1) ? K2_V : K2_K) + (t - 1) * 1024);
    }
    asm volatile("s_waitcnt vmcnt(32)" : "+v"(qa), "+v"(qb_), "+v"(k0), "+v"(k1), "+v"(v00), "+v"(v01) :: "memory");
    const bf16x8 qA = __builtin_bit_cast(bf16x8, qa), qB = __builtin_bit_cast(bf16x8, qb_);
    int sidx = (int)0xDDDDDDDDu; PIN(sidx);
    f32x16 cA, cB;
    {
        f32x16 z = splat16(0.f);
        f32x16 s0 = MFMA32(__builtin_bit_cast(bf16x8, k0), qA, z), s1 = MFMA32(__builtin_bit_cast(bf16x8, k1), qA, z);
        f32x16 s2 = MFMA32(__builtin_bit_cast(bf16x8, k0), qB, z), s3 = MFMA32(__builtin_bit_cast(bf16x8, k1), qB, z);
        float mA = xhalf_max(fmaxf(max16(s0), max16(s1))), mB = xhalf_max(fmaxf(max16(s2), max16(s3)));
        cA = splat16(-(mA + REF_OFF)); cB = splat16(-(mB + REF_OFF));
        PIN(cA); PIN(cB);
    }
    {
        u32x4 onesu = {0x3f803f80u, 0x3f803f80u, 0x3f803f80u, 0x3f803f80u};
        if (lane & 16) { v00 = onesu; v01 = onesu; }
    }
    bf16x8 kf = __builtin_bit_cast(bf16x8, k0), kn = __builtin_bit_cast(bf16x8, k1);
    bf16x8 v0 = __builtin_bit_cast(bf16x8, v00), v1 = __builtin_bit_cast(bf16x8, v01);
    f32x16 oA = splat16(0.f), oB = splat16(0.f), sA, sB;
    sA = MFMA32(kf, qA, cA);
    WAITV(28); lds_inc(lds0 + K2_MISC + 0);
    unsigned kaddr = lds0 + K2_K + l16;
    unsigned vaddr = (lane & 16) ? (lds0 + K2_ONES + vo) : (lds0 + K2_V + vo);
    const unsigned vinc = (lane & 16) ? 0u : 1024u;
    lds_wait(lds0 + K2_MISC + 0, 4 * 64);
#define EXPS(X, a, b) do { _Pragma("unroll") for (int i_ = a; i_ < b; ++i_) X[i_] = __builtin_amdgcn_exp2f(X[i_]); PIN(X); SB(); } while (0)
#define STEP(LAST) do { \
        sB = MFMA32(kf, qB, cB); PIN(sB); SB(); \
        if (!(LAST)) { kf = ldsfrag(kaddr); kaddr += 1024; } SB(); \
        EXPS(sA, 0, 8); EXPS(sA, 8, 12); \
        oA = SMF(v0, lo8(sA), oA); PIN(oA); SB(); \
        EXPS(sA, 12, 16); EXPS(sB, 0, 4); \
        oA = SMF(v1, hi8(sA), oA); PIN(oA); SB(); \
        EXPS(sB, 4, 8); \
        if (!(LAST)) { sA = MFMA32(kf, qA, cA); PIN(sA); } SB(); \
        EXPS(sB, 8, 12); \
        oB = SMF(v0, lo8(sB), oB); PIN(oB); SB(); \
        if (!(LAST)) { v0 = ldsfrag(vaddr); } SB(); \
        EXPS(sB, 12, 16); \
        oB = SMF(v1, hi8(sB), oB); PIN(oB); SB(); \
        if (!(LAST)) { v1 = ldsfrag(vaddr + 512); vaddr += vinc; } SB(); \
    } while (0)
    (void)kn;
    for (int t = 0; t < 63; ++t) {
        if (t == 4) { WAITV(20); lds_inc(lds0 + K2_MISC + 4); }
        if (t == 8) lds_wait(lds0 + K2_MISC + 4, 4 * 64);
        if (t == 16) { WAITV(0); lds_inc(lds0 + K2_MISC + 8); }
        if (t == 24) lds_wait(lds0 + K2_MISC + 8, 4 * 64);
        if (t == 50) { asm volatile("s_waitcnt lgkmcnt(0)" ::: "memory"); lds_inc(lds0 + K2_MISC + 12); }
        STEP(false);
    }
    STEP(true);
    if (__builtin_expect(att_bad(oA), 0)) att_exact(kg, vg, qA, oA);
    if (__builtin_expect(att_bad(oB), 0)) att_exact(kg, vg, qB, oB);
    char* dst = ws + WS_CAT + ((size_t)(qb * 8 + w * 2) * CAT_KS + head) * 1024;
    att_store(oA, dst, lane);
    att_store(oB, dst + CAT_KS * 1024, lane);
}

DI float wave_sum(float v) {
#pragma unroll
    for (int o = 1; o < 64; o <<= 1) v += __shfl_xor(v, o);
    return v;
}

DI void spg_role(int b, int w, const char* __restrict__ wsp, const __hip_bfloat16* __restrict__ bsp, char* __restrict__ ws, unsigned lds0) {
    const int lane = threadIdx.x & 63;
    const int xc = b & 7, rr = b >> 3;
    const int tt = xc * 4 + (rr & 3), ft = rr >> 2;
    const bool active = ft < 7;
    const unsigned wring = lds0 + K2_W + w * 8192;
    const char* wbase = wsp + (size_t)tt * 64 * 4096 + w * 1024;
    unsigned wvoff[2];
#pragma unroll
    for (int p = 0; p < 2; ++p) wvoff[p] = (lane >> 3) * 4096 + (((lane & 7) ^ ((4 * p + (lane >> 4)) & 7)) << 4);
    auto issueW = [&](int c) {
#pragma unroll
        for (int i = 0; i < 8; ++i) glds16(wbase + (size_t)i * 32768 + c * 128, wvoff[i & 1], wring + i * 1024);
    };
    {
        const char* src = ws + WS_Z2 + (size_t)b * FH * 16;
        char* dst = ws + WS_Z2N + (size_t)b * FH * 16;
        u32x4 zc[4], z0;
        const unsigned o0 = (w * 224 + lane) * 16;
        const unsigned o3 = (w * 224 + 192 + (lane & 31)) * 16;
        const unsigned zero = 0;
        GLD16(zc[0], o0, src, 0); GLD16(zc[1], o0, src, 1024); GLD16(zc[2], o0, src, 2048); GLD16(zc[3], o3, src, 0); GLD16(z0, zero, src, 0);
        issueW(0);
        asm volatile("s_waitcnt vmcnt(8)" : "+v"(zc[0]), "+v"(zc[1]), "+v"(zc[2]), "+v"(zc[3]), "+v"(z0) :: "memory");
        float sh[8], s1[8], s2[8];
#pragma unroll
        for (int e = 0; e < 4; ++e) { sh[2 * e] = bflo(z0[e]); sh[2 * e + 1] = bfhi(z0[e]); }
        const float m3 = lane < 32 ? 1.f : 0.f;
#pragma unroll
        for (int e = 0; e < 8; ++e) {
            float a = ((e & 1) ? bfhi(zc[0][e >> 1]) : bflo(zc[0][e >> 1])) - sh[e];
            float bb = ((e & 1) ? bfhi(zc[1][e >> 1]) : bflo(zc[1][e >> 1])) - sh[e];
            float c = ((e & 1) ? bfhi(zc[2][e >> 1]) : bflo(zc[2][e >> 1])) - sh[e];
            float d = (((e & 1) ? bfhi(zc[3][e >> 1]) : bflo(zc[3][e >> 1])) - sh[e]) * m3;
            s1[e] = (a + bb) + (c + d);
            s2[e] = (a * a + bb * bb) + (c * c + d * d);
        }
#pragma unroll
        for (int e = 0; e < 8; ++e) { s1[e] = wave_sum(s1[e]); s2[e] = wave_sum(s2[e]); }
        LAS float* part = (LAS float*)(uintptr_t)(lds0 + K2_MISC + 256);
        if (lane == 0) {
#pragma unroll
            for (int e = 0; e < 8; ++e) { part[w * 16 + e] = s1[e]; part[w * 16 + 8 + e] = s2[e]; }
        }
        asm volatile("s_waitcnt lgkmcnt(0)" ::: "memory");
        lds_inc(lds0 + K2_MISC + 16);
        lds_wait(lds0 + K2_MISC + 16, 4 * 64);
        float rs[8], nm[8];
#pragma unroll
        for (int e = 0; e < 8; ++e) {
            float a = (part[e] + part[16 + e]) + (part[32 + e] + part[48 + e]);
            float q = (part[8 + e] + part[24 + e]) + (part[40 + e] + part[56 + e]);
            float mean = a * (1.0f / FH);
            float var = fmaxf(q * (1.0f / FH) - mean * mean, 0.f);
            rs[e] = __builtin_amdgcn_rsqf(var + 1e-5f);
            nm[e] = -(mean + sh[e]) * rs[e];
        }
#pragma unroll
        for (int j = 0; j < 4; ++j) {
            u32x4 v;
#pragma unroll
            for (int e = 0; e < 4; ++e) v[e] = pk2(__builtin_fmaf(bflo(zc[j][e]), rs[2 * e], nm[2 * e]), __builtin_fmaf(bfhi(zc[j][e]), rs[2 * e + 1], nm[2 * e + 1]));
            const unsigned o = j < 3 ? o0 + j * 1024 : o3;
            asm volatile("global_store_dwordx4 %0, %1, %2 sc1\n\ts_nop 1" :: "v"(o), "v"(v), "s"(dst) : "memory");
        }
        WAITV(0);
        unsigned old = 0;
        if (lane == 0) old = __hip_atomic_fetch_add((LAS unsigned*)(uintptr_t)(lds0 + K2_MISC + 20), 1u, __ATOMIC_RELAXED, __HIP_MEMORY_SCOPE_WORKGROUP);
        if (lane == 0 && old == 3) __hip_atomic_store((unsigned*)(ws + WS_FLAG) + b, FLAG_TAG, __ATOMIC_RELAXED, __HIP_MEMORY_SCOPE_AGENT);
    }
    if (!active) return;
    {
        const unsigned* fl = (const unsigned*)(ws + WS_FLAG) + w * 64 + lane;
        for (int it = 0; it < SPIN_MAX && !__all(__hip_atomic_load(fl, __ATOMIC_RELAXED, __HIP_MEMORY_SCOPE_AGENT) == FLAG_TAG); ++it) __builtin_amdgcn_s_sleep(2);
        asm volatile("" ::: "memory");
    }
    const char* zb = ws + WS_Z2N + ((size_t)w * 64 * FH + ft * 128) * 16;
    unsigned zvoff[4];
#pragma unroll
    for (int j = 0; j < 4; ++j) zvoff[j] = (lane >> 5) * (FH * 16) + (j ^ w) * 512 + (lane & 31) * 16;
    u32x4 zf[4][4];
#define ISSUEZ(c, ks) do { const char* zp_ = zb + (size_t)((c) * 4 + (ks)) * (2 * FH * 16); _Pragma("unroll") for (int j_ = 0; j_ < 4; ++j_) GLD16S(zf[ks][j_], zvoff[j_], zp_); } while (0)
    ISSUEZ(0, 0); ISSUEZ(0, 1); ISSUEZ(0, 2); ISSUEZ(0, 3);
    f32x16 acc[4][2];
#pragma unroll
    for (int j = 0; j < 4; ++j) { acc[j][0] = splat16(0.f); acc[j][1] = splat16(0.f); }
    unsigned waddr[4];
#pragma unroll
    for (int ks = 0; ks < 4; ++ks) waddr[ks] = wring + (lane & 31) * 128 + (((ks * 2 + (lane >> 5)) ^ ((lane >> 1) & 7)) << 4);
#define ZWAIT(n, ks) asm volatile("s_waitcnt vmcnt(" #n ")" : "+v"(zf[ks][0]), "+v"(zf[ks][1]), "+v"(zf[ks][2]), "+v"(zf[ks][3]) :: "memory")
#define KSTEP(ks) do { _Pragma("unroll") for (int j_ = 0; j_ < 4; ++j_) { \
        acc[j_][0] = MFMA32(__builtin_bit_cast(bf16x8, zf[ks][j_]), wf[0][ks], acc[j_][0]); \
        acc[j_][1] = MFMA32(__builtin_bit_cast(bf16x8, zf[ks][j_]), wf[1][ks], acc[j_][1]); } \
        PIN(acc[0][0]); PIN(acc[0][1]); PIN(acc[1][0]); PIN(acc[1][1]); PIN(acc[2][0]); PIN(acc[2][1]); PIN(acc[3][0]); PIN(acc[3][1]); SB(); } while (0)
    for (int c = 0; c < 7; ++c) {
        bf16x8 wf[2][4];
        WAITV(16);
#pragma unroll
        for (int ks = 0; ks < 4; ++ks) { wf[0][ks] = ldsfrag(waddr[ks]); wf[1][ks] = ldsfrag(waddr[ks] + 4096); }
        asm volatile("s_waitcnt lgkmcnt(0)" : "+v"(wf[0][0]), "+v"(wf[0][1]), "+v"(wf[0][2]), "+v"(wf[0][3]), "+v"(wf[1][0]), "+v"(wf[1][1]), "+v"(wf[1][2]), "+v"(wf[1][3]) :: "memory");
        issueW(c + 1);
        ZWAIT(20, 0); KSTEP(0); ISSUEZ(c + 1, 0);
        ZWAIT(20, 1); KSTEP(1); ISSUEZ(c + 1, 1);
        ZWAIT(20, 2); KSTEP(2); ISSUEZ(c + 1, 2);
        ZWAIT(20, 3); KSTEP(3); ISSUEZ(c + 1, 3);
    }
    {
        bf16x8 wf[2][4];
        WAITV(16);
#pragma unroll
        for (int ks = 0; ks < 4; ++ks) { wf[0][ks] = ldsfrag(waddr[ks]); wf[1][ks] = ldsfrag(waddr[ks] + 4096); }
        ZWAIT(12, 0); KSTEP(0);
        ZWAIT(8, 1); KSTEP(1);
        ZWAIT(4, 2); KSTEP(2);
        ZWAIT(0, 3); KSTEP(3);
    }
    const int fs0 = ft * 8 + w * 2;
    u32x4 z1[2][2]; float bias[2];
#pragma unroll
    for (int t = 0; t < 2; ++t) {
#pragma unroll
        for (int s = 0; s < 2; ++s) z1[t][s] = *(const u32x4*)(ws + WS_Z1 + ((size_t)(tt * 2 + t) * 56 + fs0 + s) * 1024 + lane * 16);
        bias[t] = __bfloat162float(bsp[tt * 64 + t * 32 + (lane & 31)]);
    }
    lds_wait(lds0 + K2_MISC + 12, 4 * 64);
    auto blk = [&](int dstw, int j) { int idx = dstw * 3 + (j - 1); return lds0 + (idx < 6 ? K2_K + idx * 8192 : K2_V + (idx - 6) * 8192) + lane * 16; };
#pragma unroll
    for (int j = 1; j < 4; ++j) {
        const unsigned a = blk(j ^ w, j);
#pragma unroll
        for (int t = 0; t < 2; ++t)
#pragma unroll
            for (int q = 0; q < 4; ++q) {
                f32x4 v = {acc[j][t][4 * q], acc[j][t][4 * q + 1], acc[j][t][4 * q + 2], acc[j][t][4 * q + 3]};
                *(LAS f32x4*)(uintptr_t)(a + (t * 4 + q) * 1024) = v;
            }
    }
    asm volatile("s_waitcnt lgkmcnt(0)" ::: "memory");
    lds_inc(lds0 + K2_MISC + 24);
    lds_wait(lds0 + K2_MISC + 24, 4 * 64);
#pragma unroll
    for (int j = 1; j < 4; ++j) {
        const unsigned a = blk(w, j);
#pragma unroll
        for (int t = 0; t < 2; ++t)
#pragma unroll
            for (int q = 0; q < 4; ++q) {
                f32x4 v = *(LAS f32x4*)(uintptr_t)(a + (t * 4 + q) * 1024);
#pragma unroll
                for (int e = 0; e < 4; ++e) acc[0][t][4 * q + e] += v[e];
            }
    }
#pragma unroll
    for (int t = 0; t < 2; ++t)
#pragma unroll
        for (int s = 0; s < 2; ++s) {
            auto a0 = __builtin_amdgcn_permlane32_swap(z1[t][s][0], z1[t][s][2], false, false);
            auto a1 = __builtin_amdgcn_permlane32_swap(z1[t][s][1], z1[t][s][3], false, false);
            const unsigned zc[4] = {a0[0], a1[0], a0[1], a1[1]};
            unsigned p[4];
#pragma unroll
            for (int e = 0; e < 4; ++e)
                p[e] = pk2(bflo(zc[e]) * (acc[0][t][8 * s + 2 * e] + bias[t]), bfhi(zc[e]) * (acc[0][t][8 * s + 2 * e + 1] + bias[t]));
            auto s0 = __builtin_amdgcn_permlane32_swap(p[0], p[2], false, false);
            auto s1 = __builtin_amdgcn_permlane32_swap(p[1], p[3], false, false);
            u32x4 v = {s0[0], s1[0], s0[1], s1[1]};
            *(u32x4*)(ws + WS_CAT + ((size_t)(tt * 2 + t) * CAT_KS + 32 + fs0 + s) * 1024 + lane * 16) = v;
        }
}

__global__ __launch_bounds__(512, 2) void k2_kernel(const char* __restrict__ wsp, const __hip_bfloat16* __restrict__ bsp, char* __restrict__ ws) {
    extern __shared__ __attribute__((aligned(16))) char smem[];
    const unsigned lds0 = (unsigned)(uintptr_t)(LAS char*)smem;
    const int tid = threadIdx.x;
    const int wid = __builtin_amdgcn_readfirstlane(tid >> 6);
    if (tid < 256) *(LAS unsigned*)(uintptr_t)(lds0 + K2_ONES + tid * 4) = 0x3f803f80u;
    else *(LAS unsigned*)(uintptr_t)(lds0 + K2_MISC + (tid - 256) * 4) = 0u;
    __syncthreads();
    const int b = blockIdx.x;
    if (wid < 4) att_role((b & 7) * 4 + (b >> 6), (b >> 3) & 7, wid, ws, lds0);
    else spg_role(b, wid - 4, wsp, bsp, ws, lds0);
}

constexpr int K3_SLOTS = 18, K3_LDS = 4 * K3_SLOTS * 2048;

__global__ __launch_bounds__(512, 2) void k3_kernel(const char* __restrict__ x, const char* __restrict__ wo, const char* __restrict__ wout,
                                                    const char* __restrict__ ws, char* __restrict__ out) {
    extern __shared__ __attribute__((aligned(16))) char smem[];
    const unsigned lds0 = (unsigned)(uintptr_t)(LAS char*)smem;
    const int tid = threadIdx.x, lane = tid & 63;
    const int wid = __builtin_amdgcn_readfirstlane(tid >> 6);
    const int wm = wid & 1, kq = wid >> 1;
    const int b = blockIdx.x, xc = b & 7, rr = b >> 3;
    const int tt = xc * 4 + (rr & 3), nt = rr >> 2;
    const unsigned bvoff = ((lane >> 2) & 7) * 1024 + (lane >> 5) * 64 + (lane & 3) * 16;
    const unsigned breg = lds0 + kq * (K3_SLOTS * 2048);
    auto issueB = [&](int s, int slot) {
        const int k = (kq * 22 + s) * 16 + wm * 8;
        const char* src = (k < 512 ? wo + (size_t)k * 1024 : wout + (size_t)(k - 512) * 1024) + nt * 128;
        glds16(src, bvoff, breg + slot * 2048 + wm * 1024);
    };
    const char* ab = ws + WS_CAT + ((size_t)(tt * 2 + wm) * CAT_KS + kq * 22) * 1024;
    const unsigned l16 = lane * 16;
    u32x4 af[22];
#pragma unroll
    for (int s = 0; s < 22; ++s) {
        asm volatile("global_load_dwordx4 %0, %1, %2" : "=v"(af[s]) : "v"(l16), "s"(ab + s * 1024) : "memory");
        if (s < K3_SLOTS) issueB(s, s);
    }
    f32x16 acc[2] = {splat16(0.f), splat16(0.f)};
    const unsigned baddr = breg + (lane >> 5) * 1024 + ((lane & 15) >> 2) * 64 + ((lane >> 4) & 1) * 32 + (lane & 3) * 8;
#define K3STEP(s, slot) do { _Pragma("unroll") for (int n_ = 0; n_ < 2; ++n_) { \
        bf16x8 wf_ = trfrag(baddr + (slot) * 2048 + n_ * 512, baddr + (slot) * 2048 + n_ * 512 + 256); \
        acc[n_] = MFMA32(wf_, __builtin_bit_cast(bf16x8, af[s]), acc[n_]); } } while (0)
#define APIN6(i) "+v"(af[i]), "+v"(af[i + 1]), "+v"(af[i + 2]), "+v"(af[i + 3]), "+v"(af[i + 4]), "+v"(af[i + 5])
    asm volatile("s_waitcnt vmcnt(28)\n\ts_barrier" : APIN6(0) :: "memory");
#pragma unroll
    for (int s = 0; s < 6; ++s) K3STEP(s, s);
    asm volatile("s_waitcnt vmcnt(16) lgkmcnt(0)\n\ts_barrier" : APIN6(6) :: "memory");
#pragma unroll
    for (int s = 18; s < 22; ++s) issueB(s, s - 18);
#pragma unroll
    for (int s = 6; s < 12; ++s) K3STEP(s, s);
    asm volatile("s_waitcnt vmcnt(8)\n\ts_barrier" : APIN6(12) :: "memory");
#pragma unroll
    for (int s = 12; s < 18; ++s) K3STEP(s, s);
    asm volatile("s_waitcnt vmcnt(0)\n\ts_barrier" : APIN6(16) :: "memory");
#pragma unroll
    for (int s = 18; s < 22; ++s) K3STEP(s, s - 18);
    __syncthreads();
#pragma unroll
    for (int n = 0; n < 2; ++n)
#pragma unroll
        for (int q = 0; q < 4; ++q) {
            f32x4 v = {acc[n][4 * q], acc[n][4 * q + 1], acc[n][4 * q + 2], acc[n][4 * q + 3]};
            *(LAS f32x4*)(uintptr_t)(lds0 + (((wm * 4 + kq) * 2 + n) * 4 + q) * 1024 + l16) = v;
        }
    __syncthreads();
    const int n = kq >> 1, q0 = (kq & 1) * 2;
    f32x4 r0 = {0, 0, 0, 0}, r1 = {0, 0, 0, 0};
#pragma unroll
    for (int s = 0; s < 4; ++s) {
        const unsigned a = lds0 + (((wm * 4 + s) * 2 + n) * 4 + q0 + (lane >> 5)) * 1024 + (lane & 31) * 16;
        r0 += *(LAS f32x4*)(uintptr_t)a;
        r1 += *(LAS f32x4*)(uintptr_t)(a + 512);
    }
    const size_t o = (size_t)(tt * 64 + wm * 32 + (lane & 31)) * 1024 + (nt * 64 + kq * 16 + (lane >> 5) * 8) * 2;
    const u32x4 xv = *(const u32x4*)(x + o);
    u32x4 v;
    v[0] = pk2(bflo(xv[0]) + r0[0], bfhi(xv[0]) + r0[1]); v[1] = pk2(bflo(xv[1]) + r0[2], bfhi(xv[1]) + r0[3]);
    v[2] = pk2(bflo(xv[2]) + r1[0], bfhi(xv[2]) + r1[1]); v[3] = pk2(bflo(xv[3]) + r1[2], bfhi(xv[3]) + r1[3]);
    *(u32x4*)(out + o) = v;
}

extern "C" void kernel_launch(void* const* d_in, const int* in_sizes, int n_in, void* d_out, int out_size, void* d_ws, size_t ws_size,
                              hipStream_t stream) {
    const char* x = (const char*)d_in[0];
    const char* wqkv = (const char*)d_in[1];
    const char* wo = (const char*)d_in[2];
    const char* win = (const char*)d_in[4];
    const char* wsp = (const char*)d_in[5];
    const __hip_bfloat16* bsp = (const __hip_bfloat16*)d_in[6];
    const char* wout = (const char*)d_in[7];
    char* ws = (char*)d_ws;
    (void)hipFuncSetAttribute((const void*)k1_kernel, hipFuncAttributeMaxDynamicSharedMemorySize, K1_LDS);
    (void)hipFuncSetAttribute((const void*)k2_kernel, hipFuncAttributeMaxDynamicSharedMemorySize, K2_LDS);
    (void)hipFuncSetAttribute((const void*)k3_kernel, hipFuncAttributeMaxDynamicSharedMemorySize, K3_LDS);
    hipLaunchKernelGGL(k1_kernel, dim3(256), dim3(512), K1_LDS, stream, x, wqkv, win, ws);
    hipLaunchKernelGGL(k2_kernel, dim3(256), dim3(512), K2_LDS, stream, wsp, bsp, ws);
    hipLaunchKernelGGL(k3_kernel, dim3(256), dim3(512), K3_LDS, stream, x, wo, wout, (const char*)ws, (char*)d_out);
}
```

```cpp
#include <hip/hip_runtime.h>
#include <hip/hip_bf16.h>
#include <stdint.h>

typedef __attribute__((ext_vector_type(2))) __bf16 bf16x2;
typedef __attribute__((ext_vector_type(8))) __bf16 bf16x8;
typedef __attribute__((ext_vector_type(16))) __bf16 bf16x16;
typedef __attribute__((ext_vector_type(2))) float f32x2;
typedef __attribute__((ext_vector_type(4))) float f32x4;
typedef __attribute__((ext_vector_type(8))) float f32x8;
typedef __attribute__((ext_vector_type(16))) float f32x16;
typedef __attribute__((ext_vector_type(2))) unsigned u32x2;
typedef __attribute__((ext_vector_type(4))) unsigned u32x4;
typedef __attribute__((ext_vector_type(4))) short s16x4;
#define LAS __attribute__((address_space(3)))
#define DI __device__ __forceinline__
#define SB() __builtin_amdgcn_sched_barrier(0)
#define PIN(x) asm volatile("" : "+v"(x))
#define WAITV(n) asm volatile("s_waitcnt vmcnt(" #n ")" ::: "memory")
#define MFMA16(a, b, c) __builtin_amdgcn_mfma_f32_16x16x32_bf16(a, b, c, 0, 0, 0)
#define MFMA32(a, b, c) __builtin_amdgcn_mfma_f32_32x32x16_bf16(a, b, c, 0, 0, 0)

constexpr int S_ = 2048, D_ = 512, FH = 896;
constexpr size_t WS_Q = 0, WS_K = 2u << 20, WS_V = 4u << 20, WS_Z1 = 6u << 20, WS_Z2 = 10u << 20, WS_Z2N = 14u << 20,
                 WS_CAT = 18u << 20, WS_FLAG = 24u << 20;
constexpr int FLAG_REP = 32, FLAG_STRIDE = 4352;
constexpr int CAT_KS = 88;

#define STAMP_DECL
#define STAMP(i)
#define STAMPC(i)
#define STAMP_FLUSH(kern, wsp_)

DI void glds16(const void* sbase, unsigned voff, unsigned ldsdst) {
    unsigned keep;
    asm volatile("s_mov_b32 %0, m0\n\ts_mov_b32 m0, %3\n\ts_nop 0\n\tglobal_load_lds_dwordx4 %1, %2\n\ts_mov_b32 m0, %0"
                 : "=&s"(keep) : "v"(voff), "s"(sbase), "s"(ldsdst) : "memory");
}
DI unsigned pk2(float a, float b) { f32x2 v = {a, b}; return __builtin_bit_cast(unsigned, __builtin_convertvector(v, bf16x2)); }
DI float bflo(unsigned u) { return __uint_as_float(u << 16); }
DI float bfhi(unsigned u) { return __uint_as_float(u & 0xffff0000u); }
DI s16x4 trread(unsigned addr) { return __builtin_amdgcn_ds_read_tr16_b64_v4i16((LAS s16x4*)(uintptr_t)addr); }
DI bf16x8 trfrag(unsigned addr0, unsigned addr1) {
    s16x4 lo = trread(addr0), hi = trread(addr1);
    return __builtin_bit_cast(bf16x8, __builtin_shufflevector(lo, hi, 0, 1, 2, 3, 4, 5, 6, 7));
}
DI bf16x8 ldsfrag(unsigned addr) { return *(const LAS bf16x8*)(uintptr_t)addr; }
DI float gelu_t(float u) {
    const float k1 = -2.3022081985f;
    float w = u * __builtin_fmaf(u * u, k1 * 0.044715f, k1);
    float e = __builtin_amdgcn_exp2f(w);
    return u * __builtin_amdgcn_rcpf(1.0f + e);
}

constexpr int K1_A = 0, K1_B = 3 * 16384, K1_ST = K1_B + 3 * 32768, K1_LDS = K1_ST + 1024 + 2048;
enum { EPI_Q = 0, EPI_K = 1, EPI_V = 2, EPI_Z1 = 3, EPI_Z2 = 4 };

template <int NGW, int EPI>
DI void k1_body(const char* __restrict__ x, const char* __restrict__ W, int ldw  , int ncol0  , int ocol0  ,
                int rp, char* __restrict__ ws, unsigned lds0) {
    constexpr bool ORI2 = (EPI == EPI_V || EPI == EPI_Z2);
    constexpr int NB = NGW * 2;
    constexpr int LB = NGW;
    constexpr int L = 2 + LB;
    const int tid = threadIdx.x, lane = tid & 63;
    const int wid = __builtin_amdgcn_readfirstlane(tid >> 6);
    const int wm = wid & 1, wn = wid >> 1;
    STAMP_DECL; STAMP(0); STAMPC(14);
    const char* xa = x + (size_t)rp * 128 * 1024;
    const unsigned avoff = (unsigned)((wid * 16 + (lane >> 3)) * 1024 + (((lane & 7) ^ (lane >> 3)) << 4));
    const char* wb = W + (size_t)ncol0 * 2;
    const unsigned bvoff = (unsigned)((wid * 8 + ((lane >> 2) & 7)) * ldw + (lane >> 5) * 64 + ((((lane & 3) ^ ((wid & 1) << 1))) << 4));
    auto issue = [&](int c, int slot) {
        glds16(xa + c * 128, avoff, lds0 + K1_A + slot * 16384 + wid * 2048);
        glds16(xa + c * 128 + 8 * 1024, avoff, lds0 + K1_A + slot * 16384 + wid * 2048 + 1024);
#pragma unroll
        for (int i = 0; i < LB; ++i)
            glds16(wb + (size_t)c * 64 * ldw + i * 128, bvoff, lds0 + K1_B + slot * 32768 + (wid * NB + 2 * i) * 512);
    };
    issue(0, 0); issue(1, 1); issue(2, 2);
    STAMP(1);
    const int r = lane & 15, g = lane >> 4;
    unsigned aaddr[2][4];
#pragma unroll
    for (int kk = 0; kk < 2; ++kk)
#pragma unroll
        for (int j = 0; j < 4; ++j) {
            int row = wm * 64 + (j ^ wn) * 16 + r;
            int ch = (((kk ^ wm) * 4 + g) ^ (r & 7));
            aaddr[kk][j] = lds0 + K1_A + row * 128 + ch * 16;
        }
    const int nt0 = wn * NGW;
    unsigned baddr[2][NGW];
#pragma unroll
    for (int kk = 0; kk < 2; ++kk)
#pragma unroll
        for (int j = 0; j < NGW; ++j) {
            const int kb = (kk ^ wm) * 4 + g, nt = nt0 + j;
            baddr[kk][j] = lds0 + K1_B + (kb * NB + (nt >> 1)) * 512 + ((lane & 15) >> 2) * 64 + (((nt ^ g) & 1) * 32) + (lane & 3) * 8;
        }
    f32x4 acc[4][NGW];
    f32x4 rsum = {0, 0, 0, 0}, gram = {0, 0, 0, 0}, csum[NGW];
#pragma unroll
    for (int j = 0; j < NGW; ++j) { csum[j] = f32x4{0, 0, 0, 0};
#pragma unroll
        for (int i = 0; i < 4; ++i) acc[i][j] = f32x4{0, 0, 0, 0}; }
    u32x4 onesu = {0x3f803f80u, 0x3f803f80u, 0x3f803f80u, 0x3f803f80u};
    const bf16x8 ones = __builtin_bit_cast(bf16x8, onesu);
#pragma unroll
    for (int c = 0; c < 8; ++c) {
        const int slot = c % 3;
        if (c == 0) { if (L == 6) WAITV(12); else WAITV(10); }
        else if (c < 7) { if (L == 6) WAITV(6); else WAITV(5); }
        else WAITV(0);
        asm volatile("s_waitcnt lgkmcnt(0)\n\ts_barrier" ::: "memory");
        STAMP(2 + c);
        if (c >= 1 && c + 2 < 8) issue(c + 2, (c + 2) % 3);
#pragma unroll
        for (int kk = 0; kk < 2; ++kk) {
            bf16x8 af[4], bfr[NGW];
#pragma unroll
            for (int j = 0; j < 4; ++j) af[j] = ldsfrag(aaddr[kk][j] + slot * 16384);
#pragma unroll
            for (int j = 0; j < NGW; ++j) {
                const unsigned a = baddr[kk][j] + slot * 32768;
                bfr[j] = trfrag(a, a + 256);
            }
#pragma unroll
            for (int i = 0; i < 4; ++i)
#pragma unroll
                for (int j = 0; j < NGW; ++j)
                    acc[i][j] = ORI2 ? MFMA16(af[i], bfr[j], acc[i][j]) : MFMA16(bfr[j], af[i], acc[i][j]);
            rsum = MFMA16(ones, af[0], rsum);
            gram = MFMA16(af[0], af[0], gram);
            if (kk == 0) {
#pragma unroll
                for (int j = 0; j < NGW; ++j) csum[j] = MFMA16(ones, bfr[j], csum[j]);
            }
        }
    }
    STAMP(10);
    LAS float* st_r = (LAS float*)(uintptr_t)(lds0 + K1_ST);
    LAS float* st_m = st_r + 128;
    LAS float* st_c = st_r + 256;
    {
        float ss = (lane & 2) ? ((lane & 1) ? gram[3] : gram[2]) : ((lane & 1) ? gram[1] : gram[0]);
        float mean = rsum[0] * (1.0f / 512.0f);
        float var = fmaxf(ss * (1.0f / 512.0f) - mean * mean, 0.0f);
        float rs = __builtin_amdgcn_rsqf(var + 1e-5f);
        if (EPI == EPI_Q) rs *= 0.25f * 1.4426950408889634f;
        if (g == (r >> 2)) { int row = wm * 64 + wn * 16 + r; st_r[row] = rs; st_m[row] = -mean * rs; }
        if (g == 0) {
#pragma unroll
            for (int j = 0; j < NGW; ++j) st_c[wm * 256 + (nt0 + j) * 16 + r] = csum[j][0];
        }
    }
    __syncthreads();
    STAMP(11);
    const int row0 = rp * 128 + wm * 64;
    if (!ORI2) {
        float rs[4], nm[4];
#pragma unroll
        for (int i = 0; i < 4; ++i) { int row = wm * 64 + (i ^ wn) * 16 + r; rs[i] = st_r[row]; nm[i] = st_m[row]; }
#pragma unroll
        for (int j = 0; j < NGW; ++j) {
            f32x4 c0 = *(LAS f32x4*)(st_c + (nt0 + j) * 16 + 4 * g), c1 = *(LAS f32x4*)(st_c + 256 + (nt0 + j) * 16 + 4 * g);
            f32x4 cs = c0 + c1;
            const int ocol = ocol0 + (nt0 + j) * 16;
#pragma unroll
            for (int ip = 0; ip < 2; ++ip) {
                unsigned p[2][2];
#pragma unroll
                for (int s = 0; s < 2; ++s) {
                    const int i = ip * 2 + s;
                    float y[4];
#pragma unroll
                    for (int e = 0; e < 4; ++e) { y[e] = __builtin_fmaf(nm[i], cs[e], acc[i][j][e] * rs[i]); if (EPI == EPI_Z1) y[e] = gelu_t(y[e]); }
                    p[s][0] = pk2(y[0], y[1]); p[s][1] = pk2(y[2], y[3]);
                }
                auto s0 = __builtin_amdgcn_permlane16_swap(p[0][0], p[1][0], false, false);
                auto s1 = __builtin_amdgcn_permlane16_swap(p[0][1], p[1][1], false, false);
                u32x4 v = {s0[0], s1[0], s0[1], s1[1]};
                const int mt = (ip * 2 + (g & 1)) ^ wn;
                const int rt = (row0 >> 5) + (mt >> 1);
                const int key = (mt & 1) * 16 + r;
                size_t off;
                if (EPI == EPI_Z1) off = WS_Z1 + ((size_t)rt * 56 + (ocol >> 4)) * 1024;
                else off = (EPI == EPI_Q ? WS_Q : WS_K) + ((size_t)(ocol >> 4) * 64 + rt) * 1024;
                *(u32x4*)(ws + off + (g >> 1) * 512 + key * 16) = v;
            }
        }
    } else {
        float cs[NGW];
#pragma unroll
        for (int j = 0; j < NGW; ++j) cs[j] = st_c[(nt0 + j) * 16 + r] + st_c[256 + (nt0 + j) * 16 + r];
#pragma unroll
        for (int ip = 0; ip < 2; ++ip) {
            f32x4 rs[2], nm[2];
#pragma unroll
            for (int s = 0; s < 2; ++s) { int row = wm * 64 + ((ip * 2 + s) ^ wn) * 16 + 4 * g; rs[s] = *(LAS f32x4*)(st_r + row); nm[s] = *(LAS f32x4*)(st_m + row); }
#pragma unroll
            for (int j = 0; j < NGW; ++j) {
                const int ocol = ocol0 + (nt0 + j) * 16;
                unsigned p[2][2];
#pragma unroll
                for (int s = 0; s < 2; ++s) {
                    const int i = ip * 2 + s;
                    float y[4];
#pragma unroll
                    for (int e = 0; e < 4; ++e) { y[e] = __builtin_fmaf(nm[s][e], cs[j], acc[i][j][e] * rs[s][e]); if (EPI == EPI_Z2) y[e] = gelu_t(y[e]); }
                    p[s][0] = pk2(y[0], y[1]); p[s][1] = pk2(y[2], y[3]);
                }
                auto s0 = __builtin_amdgcn_permlane16_swap(p[0][0], p[1][0], false, false);
                auto s1 = __builtin_amdgcn_permlane16_swap(p[0][1], p[1][1], false, false);
                u32x4 v = {s0[0], s1[0], s0[1], s1[1]};
                const int mt = (ip * 2 + (g & 1)) ^ wn;
                const int step = (row0 >> 4) + mt;
                size_t off;
                if (EPI == EPI_V) off = WS_V + ((size_t)(ocol >> 4) * 128 + step) * 512 + (g >> 1) * 256 + r * 16;
                else off = WS_Z2 + ((size_t)(step * 2 + (g >> 1)) * FH + ocol + r) * 16;
                *(u32x4*)(ws + off) = v;
            }
        }
    }
    STAMP(12); WAITV(0); STAMP(13);
    STAMP_FLUSH(0, ws);
}

__global__ __launch_bounds__(512, 2) void k1_kernel(const char* __restrict__ x, const char* __restrict__ wqkv, const char* __restrict__ win, char* __restrict__ ws) {
    extern __shared__ __attribute__((aligned(16))) char smem[];
    const unsigned lds0 = (unsigned)(uintptr_t)(LAS char*)smem;
    const int b = blockIdx.x, xc = b & 7, rr = b >> 3;
    const int rp = (xc & 1) * 8 + (rr & 7), ct = (xc >> 1) * 4 + (rr >> 3);
    if (threadIdx.x < FLAG_REP) *(unsigned*)(ws + WS_FLAG + threadIdx.x * FLAG_STRIDE + b * 4) = 0u;
    if (ct < 2) k1_body<4, EPI_Q>(x, wqkv, 3072, ct * 256, ct * 256, rp, ws, lds0);
    else if (ct < 4) k1_body<4, EPI_K>(x, wqkv, 3072, ct * 256, (ct - 2) * 256, rp, ws, lds0);
    else if (ct < 6) k1_body<4, EPI_V>(x, wqkv, 3072, ct * 256, (ct - 4) * 256, rp, ws, lds0);
    else if (ct < 11) { int c0 = min((ct - 6) * 192, 704); k1_body<3, EPI_Z1>(x, win, 3584, c0, c0, rp, ws, lds0); }
    else { int c0 = min((ct - 11) * 192, 704); k1_body<3, EPI_Z2>(x, win, 3584, FH + c0, c0, rp, ws, lds0); }
}

constexpr int K2_K = 0, K2_V = 62 * 1024, K2_W = 126 * 1024, K2_ONES = K2_W + 32768, K2_MISC = K2_ONES + 1024, K2_LDS = K2_MISC + 1024;
enum { SY_KVA = 0, SY_KVB = 4, SY_KVC = 8, SY_P24 = 12, SY_LN = 16, SY_LNST = 20, SY_RED = 24, SY_FLAG = 28, SY_P30 = 32, SY_CMB = 36 };
constexpr int SPIN_MAX = 1 << 20;
constexpr unsigned FLAG_TAG = 0x5eed0001u;
constexpr float REF_OFF = 64.0f;

DI void lds_inc(unsigned addr) { __hip_atomic_fetch_add((LAS unsigned*)(uintptr_t)addr, 1u, __ATOMIC_RELAXED, __HIP_MEMORY_SCOPE_WORKGROUP); }
DI void lds_wait(unsigned addr, unsigned n) {
#pragma unroll 1
    for (int it = 0; it < SPIN_MAX && __builtin_amdgcn_readfirstlane(__hip_atomic_load((LAS unsigned*)(uintptr_t)addr, __ATOMIC_RELAXED, __HIP_MEMORY_SCOPE_WORKGROUP)) < n; ++it)
        __builtin_amdgcn_s_sleep(1);
    asm volatile("" ::: "memory");
}
DI bf16x16 lo8(const f32x16& x) { f32x8 t = __builtin_shufflevector(x, x, 0, 1, 2, 3, 4, 5, 6, 7); return __builtin_bit_cast(bf16x16, t); }
DI bf16x16 hi8(const f32x16& x) { f32x8 t = __builtin_shufflevector(x, x, 8, 9, 10, 11, 12, 13, 14, 15); return __builtin_bit_cast(bf16x16, t); }
#define SMF(a, b, c) __builtin_amdgcn_smfmac_f32_32x32x32_bf16(a, b, c, sidx, 0, 0)
#define GLD16(dst, voff, sbase, IMM) asm volatile("global_load_dwordx4 %0, %1, %2 offset:" #IMM : "=v"(dst) : "v"(voff), "s"(sbase) : "memory")
#define GLD16S(dst, voff, sbase) asm volatile("global_load_dwordx4 %0, %1, %2 sc1" : "=v"(dst) : "v"(voff), "s"(sbase) : "memory")

DI float max16(const f32x16& s) {
    float a = fmaxf(fmaxf(s[0], s[1]), s[2]), b = fmaxf(fmaxf(s[3], s[4]), s[5]);
    a = fmaxf(fmaxf(a, s[6]), s[7]); b = fmaxf(fmaxf(b, s[8]), s[9]);
    a = fmaxf(fmaxf(a, s[10]), s[11]); b = fmaxf(fmaxf(b, s[12]), s[13]);
    a = fmaxf(fmaxf(a, s[14]), s[15]);
    return fmaxf(a, b);
}
DI float xhalf_max(float m) {
    auto rr = __builtin_amdgcn_permlane32_swap(__float_as_uint(m), __float_as_uint(m), false, false);
    return fmaxf(__uint_as_float(rr[0]), __uint_as_float(rr[1]));
}
DI f32x16 splat16(float v) { f32x16 r; for (int i = 0; i < 16; ++i) r[i] = v; return r; }

DI void att_exact(const char* kg, const char* vg, bf16x8 q, f32x16& o_out) {
    const int lane = threadIdx.x & 63;
    const int sidx = (int)0xDDDDDDDDu;
    f32x16 z = splat16(0.f);
    float m = -INFINITY;
    for (int t = 0; t < 64; ++t) {
        bf16x8 kf = *(const bf16x8*)(kg + t * 1024 + lane * 16);
        f32x16 s = MFMA32(kf, q, z);
        m = fmaxf(m, max16(s));
    }
    m = xhalf_max(m);
    f32x16 c = splat16(-m), o = z;
    const bool isv = (lane & 16) == 0;
    u32x4 onesu = {0x3f803f80u, 0x3f803f80u, 0x3f803f80u, 0x3f803f80u};
    const unsigned vo = (lane >> 5) * 256 + (lane & 15) * 16;
    for (int t = 0; t < 64; ++t) {
        bf16x8 kf = *(const bf16x8*)(kg + t * 1024 + lane * 16);
        u32x4 v0 = *(const u32x4*)(vg + t * 1024 + vo), v1 = *(const u32x4*)(vg + t * 1024 + 512 + vo);
        if (!isv) { v0 = onesu; v1 = onesu; }
        f32x16 s = MFMA32(kf, q, c);
#pragma unroll
        for (int i = 0; i < 16; ++i) s[i] = __builtin_amdgcn_exp2f(s[i]);
        o = SMF(__builtin_bit_cast(bf16x8, v0), lo8(s), o);
        o = SMF(__builtin_bit_cast(bf16x8, v1), hi8(s), o);
    }
    o_out = o;
}

DI void att_store(const f32x16& o, char* dst, int lane) {
    const float rl = __builtin_amdgcn_rcpf(o[8]);
    unsigned p0 = pk2(o[0] * rl, o[1] * rl), p1 = pk2(o[2] * rl, o[3] * rl), p2 = pk2(o[4] * rl, o[5] * rl), p3 = pk2(o[6] * rl, o[7] * rl);
    auto s0 = __builtin_amdgcn_permlane32_swap(p0, p2, false, false);
    auto s1 = __builtin_amdgcn_permlane32_swap(p1, p3, false, false);
    u32x4 v = {s0[0], s1[0], s0[1], s1[1]};
    *(u32x4*)(dst + lane * 16) = v;
}
DI bool att_bad(const f32x16& o) {
    float a = fmaxf(fmaxf(fabsf(o[0]), fabsf(o[1])), fmaxf(fabsf(o[2]), fabsf(o[3])));
    float b = fmaxf(fmaxf(fabsf(o[4]), fabsf(o[5])), fmaxf(fabsf(o[6]), fabsf(o[7])));
    float s = (o[0] + o[1] + o[2] + o[3]) + (o[4] + o[5] + o[6] + o[7]);
    bool ok = (o[8] < 0x1p120f) && (o[8] > 0x1p-80f) && (fmaxf(a, b) < 0x1p124f) && (s == s);
    return __any(!ok);
}

DI void att_role(int head, int qb, int w, char* __restrict__ ws, unsigned lds0) {
    const int lane = threadIdx.x & 63;
    const int qh = w & 1, kh = w >> 1;
    const char* kg = ws + WS_K + (size_t)head * 65536;
    const char* vg = ws + WS_V + (size_t)head * 65536;
    const char* qg = ws + WS_Q + (size_t)head * 65536 + (size_t)(qb * 8 + qh * 4) * 1024;
    const unsigned l16 = lane * 16;
    const unsigned vo = (lane >> 5) * 256 + (lane & 15) * 16;
    u32x4 qr[4], k0, k1;
    STAMP_DECL; STAMP(0); STAMPC(14);
    const unsigned qo = l16 + kh * 2048, qo2 = l16 + (kh ^ 1) * 2048;
    GLD16(k0, l16, kg + kh * 32768, 0); GLD16(k1, l16, kg + (kh ^ 1) * 32768, 0);
    GLD16(qr[0], qo, qg, 0); GLD16(qr[1], qo, qg, 1024); GLD16(qr[2], qo2, qg, 0); GLD16(qr[3], qo2, qg, 1024);
    const char* ksrc = (qh ? vg : kg + 1024) + kh * 32768;
    const unsigned kdst = lds0 + (qh ? K2_V + kh * 32768 : K2_K + kh * 31744);
#pragma unroll
    for (int i = 0; i < 5; ++i) glds16(ksrc + i * 1024, l16, kdst + i * 1024);
    *(LAS unsigned*)(uintptr_t)(lds0 + K2_ONES + threadIdx.x * 4) = ((threadIdx.x >> 2) & 11) == 0 ? 0x3f803f80u : 0u;
    asm volatile("s_waitcnt lgkmcnt(0)\n\ts_barrier" ::: "memory");
#pragma unroll
    for (int i = 5; i < 13; ++i) glds16(ksrc + i * 1024, l16, kdst + i * 1024);
    STAMP(1);
    asm volatile("s_waitcnt vmcnt(13)" : "+v"(qr[0]), "+v"(qr[1]), "+v"(qr[2]), "+v"(qr[3]), "+v"(k0), "+v"(k1) :: "memory");
    STAMP(2);
#define QF(i) __builtin_bit_cast(bf16x8, qr[i])
    int sidx = (int)0xDDDDDDDDu; PIN(sidx);
    f32x16 cc[4];
#pragma unroll
    for (int i = 0; i < 4; i += 2) {
        f32x16 z = splat16(0.f);
        f32x16 s0 = MFMA32(__builtin_bit_cast(bf16x8, k0), QF(i), z), s1 = MFMA32(__builtin_bit_cast(bf16x8, k1), QF(i), z);
        f32x16 s2 = MFMA32(__builtin_bit_cast(bf16x8, k0), QF(i + 1), z), s3 = MFMA32(__builtin_bit_cast(bf16x8, k1), QF(i + 1), z);
        PIN(s0); PIN(s1); PIN(s2); PIN(s3);
        float mA = xhalf_max(fmaxf(max16(s0), max16(s1))), mB = xhalf_max(fmaxf(max16(s2), max16(s3)));
        cc[i] = splat16(-(mA + REF_OFF)); cc[i + 1] = splat16(-(mB + REF_OFF));
        PIN(cc[i]); PIN(cc[i + 1]);
    }
    bf16x8 kf = __builtin_bit_cast(bf16x8, k0);
    f32x16 oo[4] = {splat16(0.f), splat16(0.f), splat16(0.f), splat16(0.f)}, sA, sB = splat16(0.f);
    sA = MFMA32(kf, QF(0), cc[0]);
    WAITV(8); lds_inc(lds0 + K2_MISC + SY_KVA);
    unsigned kaddr = lds0 + K2_K + kh * 31744 + l16;
    unsigned vaddr = (lane & 16) ? (lds0 + K2_ONES + vo) : (lds0 + K2_V + kh * 32768 + vo);
    const unsigned vinc = (lane & 16) ? 0u : 1024u;
    lds_wait(lds0 + K2_MISC + SY_KVA, 4 * 64);
    bf16x8 v0 = ldsfrag(vaddr), v1 = v0;
    STAMP(3);
#define EXPS(X, a, b) do { _Pragma("unroll") for (int i_ = a; i_ < b; ++i_) X[i_] = __builtin_amdgcn_exp2f(X[i_]); PIN(X); SB(); } while (0)
#define HSTEP(X, Y, i, A1, A2, A3) do { \
        EXPS(X, 0, 2); \
        oo[(i + 3) & 3] = SMF(v1, hi8(Y), oo[(i + 3) & 3]); PIN(oo[(i + 3) & 3]); SB(); A1; SB(); \
        EXPS(X, 2, 5); \
        Y = MFMA32(kf, QF((i + 1) & 3), cc[(i + 1) & 3]); PIN(Y); SB(); A2; SB(); \
        EXPS(X, 5, 11); \
        oo[i] = SMF(v0, lo8(X), oo[i]); PIN(oo[i]); SB(); A3; SB(); \
        EXPS(X, 11, 16); \
    } while (0)
#pragma unroll 1
    for (int seg = 0; seg < 5; ++seg) {
        int n = 4;
        if (seg == 1) { n = 8; STAMP(4); WAITV(0); lds_inc(lds0 + K2_MISC + SY_KVB); lds_wait(lds0 + K2_MISC + SY_KVB, 4 * 64); STAMP(5); STAMPC(10); }
        else if (seg == 2) { n = 13; STAMP(6); STAMPC(11); lds_wait(lds0 + K2_MISC + SY_KVC, 4 * 64); STAMP(7); STAMPC(12); }
        else if (seg == 3) { n = 6; asm volatile("s_waitcnt lgkmcnt(0)" ::: "memory"); lds_inc(lds0 + K2_MISC + SY_P24); }
        else if (seg == 4) { n = 1; asm volatile("s_waitcnt lgkmcnt(0)" ::: "memory"); lds_inc(lds0 + K2_MISC + SY_P30); }
#pragma unroll 1
        for (int i = 0; i < n; ++i) {
            HSTEP(sA, sB, 0, { v1 = ldsfrag(vaddr + 512); vaddr += vinc; }, {}, {});
            HSTEP(sB, sA, 1, {}, {}, {});
            HSTEP(sA, sB, 2, {}, { kf = ldsfrag(kaddr); kaddr += 1024; }, {});
            HSTEP(sB, sA, 3, {}, {}, { v0 = ldsfrag(vaddr); });
        }
    }
    oo[3] = SMF(v1, hi8(sB), oo[3]);
    STAMP(8); STAMPC(13);
    {
        auto cbase = [&](int ww) { return lds0 + ((ww & 1) ? K2_V + (ww >> 1) * 32768 + 25 * 1024 : K2_K + (ww >> 1) * 31744 + 24 * 1024); };
        lds_wait(lds0 + K2_MISC + SY_P30, 4 * 64);
        const unsigned wa = cbase(w), ra = cbase(w ^ 2);
#pragma unroll
        for (int i = 0; i < 2; ++i) {
            f32x4 a = {oo[2 + i][0], oo[2 + i][1], oo[2 + i][2], oo[2 + i][3]}, b = {oo[2 + i][4], oo[2 + i][5], oo[2 + i][6], oo[2 + i][7]};
            *(LAS f32x4*)(uintptr_t)(wa + i * 2048 + l16) = a;
            *(LAS f32x4*)(uintptr_t)(wa + i * 2048 + 1024 + l16) = b;
            *(LAS float*)(uintptr_t)(wa + 4096 + i * 256 + lane * 4) = oo[2 + i][8];
        }
        asm volatile("s_waitcnt lgkmcnt(0)" ::: "memory");
        lds_inc(lds0 + K2_MISC + SY_CMB + qh * 4);
        lds_wait(lds0 + K2_MISC + SY_CMB + qh * 4, 2 * 64);
#pragma unroll
        for (int i = 0; i < 2; ++i) {
            f32x4 a = *(LAS f32x4*)(uintptr_t)(ra + i * 2048 + l16), b = *(LAS f32x4*)(uintptr_t)(ra + i * 2048 + 1024 + l16);
            float l = *(LAS float*)(uintptr_t)(ra + 4096 + i * 256 + lane * 4);
#pragma unroll
            for (int e = 0; e < 4; ++e) { oo[i][e] += a[e]; oo[i][4 + e] += b[e]; }
            oo[i][8] += l;
        }
    }
    if (__builtin_expect(att_bad(oo[0]), 0)) att_exact(kg, vg, QF(0), oo[0]);
    if (__builtin_expect(att_bad(oo[1]), 0)) att_exact(kg, vg, QF(1), oo[1]);
    char* dst = ws + WS_CAT + ((size_t)(qb * 8 + qh * 4 + kh * 2) * CAT_KS + head) * 1024;
    att_store(oo[0], dst, lane);
    att_store(oo[1], dst + CAT_KS * 1024, lane);
    STAMP(9);
    STAMP_FLUSH(1, ws);
}

template <int CTRL> DI float dppf(float x) { return __builtin_bit_cast(float, __builtin_amdgcn_mov_dpp(__builtin_bit_cast(int, x), CTRL, 0xf, 0xf, true)); }
DI float fold32(float a, float b) {
    auto r = __builtin_amdgcn_permlane32_swap(__float_as_uint(a), __float_as_uint(b), false, false);
    return __uint_as_float(r[0]) + __uint_as_float(r[1]);
}
DI float fold16(float a, float b) {
    auto r = __builtin_amdgcn_permlane16_swap(__float_as_uint(a), __float_as_uint(b), false, false);
    return __uint_as_float(r[0]) + __uint_as_float(r[1]);
}
DI float rowsum16(float v) { v += dppf<0xB1>(v); v += dppf<0x4E>(v); v += dppf<0x124>(v); v += dppf<0x128>(v); return v; }

DI void spg_role(int b, int head, int w, const char* __restrict__ wsp, const __hip_bfloat16* __restrict__ bsp, char* __restrict__ ws, unsigned lds0) {
    const int lane = threadIdx.x & 63;
    const int xc = b & 7, rr = b >> 3;
    const int tt = xc * 4 + (rr & 3), ft = rr >> 2;
    const bool active = ft < 7;
    STAMP_DECL; STAMP(0); STAMPC(14);
    __builtin_amdgcn_s_setprio(2);
    const unsigned wring = lds0 + K2_W + w * 8192;
    const char* wbase = wsp + (size_t)tt * 64 * 4096 + w * 1024;
    unsigned wvoff[2];
#pragma unroll
    for (int p = 0; p < 2; ++p) wvoff[p] = (lane >> 3) * 4096 + (((lane & 7) ^ ((4 * p + (lane >> 4)) & 7)) << 4);
    const int rot = 0;
#define CROT(c) (((c) + rot) & 7)
#define ISSUEW(c, i) glds16(wbase + (size_t)(i) * 32768 + CROT(c) * 128, wvoff[(i) & 1], wring + (i) * 1024)
    {
        const char* src = ws + WS_Z2 + (size_t)b * FH * 16;
        char* dst = ws + WS_Z2N + (size_t)b * FH * 16;
        u32x4 zc[4], z0;
        const unsigned o0 = (w * 224 + lane) * 16;
        const unsigned o3 = (w * 224 + 192 + (lane & 31)) * 16;
        const unsigned zero = 0;
        GLD16(zc[0], o0, src, 0); GLD16(zc[1], o0, src, 1024); GLD16(zc[2], o0, src, 2048); GLD16(zc[3], o3, src, 0); GLD16(z0, zero, src, 0);
        *(LAS unsigned*)(uintptr_t)(lds0 + K2_MISC + (threadIdx.x - 256) * 4) = 0u;
        asm volatile("s_waitcnt lgkmcnt(0)\n\ts_barrier" ::: "memory");
        asm volatile("s_waitcnt vmcnt(0)" : "+v"(zc[0]), "+v"(zc[1]), "+v"(zc[2]), "+v"(zc[3]), "+v"(z0) :: "memory");
        STAMP(1);
        float sh[8], s1[8], s2[8];
#pragma unroll
        for (int e = 0; e < 4; ++e) { sh[2 * e] = bflo(z0[e]); sh[2 * e + 1] = bfhi(z0[e]); }
        const float m3 = lane < 32 ? 1.f : 0.f;
#pragma unroll
        for (int e = 0; e < 8; ++e) {
            float a = ((e & 1) ? bfhi(zc[0][e >> 1]) : bflo(zc[0][e >> 1])) - sh[e];
            float bb = ((e & 1) ? bfhi(zc[1][e >> 1]) : bflo(zc[1][e >> 1])) - sh[e];
            float c = ((e & 1) ? bfhi(zc[2][e >> 1]) : bflo(zc[2][e >> 1])) - sh[e];
            float d = (((e & 1) ? bfhi(zc[3][e >> 1]) : bflo(zc[3][e >> 1])) - sh[e]) * m3;
            s1[e] = (a + bb) + (c + d);
            s2[e] = (a * a + bb * bb) + (c * c + d * d);
        }
        f32x4 q;
#pragma unroll
        for (int i = 0; i < 4; ++i) q[i] = rowsum16(fold16(fold32(s1[i], s2[i]), fold32(s1[i + 4], s2[i + 4])));
        LAS float* part = (LAS float*)(uintptr_t)(lds0 + K2_MISC + 256);
        *(LAS f32x4*)(part + w * 16 + (lane >> 4) * 4) = q;
        asm volatile("s_waitcnt lgkmcnt(0)" ::: "memory");
        lds_inc(lds0 + K2_MISC + SY_LN);
        lds_wait(lds0 + K2_MISC + SY_LN, 4 * 64);
        STAMP(2);
        float rs[8], nm[8];
        {
            f32x4 t[4];
#pragma unroll
            for (int i = 0; i < 4; ++i) t[i] = (*(LAS f32x4*)(part + i * 4) + *(LAS f32x4*)(part + 16 + i * 4)) + (*(LAS f32x4*)(part + 32 + i * 4) + *(LAS f32x4*)(part + 48 + i * 4));
#pragma unroll
            for (int e = 0; e < 8; ++e) {
                float mean = t[e >> 2][e & 3] * (1.0f / FH);
                float var = fmaxf(t[2 + (e >> 2)][e & 3] * (1.0f / FH) - mean * mean, 0.f);
                rs[e] = __builtin_amdgcn_rsqf(var + 1e-5f);
                nm[e] = -(mean + sh[e]) * rs[e];
            }
        }
#pragma unroll
        for (int j = 0; j < 4; ++j) {
            u32x4 v;
#pragma unroll
            for (int e = 0; e < 4; ++e) v[e] = pk2(__builtin_fmaf(bflo(zc[j][e]), rs[2 * e], nm[2 * e]), __builtin_fmaf(bfhi(zc[j][e]), rs[2 * e + 1], nm[2 * e + 1]));
            const unsigned o = j < 3 ? o0 + j * 1024 : o3;
            asm volatile("global_store_dwordx4 %0, %1, %2 sc1\n\ts_nop 1" :: "v"(o), "v"(v), "s"(dst) : "memory");
        }
        STAMP(3);
        WAITV(0);
        STAMP(4);
        unsigned old = 0;
        if (lane == 0) old = __hip_atomic_fetch_add((LAS unsigned*)(uintptr_t)(lds0 + K2_MISC + SY_LNST), 1u, __ATOMIC_RELAXED, __HIP_MEMORY_SCOPE_WORKGROUP);
        old = __builtin_amdgcn_readfirstlane(old);
        if (old == 3 && lane < FLAG_REP) __hip_atomic_store((unsigned*)(ws + WS_FLAG + lane * FLAG_STRIDE) + b, FLAG_TAG, __ATOMIC_RELAXED, __HIP_MEMORY_SCOPE_AGENT);
    }
    {
        const char* src = ws + ((w & 1) ? WS_V : WS_K) + (size_t)head * 65536 + (w >> 1) * 32768;
        const unsigned dst = lds0 + ((w & 1) ? K2_V + (w >> 1) * 32768 : K2_K + (w >> 1) * 31744 - 1024);
#pragma unroll 1
        for (int t = 13; t < 32; ++t) glds16(src + t * 1024, lane * 16, dst + t * 1024);
#pragma unroll
        for (int i = 0; i < 8; ++i) ISSUEW(0, i);
        WAITV(8);
        lds_inc(lds0 + K2_MISC + SY_KVC);
    }
    if (!active) { STAMP_FLUSH(1, ws); return; }
    const char* zb = ws + WS_Z2N + ((size_t)w * 64 * FH + ft * 128) * 16;
    unsigned zvoff[4];
#pragma unroll
    for (int j = 0; j < 4; ++j) { zvoff[j] = (lane >> 5) * (FH * 16) + (j ^ w) * 512 + (lane & 31) * 16; PIN(zvoff[j]); }
    u32x4 zf[4][4];
#define ZPTR(c, ks) (zb + (size_t)(CROT(c) * 4 + (ks)) * (2 * FH * 16))
    f32x16 acc[4][2];
#pragma unroll
    for (int j = 0; j < 4; ++j) { acc[j][0] = splat16(0.f); acc[j][1] = splat16(0.f); PIN(acc[j][0]); PIN(acc[j][1]); }
    unsigned waddr[4];
#pragma unroll
    for (int ks = 0; ks < 4; ++ks) { waddr[ks] = wring + (lane & 31) * 128 + (((ks * 2 + (lane >> 5)) ^ ((lane >> 1) & 7)) << 4); PIN(waddr[ks]); }
    if (w == 0) {
        const char* fl = ws + WS_FLAG + (size_t)(b >> 3) * FLAG_STRIDE;
        const unsigned fo = lane * 16;
#pragma unroll 1
        for (int it = 0; it < SPIN_MAX; ++it) {
            u32x4 f;
            asm volatile("global_load_dwordx4 %0, %1, %2 sc1\n\ts_waitcnt vmcnt(0)" : "=&v"(f) : "v"(fo), "s"(fl) : "memory");
            if (__all(f[0] == FLAG_TAG && f[1] == FLAG_TAG && f[2] == FLAG_TAG && f[3] == FLAG_TAG)) break;
            __builtin_amdgcn_s_sleep(4);
        }
        lds_inc(lds0 + K2_MISC + SY_FLAG);
    } else lds_wait(lds0 + K2_MISC + SY_FLAG, 64);
    STAMP(5);
#pragma unroll
    for (int ks = 0; ks < 4; ++ks)
#pragma unroll
        for (int j = 0; j < 4; ++j) GLD16S(zf[ks][j], zvoff[j], ZPTR(0, ks));
    __builtin_amdgcn_s_setprio(0);
#define ZWAIT(n, ks) asm volatile("s_waitcnt vmcnt(" #n ")" : "+v"(zf[ks][0]), "+v"(zf[ks][1]), "+v"(zf[ks][2]), "+v"(zf[ks][3]) :: "memory")
#define KSTEP(ks, MORE) do { _Pragma("unroll") for (int j_ = 0; j_ < 4; ++j_) { \
        acc[j_][0] = MFMA32(__builtin_bit_cast(bf16x8, zf[ks][j_]), wf[0][ks], acc[j_][0]); PIN(acc[j_][0]); SB(); \
        if (MORE && ks == 0) ISSUEW(c + 1, 2 * j_); \
        acc[j_][1] = MFMA32(__builtin_bit_cast(bf16x8, zf[ks][j_]), wf[1][ks], acc[j_][1]); PIN(acc[j_][1]); SB(); \
        if (MORE && ks == 0) ISSUEW(c + 1, 2 * j_ + 1); \
        if (MORE) GLD16S(zf[ks][j_], zvoff[j_], ZPTR(c + 1, ks)); } } while (0)
#define WREAD() _Pragma("unroll") for (int ks = 0; ks < 4; ++ks) { wf[0][ks] = ldsfrag(waddr[ks]); wf[1][ks] = ldsfrag(waddr[ks] + 4096); } \
        asm volatile("s_waitcnt lgkmcnt(0)" : "+v"(wf[0][0]), "+v"(wf[0][1]), "+v"(wf[0][2]), "+v"(wf[0][3]), "+v"(wf[1][0]), "+v"(wf[1][1]), "+v"(wf[1][2]), "+v"(wf[1][3]) :: "memory")
#pragma unroll 1
    for (int c = 0; c < 7; ++c) {
        bf16x8 wf[2][4];
        ZWAIT(12, 0);
        WREAD();
        KSTEP(0, true);
        ZWAIT(20, 1); KSTEP(1, true);
        ZWAIT(20, 2); KSTEP(2, true);
        ZWAIT(20, 3); KSTEP(3, true);
    }
    const int fs0 = ft * 8 + w * 2;
    const char* z1p = ws + WS_Z1 + ((size_t)tt * 2 * 56 + fs0) * 1024;
    const char* bp = (const char*)bsp + tt * 128;
    const unsigned l16 = lane * 16, bo = (lane & 31) * 2;
    unsigned braw[2];
    {
        const int c = 7; (void)c;
        bf16x8 wf[2][4];
        ZWAIT(12, 0);
        WREAD();
        KSTEP(0, false);
        GLD16(zf[0][0], l16, z1p, 0); GLD16(zf[0][1], l16, z1p, 1024); GLD16(zf[0][2], l16, z1p + 56 * 1024, 0); GLD16(zf[0][3], l16, z1p + 56 * 1024, 1024);
        asm volatile("global_load_ushort %0, %1, %2" : "=v"(braw[0]) : "v"(bo), "s"(bp) : "memory");
        asm volatile("global_load_ushort %0, %1, %2 offset:64" : "=v"(braw[1]) : "v"(bo), "s"(bp) : "memory");
        ZWAIT(14, 1); KSTEP(1, false);
        ZWAIT(10, 2); KSTEP(2, false);
        ZWAIT(6, 3); KSTEP(3, false);
    }
    STAMP(6);
    __builtin_amdgcn_s_setprio(2);
    lds_wait(lds0 + K2_MISC + SY_P24, 4 * 64);
    STAMP(7);
    auto blk = [&](int dstw, int j) { return lds0 + ((dstw & 1) ? K2_V + (dstw >> 1) * 32768 : K2_K + (dstw >> 1) * 31744) + (j - 1) * 8192 + lane * 16; };
#pragma unroll
    for (int j = 1; j < 4; ++j) {
        const unsigned a = blk(j ^ w, j);
#pragma unroll
        for (int t = 0; t < 2; ++t)
#pragma unroll
            for (int q = 0; q < 4; ++q) {
                f32x4 v = {acc[j][t][4 * q], acc[j][t][4 * q + 1], acc[j][t][4 * q + 2], acc[j][t][4 * q + 3]};
                *(LAS f32x4*)(uintptr_t)(a + (t * 4 + q) * 1024) = v;
            }
    }
    asm volatile("s_waitcnt lgkmcnt(0)" ::: "memory");
    lds_inc(lds0 + K2_MISC + SY_RED);
    lds_wait(lds0 + K2_MISC + SY_RED, 4 * 64);
    STAMP(8);
#pragma unroll
    for (int j = 1; j < 4; ++j) {
        const unsigned a = blk(w, j);
#pragma unroll
        for (int t = 0; t < 2; ++t)
#pragma unroll
            for (int q = 0; q < 4; ++q) {
                f32x4 v = *(LAS f32x4*)(uintptr_t)(a + (t * 4 + q) * 1024);
#pragma unroll
                for (int e = 0; e < 4; ++e) acc[0][t][4 * q + e] += v[e];
            }
    }
    asm volatile("s_waitcnt vmcnt(0)" : "+v"(zf[0][0]), "+v"(zf[0][1]), "+v"(zf[0][2]), "+v"(zf[0][3]), "+v"(braw[0]), "+v"(braw[1]) :: "memory");
    const float bias[2] = {bflo(braw[0]), bflo(braw[1])};
#pragma unroll
    for (int t = 0; t < 2; ++t)
#pragma unroll
        for (int s = 0; s < 2; ++s) {
            auto a0 = __builtin_amdgcn_permlane32_swap(zf[0][2 * t + s][0], zf[0][2 * t + s][2], false, false);
            auto a1 = __builtin_amdgcn_permlane32_swap(zf[0][2 * t + s][1], zf[0][2 * t + s][3], false, false);
            const unsigned zc[4] = {a0[0], a1[0], a0[1], a1[1]};
            unsigned p[4];
#pragma unroll
            for (int e = 0; e < 4; ++e)
                p[e] = pk2(bflo(zc[e]) * (acc[0][t][8 * s + 2 * e] + bias[t]), bfhi(zc[e]) * (acc[0][t][8 * s + 2 * e + 1] + bias[t]));
            auto s0 = __builtin_amdgcn_permlane32_swap(p[0], p[2], false, false);
            auto s1 = __builtin_amdgcn_permlane32_swap(p[1], p[3], false, false);
            u32x4 v = {s0[0], s1[0], s0[1], s1[1]};
            *(u32x4*)(ws + WS_CAT + ((size_t)(tt * 2 + t) * CAT_KS + 32 + fs0 + s) * 1024 + lane * 16) = v;
        }
    STAMP(9);
    STAMP_FLUSH(1, ws);
}

__global__ __launch_bounds__(512, 2) void k2_kernel(const char* __restrict__ wsp, const __hip_bfloat16* __restrict__ bsp, char* __restrict__ ws) {
    extern __shared__ __attribute__((aligned(16))) char smem[];
    const unsigned lds0 = (unsigned)(uintptr_t)(LAS char*)smem;
    const int wid = __builtin_amdgcn_readfirstlane(threadIdx.x >> 6);
    const int b = blockIdx.x;
    const int head = (b & 7) * 4 + (b >> 6);
    if (wid < 4) att_role(head, (b >> 3) & 7, wid, ws, lds0);
    else spg_role(b, head, wid - 4, wsp, bsp, ws, lds0);
}

constexpr int K3_SLOTS = 18, K3_LDS = 4 * K3_SLOTS * 2048;

__global__ __launch_bounds__(512, 2) void k3_kernel(const char* __restrict__ x, const char* __restrict__ wo, const char* __restrict__ wout,
                                                    const char* __restrict__ ws, char* __restrict__ out) {
    extern __shared__ __attribute__((aligned(16))) char smem[];
    const unsigned lds0 = (unsigned)(uintptr_t)(LAS char*)smem;
    const int tid = threadIdx.x, lane = tid & 63;
    const int wid = __builtin_amdgcn_readfirstlane(tid >> 6);
    const int wm = wid & 1, kq = wid >> 1;
    const int b = blockIdx.x, xc = b & 7, rr = b >> 3;
    const int tt = xc * 4 + (rr & 3), nt = rr >> 2;
    STAMP_DECL; STAMP(0); STAMPC(14);
    const unsigned bvoff = ((lane >> 2) & 7) * 1024 + (lane >> 5) * 64 + (lane & 3) * 16;
    const unsigned breg = lds0 + kq * (K3_SLOTS * 2048);
    auto issueB = [&](int s, int slot) {
        const int k = (kq * 22 + s) * 16 + wm * 8;
        const char* src = (k < 512 ? wo + (size_t)k * 1024 : wout + (size_t)(k - 512) * 1024) + nt * 128;
        glds16(src, bvoff, breg + slot * 2048 + wm * 1024);
    };
    const char* ab = ws + WS_CAT + ((size_t)(tt * 2 + wm) * CAT_KS + kq * 22) * 1024;
    const unsigned l16 = lane * 16;
    u32x4 af[22];
#pragma unroll
    for (int s = 0; s < 22; ++s) {
        asm volatile("global_load_dwordx4 %0, %1, %2" : "=v"(af[s]) : "v"(l16), "s"(ab + s * 1024) : "memory");
        if (s < K3_SLOTS) issueB(s, s);
    }
    STAMP(1);
    f32x16 acc[2] = {splat16(0.f), splat16(0.f)};
    const unsigned baddr = breg + (lane >> 5) * 1024 + ((lane & 15) >> 2) * 64 + ((lane >> 4) & 1) * 32 + (lane & 3) * 8;
#define K3STEP(s, slot) do { _Pragma("unroll") for (int n_ = 0; n_ < 2; ++n_) { \
        bf16x8 wf_ = trfrag(baddr + (slot) * 2048 + n_ * 512, baddr + (slot) * 2048 + n_ * 512 + 256); \
        acc[n_] = MFMA32(wf_, __builtin_bit_cast(bf16x8, af[s]), acc[n_]); } } while (0)
#define APIN6(i) "+v"(af[i]), "+v"(af[i + 1]), "+v"(af[i + 2]), "+v"(af[i + 3]), "+v"(af[i + 4]), "+v"(af[i + 5])
    asm volatile("s_waitcnt vmcnt(28)\n\ts_barrier" : APIN6(0) :: "memory");
    STAMP(2);
#pragma unroll
    for (int s = 0; s < 6; ++s) K3STEP(s, s);
    asm volatile("s_waitcnt vmcnt(16) lgkmcnt(0)\n\ts_barrier" : APIN6(6) :: "memory");
    STAMP(3);
#pragma unroll
    for (int s = 18; s < 22; ++s) issueB(s, s - 18);
#pragma unroll
    for (int s = 6; s < 12; ++s) K3STEP(s, s);
    asm volatile("s_waitcnt vmcnt(8)\n\ts_barrier" : APIN6(12) :: "memory");
    STAMP(4);
#pragma unroll
    for (int s = 12; s < 18; ++s) K3STEP(s, s);
    asm volatile("s_waitcnt vmcnt(0)\n\ts_barrier" : APIN6(16) :: "memory");
    STAMP(5);
#pragma unroll
    for (int s = 18; s < 22; ++s) K3STEP(s, s - 18);
    __syncthreads();
    STAMP(6);
#pragma unroll
    for (int n = 0; n < 2; ++n)
#pragma unroll
        for (int q = 0; q < 4; ++q) {
            f32x4 v = {acc[n][4 * q], acc[n][4 * q + 1], acc[n][4 * q + 2], acc[n][4 * q + 3]};
            *(LAS f32x4*)(uintptr_t)(lds0 + (((wm * 4 + kq) * 2 + n) * 4 + q) * 1024 + l16) = v;
        }
    __syncthreads();
    const int n = kq >> 1, q0 = (kq & 1) * 2;
    f32x4 r0 = {0, 0, 0, 0}, r1 = {0, 0, 0, 0};
#pragma unroll
    for (int s = 0; s < 4; ++s) {
        const unsigned a = lds0 + (((wm * 4 + s) * 2 + n) * 4 + q0 + (lane >> 5)) * 1024 + (lane & 31) * 16;
        r0 += *(LAS f32x4*)(uintptr_t)a;
        r1 += *(LAS f32x4*)(uintptr_t)(a + 512);
    }
    const size_t o = (size_t)(tt * 64 + wm * 32 + (lane & 31)) * 1024 + (nt * 64 + kq * 16 + (lane >> 5) * 8) * 2;
    const u32x4 xv = *(const u32x4*)(x + o);
    u32x4 v;
    v[0] = pk2(bflo(xv[0]) + r0[0], bfhi(xv[0]) + r0[1]); v[1] = pk2(bflo(xv[1]) + r0[2], bfhi(xv[1]) + r0[3]);
    v[2] = pk2(bflo(xv[2]) + r1[0], bfhi(xv[2]) + r1[1]); v[3] = pk2(bflo(xv[3]) + r1[2], bfhi(xv[3]) + r1[3]);
    *(u32x4*)(out + o) = v;
    STAMP(7); WAITV(0); STAMP(8);
    STAMP_FLUSH(2, ws);
}

extern "C" void kernel_launch(void* const* d_in, const int* in_sizes, int n_in, void* d_out, int out_size, void* d_ws, size_t ws_size,
                              hipStream_t stream) {
    const char* x = (const char*)d_in[0];
    const char* wqkv = (const char*)d_in[1];
    const char* wo = (const char*)d_in[2];
    const char* win = (const char*)d_in[4];
    const char* wsp = (const char*)d_in[5];
    const __hip_bfloat16* bsp = (const __hip_bfloat16*)d_in[6];
    const char* wout = (const char*)d_in[7];
    char* ws = (char*)d_ws;
    (void)hipFuncSetAttribute((const void*)k1_kernel, hipFuncAttributeMaxDynamicSharedMemorySize, K1_LDS);
    (void)hipFuncSetAttribute((const void*)k2_kernel, hipFuncAttributeMaxDynamicSharedMemorySize, K2_LDS);
    (void)hipFuncSetAttribute((const void*)k3_kernel, hipFuncAttributeMaxDynamicSharedMemorySize, K3_LDS);
    hipLaunchKernelGGL(k1_kernel, dim3(256), dim3(512), K1_LDS, stream, x, wqkv, win, ws);
    hipLaunchKernelGGL(k2_kernel, dim3(256), dim3(512), K2_LDS, stream, wsp, bsp, ws);
    hipLaunchKernelGGL(k3_kernel, dim3(256), dim3(512), K3_LDS, stream, x, wo, wout, (const char*)ws, (char*)d_out);
}
```

```cpp
#include <hip/hip_runtime.h>
#include <hip/hip_bf16.h>
#include <stdint.h>

typedef __attribute__((ext_vector_type(2))) __bf16 bf16x2;
typedef __attribute__((ext_vector_type(8))) __bf16 bf16x8;
typedef __attribute__((ext_vector_type(16))) __bf16 bf16x16;
typedef __attribute__((ext_vector_type(2))) float f32x2;
typedef __attribute__((ext_vector_type(4))) float f32x4;
typedef __attribute__((ext_vector_type(8))) float f32x8;
typedef __attribute__((ext_vector_type(16))) float f32x16;
typedef __attribute__((ext_vector_type(2))) unsigned u32x2;
typedef __attribute__((ext_vector_type(4))) unsigned u32x4;
typedef __attribute__((ext_vector_type(4))) short s16x4;
#define LAS __attribute__((address_space(3)))
#define DI __device__ __forceinline__
#define SB() __builtin_amdgcn_sched_barrier(0)
#define PIN(x) asm volatile("" : "+v"(x))
#define WAITV(n) asm volatile("s_waitcnt vmcnt(" #n ")" ::: "memory")
#define MFMA16(a, b, c) __builtin_amdgcn_mfma_f32_16x16x32_bf16(a, b, c, 0, 0, 0)
#define MFMA32(a, b, c) __builtin_amdgcn_mfma_f32_32x32x16_bf16(a, b, c, 0, 0, 0)

constexpr int S_ = 2048, D_ = 512, FH = 896;
constexpr size_t WS_Q = 0, WS_K = 2u << 20, WS_V = 4u << 20, WS_Z1 = 6u << 20, WS_Z2 = 10u << 20, WS_Z2N = 14u << 20,
                 WS_CAT = 18u << 20, WS_FLAG = 24u << 20;
constexpr int FLAG_REP = 32, FLAG_STRIDE = 4352;
constexpr int CAT_KS = 88;

#define STAMP_DECL
#define STAMP(i)
#define STAMPC(i)
#define STAMP_FLUSH(kern, wsp_)

#define STFLAG "sc1"
#define GST16(voff, val, sbase) asm volatile("global_store_dwordx4 %0, %1, %2 " STFLAG "\n\ts_nop 1" :: "v"(voff), "v"(val), "s"(sbase) : "memory")

DI void glds16(const void* sbase, unsigned voff, unsigned ldsdst) {
    unsigned keep;
    asm volatile("s_mov_b32 %0, m0\n\ts_mov_b32 m0, %3\n\ts_nop 0\n\tglobal_load_lds_dwordx4 %1, %2\n\ts_mov_b32 m0, %0"
                 : "=&s"(keep) : "v"(voff), "s"(sbase), "s"(ldsdst) : "memory");
}
DI unsigned pk2(float a, float b) { f32x2 v = {a, b}; return __builtin_bit_cast(unsigned, __builtin_convertvector(v, bf16x2)); }
DI float bflo(unsigned u) { return __uint_as_float(u << 16); }
DI float bfhi(unsigned u) { return __uint_as_float(u & 0xffff0000u); }
DI s16x4 trread(unsigned addr) { return __builtin_amdgcn_ds_read_tr16_b64_v4i16((LAS s16x4*)(uintptr_t)addr); }
DI bf16x8 trfrag(unsigned addr0, unsigned addr1) {
    s16x4 lo = trread(addr0), hi = trread(addr1);
    return __builtin_bit_cast(bf16x8, __builtin_shufflevector(lo, hi, 0, 1, 2, 3, 4, 5, 6, 7));
}
DI bf16x8 ldsfrag(unsigned addr) { return *(const LAS bf16x8*)(uintptr_t)addr; }
DI float gelu_t(float u) {
    const float k1 = -2.3022081985f;
    float w = u * __builtin_fmaf(u * u, k1 * 0.044715f, k1);
    float e = __builtin_amdgcn_exp2f(w);
    return u * __builtin_amdgcn_rcpf(1.0f + e);
}

constexpr int K1_MISC = 9 * 16384;
constexpr int K1_SUM = K1_MISC;
constexpr int K1_CS = K1_MISC + 1024;
constexpr int K1_PRIV = K1_MISC + 1024 + 2048;
constexpr int K1_LDS = K1_PRIV + 8 * 256;
enum { EPI_Q = 0, EPI_K = 1, EPI_V = 2, EPI_Z1 = 3, EPI_Z2 = 4 };
DI f32x16 splat16(float v) { f32x16 r; for (int i = 0; i < 16; ++i) r[i] = v; return r; }

DI float dot2m(unsigned a, unsigned m1, unsigned m2, float c) {
    const unsigned b = (a & m1) | m2;
    return __builtin_amdgcn_fdot2_f32_bf16(__builtin_bit_cast(bf16x2, a), __builtin_bit_cast(bf16x2, b), c, false);
}

template <bool ORI2, bool GELU>
DI void k1_body(const char* __restrict__ x, const char* __restrict__ wb  , int rp, char* __restrict__ dst  ,
                unsigned dvoff, unsigned jstride, unsigned pstride, float oscale, char* __restrict__ ws, unsigned lds0) {
    constexpr int LDW = GELU ? 3584 : 3072, NP = GELU ? 3 : 4;
    const int tid = threadIdx.x, lane = tid & 63;
    const int wid = __builtin_amdgcn_readfirstlane(tid >> 6);
    const int wm = wid & 3, wn = wid >> 2;
    const int r = lane & 31, h = lane >> 5;
    STAMP_DECL; STAMP(0); STAMPC(14);
    const char* xa = x + (size_t)rp * 131072 + wid * 16384;
    unsigned xvoff[2];
#pragma unroll
    for (int i = 0; i < 2; ++i) xvoff[i] = (i * 8 + (lane >> 3)) * 1024 + (((lane & 7) ^ ((4 * i + (lane >> 4)) & 7)) << 4);
    const char* wa = wb + (size_t)wid * 16 * LDW;
    const unsigned wvoff = ((lane >> 2) & 7) * LDW + (lane >> 5) * 64 + (lane & 3) * 16;
    const unsigned sdst = lds0 + wid * 2048;
#define ISSX(c, slot) do { glds16(xa + (c) * 128, xvoff[0], sdst + (slot) * 16384); glds16(xa + (c) * 128, xvoff[1], sdst + (slot) * 16384 + 1024); } while (0)
#define ISSW1(wp, st, i, sl) glds16((wp) + (size_t)((st) * 128 + (i) * 8) * LDW, wvoff, (sl) + (i) * 1024)
#define ISSW(wp, st, slot) do { ISSW1(wp, st, 0, sdst + (slot) * 16384); ISSW1(wp, st, 1, sdst + (slot) * 16384); } while (0)
    ISSX(0, 0); ISSX(1, 1); ISSW(wa, 0, 2);
    ISSX(2, 4); ISSX(3, 5); ISSW(wa, 1, 6);
    ISSX(4, 7); ISSX(5, 8); ISSW(wa, 2, 3);
    STAMP(1);
    unsigned xad[4], bad[4];
#pragma unroll
    for (int t = 0; t < 4; ++t) {
        xad[t] = lds0 + (wm * 32 + r) * 128 + (((2 * (t ^ wm) + h) ^ ((r >> 1) & 7)) << 4);
        bad[t] = lds0 + (2 * (t ^ wm) + h) * 1024 + wn * 512 + ((lane & 15) >> 2) * 64 + ((lane >> 4) & 1) * 32 + (lane & 3) * 8;
    }
    const unsigned selw = ((lane & 15) == ((lane >> 4) & 1)) ? 0x3f803f80u : 0u;
    u32x4 selu = {selw, selw, selw, selw};
    const bf16x8 sel = __builtin_bit_cast(bf16x8, selu);
    const unsigned m1 = wn ? 0u : 0xffffffffu, m2 = wn ? 0x3f803f80u : 0u;
    bf16x8 af[32];
    f32x16 acc[2];
    f32x4 csum = {0, 0, 0, 0};
    float st0 = 0.f, st1 = 0.f;
#define MM(bf, a, c) (ORI2 ? MFMA32(a, bf, c) : MFMA32(bf, a, c))
#define BFR(i, off) trfrag(bad[(i) & 3] + (off) + (((i) >> 2) & 1) * 8192, bad[(i) & 3] + (off) + (((i) >> 2) & 1) * 8192 + 256)
#define DOTS(i0) do { _Pragma("unroll") for (int i = i0; i < i0 + 2; ++i) { const u32x4 u = __builtin_bit_cast(u32x4, af[i]); \
            st0 = dot2m(u[0], m1, m2, st0); st1 = dot2m(u[1], m1, m2, st1); st0 = dot2m(u[2], m1, m2, st0); st1 = dot2m(u[3], m1, m2, st1); } \
        PIN(st0); PIN(st1); SB(); } while (0)
#define STAGE0(st, sx0, sx1, sb, G0, G1, G2) do { \
        bf16x8 bf[8]; \
        _Pragma("unroll") for (int i = 0; i < 8; ++i) { af[st * 8 + i] = ldsfrag(xad[i & 3] + (i < 4 ? sx0 : sx1) * 16384); bf[i] = BFR(i, sb * 16384); } \
        SB(); G0; SB(); \
        _Pragma("unroll") for (int i = 0; i < 8; ++i) { \
            if (st == 0 && i < 2) acc[i & 1] = MM(bf[i], af[i], splat16(0.f)); else acc[i & 1] = MM(bf[i], af[st * 8 + i], acc[i & 1]); \
            if ((i & 3) == 0) csum = MFMA16(sel, bf[i], csum); \
            if (i == 4) { PIN(acc[0]); PIN(acc[1]); SB(); G1; SB(); } } \
        PIN(acc[0]); PIN(acc[1]); PIN(csum); SB(); \
        DOTS(st * 8); DOTS(st * 8 + 2); G2; SB(); DOTS(st * 8 + 4); DOTS(st * 8 + 6); \
    } while (0)
#define BAR(n) asm volatile("s_waitcnt vmcnt(" #n ") lgkmcnt(0)\n\ts_barrier" ::: "memory")
    const char* w1 = wa + 128;
    BAR(12); STAMP(2);
    STAGE0(0, 0, 1, 2, {}, {}, {});
    BAR(6); STAMP(3);
    STAGE0(1, 4, 5, 6, ISSX(6, 0), ISSX(7, 1), ISSW(wa, 3, 2));
    BAR(6); STAMP(4);
    STAGE0(2, 7, 8, 3, ISSW(w1, 0, 4), ISSW(w1, 1, 5), ISSW(w1, 2, 6));
    BAR(6); STAMP(5);
    STAGE0(3, 0, 1, 2, ISSW(w1, 3, 7), {}, {});
    const unsigned csw = lds0 + K1_CS + wn * 512 + (lane & 15) * 16 + wm * 4;
    const unsigned csr = lds0 + K1_CS + wn * 512 + r * 16;
    const unsigned priv = lds0 + K1_PRIV + wid * 256;
#define CSWRITE(par) do { if (lane < 16) { *(LAS float*)(uintptr_t)(csw + (par)) = csum[0]; *(LAS float*)(uintptr_t)(csw + (par) + 256) = csum[1]; } } while (0)
    {
        float t = st0 + st1;
        auto rr = __builtin_amdgcn_permlane32_swap(__float_as_uint(t), __float_as_uint(t), false, false);
        t = __uint_as_float(rr[0]) + __uint_as_float(rr[1]);
        *(LAS float*)(uintptr_t)(lds0 + K1_SUM + wn * 512 + (wm * 32 + r) * 4) = t;
        CSWRITE(0);
    }
    BAR(0); STAMP(6);
    float rs, nm;
    {
        const float ss = *(LAS float*)(uintptr_t)(lds0 + K1_SUM + (wm * 32 + r) * 4), sm = *(LAS float*)(uintptr_t)(lds0 + K1_SUM + 512 + (wm * 32 + r) * 4);
        const float mean = sm * (1.0f / 512.0f);
        const float var = fmaxf(ss * (1.0f / 512.0f) - mean * mean, 0.0f);
        rs = __builtin_amdgcn_rsqf(var + 1e-5f) * oscale;
        nm = -mean * rs;
        if (ORI2) *(LAS float*)(uintptr_t)(priv + h * 128 + r * 4) = h ? nm : rs;
    }
    f32x4 ea, eb; float cs, ylo; unsigned pk[4];
#define EPI_BEGIN(par) do { f32x4 c4 = *(LAS f32x4*)(uintptr_t)(csr + (par)); cs = (c4[0] + c4[1]) + (c4[2] + c4[3]); \
        if (!ORI2) *(LAS float*)(uintptr_t)(priv + r * 4) = cs; } while (0)
#define EPI_LOAD(q) do { ea = *(LAS f32x4*)(uintptr_t)(priv + (q) * 32 + h * 16); if (ORI2) eb = *(LAS f32x4*)(uintptr_t)(priv + 128 + (q) * 32 + h * 16); } while (0)
#define EPI_ELEM(PV, e) do { float y_ = ORI2 ? __builtin_fmaf(eb[(e) & 3], cs, PV[e] * ea[(e) & 3]) : __builtin_fmaf(nm, ea[(e) & 3], PV[e] * rs); \
        if (GELU) y_ = gelu_t(y_); if ((e) & 1) { pk[((e) >> 1) & 3] = pk2(ylo, y_); PIN(pk[((e) >> 1) & 3]); } else { ylo = y_; PIN(ylo); } } while (0)
#define EPI_STORE(j) do { auto s0_ = __builtin_amdgcn_permlane32_swap(pk[0], pk[2], false, false); auto s1_ = __builtin_amdgcn_permlane32_swap(pk[1], pk[3], false, false); \
        u32x4 v_ = {s0_[0], s1_[0], s0_[1], s1_[1]}; GST16(dvoff, v_, dst + (j) * jstride); } while (0)
#pragma unroll
    for (int t = 0; t < 4; ++t) bad[t] += 65536;
    unsigned par = 0, nslot = sdst;
    const char* wnx = wa + 256;
    constexpr int PD = 4;
#pragma unroll 1
    for (int p = 1; p < NP; ++p) {
        f32x16 pv = acc[0] + acc[1]; PIN(pv);
        const bool more = p + 1 < NP;
        bf16x8 bf[PD + 1];
#pragma unroll
        for (int i = 0; i < PD; ++i) bf[i] = BFR(i, 0);
        SB();
#pragma unroll
        for (int i = 0; i < 32; ++i) {
            if (i + PD < 32) bf[(i + PD) % (PD + 1)] = BFR(i + PD, ((i + PD) >> 3) * 16384);
            if (i < 2) acc[i & 1] = MM(bf[i], af[i], splat16(0.f)); else acc[i & 1] = MM(bf[i % (PD + 1)], af[i], acc[i & 1]);
            PIN(acc[i & 1]);
            if ((i & 3) == 0) { if (i == 0) { f32x4 z4 = {0, 0, 0, 0}; csum = MFMA16(sel, bf[0], z4); } else csum = MFMA16(sel, bf[i % (PD + 1)], csum); PIN(csum); }
            if (i < 16 && (i & 1) && more) ISSW1(wnx, i >> 2, (i >> 1) & 1, nslot + (i >> 2) * 16384);
            if (i == 0) EPI_BEGIN(par);
            if (i >= 3) {
                const int u = i - 3;
                if (u == 14 || u == 28) EPI_STORE(u / 14 - 1);
                if (u % 7 == 0 && u < 28) EPI_LOAD(u / 7);
                if (u % 7 >= 3) EPI_ELEM(pv, (u / 7) * 4 + (u % 7) - 3);
            }
            SB();
        }
        par ^= 1024;
        CSWRITE(par);
        BAR(2);
        if (p == 1) STAMP(7); else if (p == 2) STAMP(8); else STAMP(9);
#pragma unroll
        for (int t = 0; t < 4; ++t) bad[t] ^= 65536;
        nslot ^= 65536; wnx += 128; dst += pstride;
    }
    EPI_BEGIN(par);
    const f32x16 pl = acc[0] + acc[1];
#pragma unroll
    for (int q = 0; q < 4; ++q) {
        EPI_LOAD(q);
#pragma unroll
        for (int e = 0; e < 4; ++e) EPI_ELEM(pl, q * 4 + e);
        if (q & 1) EPI_STORE(q >> 1);
    }
    STAMP(12); WAITV(0); STAMP(13);
    STAMP_FLUSH(0, ws);
}

__global__ __launch_bounds__(512, 2) void k1_kernel(const char* __restrict__ x, const char* __restrict__ wqkv, const char* __restrict__ win, char* __restrict__ ws) {
    extern __shared__ __attribute__((aligned(16))) char smem[];
    const unsigned lds0 = (unsigned)(uintptr_t)(LAS char*)smem;
    const int b = blockIdx.x, xc = b & 7, rr = b >> 3;
    const int rp = (xc & 1) * 8 + (rr & 7), ct = (xc >> 1) * 4 + (rr >> 3);
    const int lane = threadIdx.x & 63, wid = __builtin_amdgcn_readfirstlane(threadIdx.x >> 6);
    const int rt = rp * 4 + (wid & 3), wn = wid >> 2;
    if (threadIdx.x < FLAG_REP) *(unsigned*)(ws + WS_FLAG + threadIdx.x * FLAG_STRIDE + b * 4) = 0u;
    if (ct < 4) {
        const int head0 = (ct & 1) * 16 + wn * 2;
        k1_body<false, false>(x, wqkv + ct * 512, rp, ws + (ct < 2 ? WS_Q : WS_K) + ((size_t)head0 * 64 + rt) * 1024, lane * 16, 65536, 4 * 65536,
                              ct < 2 ? 0.25f * 1.4426950408889634f : 1.0f, ws, lds0);
    } else if (ct < 6) {
        const int head0 = (ct & 1) * 16 + wn * 2;
        k1_body<true, false>(x, wqkv + ct * 512, rp, ws + WS_V + ((size_t)head0 * 64 + rt) * 1024, ((lane >> 4) & 1) * 65536 + (lane >> 5) * 256 + (lane & 15) * 16, 512, 4 * 65536,
                             1.0f, ws, lds0);
    } else if (ct < 11) {
        const int c0 = min((ct - 6) * 192, 704);
        k1_body<false, true>(x, win + c0 * 2, rp, ws + WS_Z1 + ((size_t)rt * 56 + (c0 >> 4) + wn * 2) * 1024, lane * 16, 1024, 4096, 1.0f, ws, lds0);
    } else {
        const int c0 = min((ct - 11) * 192, 704);
        k1_body<true, true>(x, win + (FH + c0) * 2, rp, ws + WS_Z2 + ((size_t)rt * 4 * FH + c0 + wn * 32) * 16, (lane >> 5) * (FH * 16) + (lane & 31) * 16, 2 * FH * 16, 1024,
                            1.0f, ws, lds0);
    }
}

constexpr int K2_K = 0, K2_V = 62 * 1024, K2_W = 126 * 1024, K2_ONES = K2_W + 32768, K2_MISC = K2_ONES + 1024, K2_LDS = K2_MISC + 1024;
enum { SY_KVA = 0, SY_KVB = 4, SY_KVC = 8, SY_P24 = 12, SY_LN = 16, SY_LNST = 20, SY_RED = 24, SY_FLAG = 28, SY_P30 = 32, SY_CMB = 36 };
constexpr int SPIN_MAX = 1 << 20;
constexpr unsigned FLAG_TAG = 0x5eed0001u;
constexpr float REF_OFF = 64.0f;

DI void lds_inc(unsigned addr) { __hip_atomic_fetch_add((LAS unsigned*)(uintptr_t)addr, 1u, __ATOMIC_RELAXED, __HIP_MEMORY_SCOPE_WORKGROUP); }
DI void lds_wait(unsigned addr, unsigned n) {
#pragma unroll 1
    for (int it = 0; it < SPIN_MAX && __builtin_amdgcn_readfirstlane(__hip_atomic_load((LAS unsigned*)(uintptr_t)addr, __ATOMIC_RELAXED, __HIP_MEMORY_SCOPE_WORKGROUP)) < n; ++it)
        __builtin_amdgcn_s_sleep(1);
    asm volatile("" ::: "memory");
}
DI bf16x16 lo8(const f32x16& x) { f32x8 t = __builtin_shufflevector(x, x, 0, 1, 2, 3, 4, 5, 6, 7); return __builtin_bit_cast(bf16x16, t); }
DI bf16x16 hi8(const f32x16& x) { f32x8 t = __builtin_shufflevector(x, x, 8, 9, 10, 11, 12, 13, 14, 15); return __builtin_bit_cast(bf16x16, t); }
#define SMF(a, b, c) __builtin_amdgcn_smfmac_f32_32x32x32_bf16(a, b, c, sidx, 0, 0)
#define GLD16(dst, voff, sbase, IMM) asm volatile("global_load_dwordx4 %0, %1, %2 offset:" #IMM : "=v"(dst) : "v"(voff), "s"(sbase) : "memory")
#define GLD16S(dst, voff, sbase) asm volatile("global_load_dwordx4 %0, %1, %2 sc1" : "=v"(dst) : "v"(voff), "s"(sbase) : "memory")

DI float max16(const f32x16& s) {
    float a = fmaxf(fmaxf(s[0], s[1]), s[2]), b = fmaxf(fmaxf(s[3], s[4]), s[5]);
    a = fmaxf(fmaxf(a, s[6]), s[7]); b = fmaxf(fmaxf(b, s[8]), s[9]);
    a = fmaxf(fmaxf(a, s[10]), s[11]); b = fmaxf(fmaxf(b, s[12]), s[13]);
    a = fmaxf(fmaxf(a, s[14]), s[15]);
    return fmaxf(a, b);
}
DI float xhalf_max(float m) {
    auto rr = __builtin_amdgcn_permlane32_swap(__float_as_uint(m), __float_as_uint(m), false, false);
    return fmaxf(__uint_as_float(rr[0]), __uint_as_float(rr[1]));
}

DI void att_exact(const char* kg, const char* vg, bf16x8 q, f32x16& o_out) {
    const int lane = threadIdx.x & 63;
    const int sidx = (int)0xDDDDDDDDu;
    f32x16 z = splat16(0.f);
    float m = -INFINITY;
    for (int t = 0; t < 64; ++t) {
        bf16x8 kf = *(const bf16x8*)(kg + t * 1024 + lane * 16);
        f32x16 s = MFMA32(kf, q, z);
        m = fmaxf(m, max16(s));
    }
    m = xhalf_max(m);
    f32x16 c = splat16(-m), o = z;
    const bool isv = (lane & 16) == 0;
    u32x4 onesu = {0x3f803f80u, 0x3f803f80u, 0x3f803f80u, 0x3f803f80u};
    const unsigned vo = (lane >> 5) * 256 + (lane & 15) * 16;
    for (int t = 0; t < 64; ++t) {
        bf16x8 kf = *(const bf16x8*)(kg + t * 1024 + lane * 16);
        u32x4 v0 = *(const u32x4*)(vg + t * 1024 + vo), v1 = *(const u32x4*)(vg + t * 1024 + 512 + vo);
        if (!isv) { v0 = onesu; v1 = onesu; }
        f32x16 s = MFMA32(kf, q, c);
#pragma unroll
        for (int i = 0; i < 16; ++i) s[i] = __builtin_amdgcn_exp2f(s[i]);
        o = SMF(__builtin_bit_cast(bf16x8, v0), lo8(s), o);
        o = SMF(__builtin_bit_cast(bf16x8, v1), hi8(s), o);
    }
    o_out = o;
}

DI void att_store(const f32x16& o, char* dst, int lane) {
    const float rl = __builtin_amdgcn_rcpf(o[8]);
    unsigned p0 = pk2(o[0] * rl, o[1] * rl), p1 = pk2(o[2] * rl, o[3] * rl), p2 = pk2(o[4] * rl, o[5] * rl), p3 = pk2(o[6] * rl, o[7] * rl);
    auto s0 = __builtin_amdgcn_permlane32_swap(p0, p2, false, false);
    auto s1 = __builtin_amdgcn_permlane32_swap(p1, p3, false, false);
    u32x4 v = {s0[0], s1[0], s0[1], s1[1]};
    *(u32x4*)(dst + lane * 16) = v;
}
DI bool att_bad(const f32x16& o) {
    float a = fmaxf(fmaxf(fabsf(o[0]), fabsf(o[1])), fmaxf(fabsf(o[2]), fabsf(o[3])));
    float b = fmaxf(fmaxf(fabsf(o[4]), fabsf(o[5])), fmaxf(fabsf(o[6]), fabsf(o[7])));
    float s = (o[0] + o[1] + o[2] + o[3]) + (o[4] + o[5] + o[6] + o[7]);
    bool ok = (o[8] < 0x1p120f) && (o[8] > 0x1p-80f) && (fmaxf(a, b) < 0x1p124f) && (s == s);
    return __any(!ok);
}

DI void att_role(int head, int qb, int w, char* __restrict__ ws, unsigned lds0) {
    const int lane = threadIdx.x & 63;
    const int qh = w & 1, kh = w >> 1;
    const char* kg = ws + WS_K + (size_t)head * 65536;
    const char* vg = ws + WS_V + (size_t)head * 65536;
    const char* qg = ws + WS_Q + (size_t)head * 65536 + (size_t)(qb * 8 + qh * 4) * 1024;
    const unsigned l16 = lane * 16;
    const unsigned vo = (lane >> 5) * 256 + (lane & 15) * 16;
    u32x4 qr[4], k0, k1;
    STAMP_DECL; STAMP(0); STAMPC(14);
    const unsigned qo = l16 + kh * 2048, qo2 = l16 + (kh ^ 1) * 2048;
    GLD16(k0, l16, kg + kh * 32768, 0); GLD16(k1, l16, kg + (kh ^ 1) * 32768, 0);
    GLD16(qr[0], qo, qg, 0); GLD16(qr[1], qo, qg, 1024); GLD16(qr[2], qo2, qg, 0); GLD16(qr[3], qo2, qg, 1024);
    const char* ksrc = (qh ? vg : kg + 1024) + kh * 32768;
    const unsigned kdst = lds0 + (qh ? K2_V + kh * 32768 : K2_K + kh * 31744);
#pragma unroll
    for (int i = 0; i < 5; ++i) glds16(ksrc + i * 1024, l16, kdst + i * 1024);
    *(LAS unsigned*)(uintptr_t)(lds0 + K2_ONES + threadIdx.x * 4) = ((threadIdx.x >> 2) & 11) == 0 ? 0x3f803f80u : 0u;
    asm volatile("s_waitcnt lgkmcnt(0)\n\ts_barrier" ::: "memory");
#pragma unroll
    for (int i = 5; i < 13; ++i) glds16(ksrc + i * 1024, l16, kdst + i * 1024);
    STAMP(1);
    asm volatile("s_waitcnt vmcnt(13)" : "+v"(qr[0]), "+v"(qr[1]), "+v"(qr[2]), "+v"(qr[3]), "+v"(k0), "+v"(k1) :: "memory");
    STAMP(2);
#define QF(i) __builtin_bit_cast(bf16x8, qr[i])
    int sidx = (int)0xDDDDDDDDu; PIN(sidx);
    f32x16 cc[4];
#pragma unroll
    for (int i = 0; i < 4; i += 2) {
        f32x16 z = splat16(0.f);
        f32x16 s0 = MFMA32(__builtin_bit_cast(bf16x8, k0), QF(i), z), s1 = MFMA32(__builtin_bit_cast(bf16x8, k1), QF(i), z);
        f32x16 s2 = MFMA32(__builtin_bit_cast(bf16x8, k0), QF(i + 1), z), s3 = MFMA32(__builtin_bit_cast(bf16x8, k1), QF(i + 1), z);
        PIN(s0); PIN(s1); PIN(s2); PIN(s3);
        float mA = xhalf_max(fmaxf(max16(s0), max16(s1))), mB = xhalf_max(fmaxf(max16(s2), max16(s3)));
        cc[i] = splat16(-(mA + REF_OFF)); cc[i + 1] = splat16(-(mB + REF_OFF));
        PIN(cc[i]); PIN(cc[i + 1]);
    }
    bf16x8 kf = __builtin_bit_cast(bf16x8, k0);
    f32x16 oo[4] = {splat16(0.f), splat16(0.f), splat16(0.f), splat16(0.f)}, sA, sB = splat16(0.f);
    sA = MFMA32(kf, QF(0), cc[0]);
    WAITV(8); lds_inc(lds0 + K2_MISC + SY_KVA);
    unsigned kaddr = lds0 + K2_K + kh * 31744 + l16;
    unsigned vaddr = (lane & 16) ? (lds0 + K2_ONES + vo) : (lds0 + K2_V + kh * 32768 + vo);
    const unsigned vinc = (lane & 16) ? 0u : 1024u;
    lds_wait(lds0 + K2_MISC + SY_KVA, 4 * 64);
    bf16x8 v0 = ldsfrag(vaddr), v1 = v0;
    STAMP(3);
#define EXPS(X, a, b) do { _Pragma("unroll") for (int i_ = a; i_ < b; ++i_) X[i_] = __builtin_amdgcn_exp2f(X[i_]); PIN(X); SB(); } while (0)
#define HSTEP(X, Y, i, A1, A2, A3) do { \
        EXPS(X, 0, 2); \
        oo[(i + 3) & 3] = SMF(v1, hi8(Y), oo[(i + 3) & 3]); PIN(oo[(i + 3) & 3]); SB(); A1; SB(); \
        EXPS(X, 2, 5); \
        Y = MFMA32(kf, QF((i + 1) & 3), cc[(i + 1) & 3]); PIN(Y); SB(); A2; SB(); \
        EXPS(X, 5, 11); \
        oo[i] = SMF(v0, lo8(X), oo[i]); PIN(oo[i]); SB(); A3; SB(); \
        EXPS(X, 11, 16); \
    } while (0)
#pragma unroll 1
    for (int seg = 0; seg < 5; ++seg) {
        int n = 4;
        if (seg == 1) { n = 8; STAMP(4); WAITV(0); lds_inc(lds0 + K2_MISC + SY_KVB); lds_wait(lds0 + K2_MISC + SY_KVB, 4 * 64); STAMP(5); STAMPC(10); }
        else if (seg == 2) { n = 13; STAMP(6); STAMPC(11); lds_wait(lds0 + K2_MISC + SY_KVC, 4 * 64); STAMP(7); STAMPC(12); }
        else if (seg == 3) { n = 6; asm volatile("s_waitcnt lgkmcnt(0)" ::: "memory"); lds_inc(lds0 + K2_MISC + SY_P24); }
        else if (seg == 4) { n = 1; asm volatile("s_waitcnt lgkmcnt(0)" ::: "memory"); lds_inc(lds0 + K2_MISC + SY_P30); }
#pragma unroll 1
        for (int i = 0; i < n; ++i) {
            HSTEP(sA, sB, 0, { v1 = ldsfrag(vaddr + 512); vaddr += vinc; }, {}, {});
            HSTEP(sB, sA, 1, {}, {}, {});
            HSTEP(sA, sB, 2, {}, { kf = ldsfrag(kaddr); kaddr += 1024; }, {});
            HSTEP(sB, sA, 3, {}, {}, { v0 = ldsfrag(vaddr); });
        }
    }
    oo[3] = SMF(v1, hi8(sB), oo[3]);
    STAMP(8); STAMPC(13);
    {
        auto cbase = [&](int ww) { return lds0 + ((ww & 1) ? K2_V + (ww >> 1) * 32768 + 25 * 1024 : K2_K + (ww >> 1) * 31744 + 24 * 1024); };
        lds_wait(lds0 + K2_MISC + SY_P30, 4 * 64);
        const unsigned wa = cbase(w), ra = cbase(w ^ 2);
#pragma unroll
        for (int i = 0; i < 2; ++i) {
            f32x4 a = {oo[2 + i][0], oo[2 + i][1], oo[2 + i][2], oo[2 + i][3]}, b = {oo[2 + i][4], oo[2 + i][5], oo[2 + i][6], oo[2 + i][7]};
            *(LAS f32x4*)(uintptr_t)(wa + i * 2048 + l16) = a;
            *(LAS f32x4*)(uintptr_t)(wa + i * 2048 + 1024 + l16) = b;
            *(LAS float*)(uintptr_t)(wa + 4096 + i * 256 + lane * 4) = oo[2 + i][8];
        }
        asm volatile("s_waitcnt lgkmcnt(0)" ::: "memory");
        lds_inc(lds0 + K2_MISC + SY_CMB + qh * 4);
        lds_wait(lds0 + K2_MISC + SY_CMB + qh * 4, 2 * 64);
#pragma unroll
        for (int i = 0; i < 2; ++i) {
            f32x4 a = *(LAS f32x4*)(uintptr_t)(ra + i * 2048 + l16), b = *(LAS f32x4*)(uintptr_t)(ra + i * 2048 + 1024 + l16);
            float l = *(LAS float*)(uintptr_t)(ra + 4096 + i * 256 + lane * 4);
#pragma unroll
            for (int e = 0; e < 4; ++e) { oo[i][e] += a[e]; oo[i][4 + e] += b[e]; }
            oo[i][8] += l;
        }
    }
    if (__builtin_expect(att_bad(oo[0]), 0)) att_exact(kg, vg, QF(0), oo[0]);
    if (__builtin_expect(att_bad(oo[1]), 0)) att_exact(kg, vg, QF(1), oo[1]);
    char* dst = ws + WS_CAT + ((size_t)(qb * 8 + qh * 4 + kh * 2) * CAT_KS + head) * 1024;
    att_store(oo[0], dst, lane);
    att_store(oo[1], dst + CAT_KS * 1024, lane);
    STAMP(9);
    STAMP_FLUSH(1, ws);
}

template <int CTRL> DI float dppf(float x) { return __builtin_bit_cast(float, __builtin_amdgcn_mov_dpp(__builtin_bit_cast(int, x), CTRL, 0xf, 0xf, true)); }
DI float fold32(float a, float b) {
    auto r = __builtin_amdgcn_permlane32_swap(__float_as_uint(a), __float_as_uint(b), false, false);
    return __uint_as_float(r[0]) + __uint_as_float(r[1]);
}
DI float fold16(float a, float b) {
    auto r = __builtin_amdgcn_permlane16_swap(__float_as_uint(a), __float_as_uint(b), false, false);
    return __uint_as_float(r[0]) + __uint_as_float(r[1]);
}
DI float rowsum16(float v) { v += dppf<0xB1>(v); v += dppf<0x4E>(v); v += dppf<0x124>(v); v += dppf<0x128>(v); return v; }

DI void spg_role(int b, int head, int w, const char* __restrict__ wsp, const __hip_bfloat16* __restrict__ bsp, char* __restrict__ ws, unsigned lds0,
                 const char* pf_x, const char* pf_wo, const char* pf_wout) {
    const int lane = threadIdx.x & 63;
    const int xc = b & 7, rr = b >> 3;
    const int tt = xc * 4 + (rr & 3), ft = rr >> 2;
    const bool active = ft < 7;
    STAMP_DECL; STAMP(0); STAMPC(14);
    __builtin_amdgcn_s_setprio(2);
    const unsigned wring = lds0 + K2_W + w * 8192;
    const char* wbase = wsp + (size_t)tt * 64 * 4096 + w * 1024;
    unsigned wvoff[2];
#pragma unroll
    for (int p = 0; p < 2; ++p) wvoff[p] = (lane >> 3) * 4096 + (((lane & 7) ^ ((4 * p + (lane >> 4)) & 7)) << 4);
    const int rot = 0;
#define CROT(c) (((c) + rot) & 7)
#define ISSUEW(c, i) glds16(wbase + (size_t)(i) * 32768 + CROT(c) * 128, wvoff[(i) & 1], wring + (i) * 1024)
    {
        const char* src = ws + WS_Z2 + (size_t)b * FH * 16;
        char* dst = ws + WS_Z2N + (size_t)b * FH * 16;
        u32x4 zc[4], z0;
        const unsigned o0 = (w * 224 + lane) * 16;
        const unsigned o3 = (w * 224 + 192 + (lane & 31)) * 16;
        const unsigned zero = 0;
        GLD16(zc[0], o0, src, 0); GLD16(zc[1], o0, src, 1024); GLD16(zc[2], o0, src, 2048); GLD16(zc[3], o3, src, 0); GLD16(z0, zero, src, 0);
        *(LAS unsigned*)(uintptr_t)(lds0 + K2_MISC + (threadIdx.x - 256) * 4) = 0u;
        asm volatile("s_waitcnt lgkmcnt(0)\n\ts_barrier" ::: "memory");
        asm volatile("s_waitcnt vmcnt(0)" : "+v"(zc[0]), "+v"(zc[1]), "+v"(zc[2]), "+v"(zc[3]), "+v"(z0) :: "memory");
        STAMP(1);
        float sh[8], s1[8], s2[8];
#pragma unroll
        for (int e = 0; e < 4; ++e) { sh[2 * e] = bflo(z0[e]); sh[2 * e + 1] = bfhi(z0[e]); }
        const float m3 = lane < 32 ? 1.f : 0.f;
#pragma unroll
        for (int e = 0; e < 8; ++e) {
            float a = ((e & 1) ? bfhi(zc[0][e >> 1]) : bflo(zc[0][e >> 1])) - sh[e];
            float bb = ((e & 1) ? bfhi(zc[1][e >> 1]) : bflo(zc[1][e >> 1])) - sh[e];
            float c = ((e & 1) ? bfhi(zc[2][e >> 1]) : bflo(zc[2][e >> 1])) - sh[e];
            float d = (((e & 1) ? bfhi(zc[3][e >> 1]) : bflo(zc[3][e >> 1])) - sh[e]) * m3;
            s1[e] = (a + bb) + (c + d);
            s2[e] = (a * a + bb * bb) + (c * c + d * d);
        }
        f32x4 q;
#pragma unroll
        for (int i = 0; i < 4; ++i) q[i] = rowsum16(fold16(fold32(s1[i], s2[i]), fold32(s1[i + 4], s2[i + 4])));
        LAS float* part = (LAS float*)(uintptr_t)(lds0 + K2_MISC + 256);
        *(LAS f32x4*)(part + w * 16 + (lane >> 4) * 4) = q;
        asm volatile("s_waitcnt lgkmcnt(0)" ::: "memory");
        lds_inc(lds0 + K2_MISC + SY_LN);
        lds_wait(lds0 + K2_MISC + SY_LN, 4 * 64);
        STAMP(2);
        float rs[8], nm[8];
        {
            f32x4 t[4];
#pragma unroll
            for (int i = 0; i < 4; ++i) t[i] = (*(LAS f32x4*)(part + i * 4) + *(LAS f32x4*)(part + 16 + i * 4)) + (*(LAS f32x4*)(part + 32 + i * 4) + *(LAS f32x4*)(part + 48 + i * 4));
#pragma unroll
            for (int e = 0; e < 8; ++e) {
                float mean = t[e >> 2][e & 3] * (1.0f / FH);
                float var = fmaxf(t[2 + (e >> 2)][e & 3] * (1.0f / FH) - mean * mean, 0.f);
                rs[e] = __builtin_amdgcn_rsqf(var + 1e-5f);
                nm[e] = -(mean + sh[e]) * rs[e];
            }
        }
#pragma unroll
        for (int j = 0; j < 4; ++j) {
            u32x4 v;
#pragma unroll
            for (int e = 0; e < 4; ++e) v[e] = pk2(__builtin_fmaf(bflo(zc[j][e]), rs[2 * e], nm[2 * e]), __builtin_fmaf(bfhi(zc[j][e]), rs[2 * e + 1], nm[2 * e + 1]));
            const unsigned o = j < 3 ? o0 + j * 1024 : o3;
            asm volatile("global_store_dwordx4 %0, %1, %2 sc1\n\ts_nop 1" :: "v"(o), "v"(v), "s"(dst) : "memory");
        }
        STAMP(3);
        WAITV(0);
        STAMP(4);
        unsigned old = 0;
        if (lane == 0) old = __hip_atomic_fetch_add((LAS unsigned*)(uintptr_t)(lds0 + K2_MISC + SY_LNST), 1u, __ATOMIC_RELAXED, __HIP_MEMORY_SCOPE_WORKGROUP);
        old = __builtin_amdgcn_readfirstlane(old);
        if (old == 3 && lane < FLAG_REP) __hip_atomic_store((unsigned*)(ws + WS_FLAG + lane * FLAG_STRIDE) + b, FLAG_TAG, __ATOMIC_RELAXED, __HIP_MEMORY_SCOPE_AGENT);
    }
    {
        const char* src = ws + ((w & 1) ? WS_V : WS_K) + (size_t)head * 65536 + (w >> 1) * 32768;
        const unsigned dst = lds0 + ((w & 1) ? K2_V + (w >> 1) * 32768 : K2_K + (w >> 1) * 31744 - 1024);
#pragma unroll 1
        for (int t = 13; t < 32; ++t) glds16(src + t * 1024, lane * 16, dst + t * 1024);
#pragma unroll
        for (int i = 0; i < 8; ++i) ISSUEW(0, i);
        WAITV(8);
        lds_inc(lds0 + K2_MISC + SY_KVC);
    }
    if (!active) {
        lds_wait(lds0 + K2_MISC + SY_P24, 4 * 64);
        const int id = (rr & 3) * 4 + w;
        const unsigned lo = lane * 16;
        u32x4 t;
#pragma unroll 4
        for (int i = 0; i < 32; ++i) GLD16(t, lo, pf_wo + (size_t)(id * 32 + i) * 1024, 0);
#pragma unroll 4
        for (int i = 0; i < 56; ++i) { GLD16(t, lo, pf_wout + (size_t)(id * 56 + i) * 1024, 0); if ((i & 3) == 3) WAITV(40); }
#pragma unroll 4
        for (int i = 0; i < 16; ++i) { GLD16(t, lo, pf_x + (size_t)(xc * 256 + id * 16 + i) * 1024, 0); if ((i & 3) == 3) WAITV(40); }
        WAITV(0);
        STAMP_FLUSH(1, ws); return;
    }
    const char* zb = ws + WS_Z2N + ((size_t)w * 64 * FH + ft * 128) * 16;
    unsigned zvoff[4];
#pragma unroll
    for (int j = 0; j < 4; ++j) { zvoff[j] = (lane >> 5) * (FH * 16) + (j ^ w) * 512 + (lane & 31) * 16; PIN(zvoff[j]); }
    u32x4 zf[4][4];
#define ZPTR(c, ks) (zb + (size_t)(CROT(c) * 4 + (ks)) * (2 * FH * 16))
    f32x16 acc[4][2];
#pragma unroll
    for (int j = 0; j < 4; ++j) { acc[j][0] = splat16(0.f); acc[j][1] = splat16(0.f); PIN(acc[j][0]); PIN(acc[j][1]); }
    unsigned waddr[4];
#pragma unroll
    for (int ks = 0; ks < 4; ++ks) { waddr[ks] = wring + (lane & 31) * 128 + (((ks * 2 + (lane >> 5)) ^ ((lane >> 1) & 7)) << 4); PIN(waddr[ks]); }
    if (w == 0) {
        const char* fl = ws + WS_FLAG + (size_t)(b >> 3) * FLAG_STRIDE;
        const unsigned fo = lane * 16;
#pragma unroll 1
        for (int it = 0; it < SPIN_MAX; ++it) {
            u32x4 f;
            asm volatile("global_load_dwordx4 %0, %1, %2 sc1\n\ts_waitcnt vmcnt(0)" : "=&v"(f) : "v"(fo), "s"(fl) : "memory");
            if (__all(f[0] == FLAG_TAG && f[1] == FLAG_TAG && f[2] == FLAG_TAG && f[3] == FLAG_TAG)) break;
            __builtin_amdgcn_s_sleep(4);
        }
        lds_inc(lds0 + K2_MISC + SY_FLAG);
    } else lds_wait(lds0 + K2_MISC + SY_FLAG, 64);
    STAMP(5);
#pragma unroll
    for (int ks = 0; ks < 4; ++ks)
#pragma unroll
        for (int j = 0; j < 4; ++j) GLD16S(zf[ks][j], zvoff[j], ZPTR(0, ks));
    __builtin_amdgcn_s_setprio(0);
#define ZWAIT(n, ks) asm volatile("s_waitcnt vmcnt(" #n ")" : "+v"(zf[ks][0]), "+v"(zf[ks][1]), "+v"(zf[ks][2]), "+v"(zf[ks][3]) :: "memory")
#define KSTEP(ks, MORE) do { _Pragma("unroll") for (int j_ = 0; j_ < 4; ++j_) { \
        acc[j_][0] = MFMA32(__builtin_bit_cast(bf16x8, zf[ks][j_]), wf[0][ks], acc[j_][0]); PIN(acc[j_][0]); SB(); \
        if (MORE && ks == 0) ISSUEW(c + 1, 2 * j_); \
        acc[j_][1] = MFMA32(__builtin_bit_cast(bf16x8, zf[ks][j_]), wf[1][ks], acc[j_][1]); PIN(acc[j_][1]); SB(); \
        if (MORE && ks == 0) ISSUEW(c + 1, 2 * j_ + 1); \
        if (MORE) GLD16S(zf[ks][j_], zvoff[j_], ZPTR(c + 1, ks)); } } while (0)
#define WREAD() _Pragma("unroll") for (int ks = 0; ks < 4; ++ks) { wf[0][ks] = ldsfrag(waddr[ks]); wf[1][ks] = ldsfrag(waddr[ks] + 4096); } \
        asm volatile("s_waitcnt lgkmcnt(0)" : "+v"(wf[0][0]), "+v"(wf[0][1]), "+v"(wf[0][2]), "+v"(wf[0][3]), "+v"(wf[1][0]), "+v"(wf[1][1]), "+v"(wf[1][2]), "+v"(wf[1][3]) :: "memory")
#pragma unroll 1
    for (int c = 0; c < 7; ++c) {
        bf16x8 wf[2][4];
        ZWAIT(12, 0);
        WREAD();
        KSTEP(0, true);
        ZWAIT(20, 1); KSTEP(1, true);
        ZWAIT(20, 2); KSTEP(2, true);
        ZWAIT(20, 3); KSTEP(3, true);
    }
    const int fs0 = ft * 8 + w * 2;
    const char* z1p = ws + WS_Z1 + ((size_t)tt * 2 * 56 + fs0) * 1024;
    const char* bp = (const char*)bsp + tt * 128;
    const unsigned l16 = lane * 16, bo = (lane & 31) * 2;
    unsigned braw[2];
    {
        const int c = 7; (void)c;
        bf16x8 wf[2][4];
        ZWAIT(12, 0);
        WREAD();
        KSTEP(0, false);
        GLD16(zf[0][0], l16, z1p, 0); GLD16(zf[0][1], l16, z1p, 1024); GLD16(zf[0][2], l16, z1p + 56 * 1024, 0); GLD16(zf[0][3], l16, z1p + 56 * 1024, 1024);
        asm volatile("global_load_ushort %0, %1, %2" : "=v"(braw[0]) : "v"(bo), "s"(bp) : "memory");
        asm volatile("global_load_ushort %0, %1, %2 offset:64" : "=v"(braw[1]) : "v"(bo), "s"(bp) : "memory");
        ZWAIT(14, 1); KSTEP(1, false);
        ZWAIT(10, 2); KSTEP(2, false);
        ZWAIT(6, 3); KSTEP(3, false);
    }
    STAMP(6);
    __builtin_amdgcn_s_setprio(2);
    lds_wait(lds0 + K2_MISC + SY_P24, 4 * 64);
    STAMP(7);
    auto blk = [&](int dstw, int j) { return lds0 + ((dstw & 1) ? K2_V + (dstw >> 1) * 32768 : K2_K + (dstw >> 1) * 31744) + (j - 1) * 8192 + lane * 16; };
#pragma unroll
    for (int j = 1; j < 4; ++j) {
        const unsigned a = blk(j ^ w, j);
#pragma unroll
        for (int t = 0; t < 2; ++t)
#pragma unroll
            for (int q = 0; q < 4; ++q) {
                f32x4 v = {acc[j][t][4 * q], acc[j][t][4 * q + 1], acc[j][t][4 * q + 2], acc[j][t][4 * q + 3]};
                *(LAS f32x4*)(uintptr_t)(a + (t * 4 + q) * 1024) = v;
            }
    }
    asm volatile("s_waitcnt lgkmcnt(0)" ::: "memory");
    lds_inc(lds0 + K2_MISC + SY_RED);
    lds_wait(lds0 + K2_MISC + SY_RED, 4 * 64);
    STAMP(8);
#pragma unroll
    for (int j = 1; j < 4; ++j) {
        const unsigned a = blk(w, j);
#pragma unroll
        for (int t = 0; t < 2; ++t)
#pragma unroll
            for (int q = 0; q < 4; ++q) {
                f32x4 v = *(LAS f32x4*)(uintptr_t)(a + (t * 4 + q) * 1024);
#pragma unroll
                for (int e = 0; e < 4; ++e) acc[0][t][4 * q + e] += v[e];
            }
    }
    asm volatile("s_waitcnt vmcnt(0)" : "+v"(zf[0][0]), "+v"(zf[0][1]), "+v"(zf[0][2]), "+v"(zf[0][3]), "+v"(braw[0]), "+v"(braw[1]) :: "memory");
    const float bias[2] = {bflo(braw[0]), bflo(braw[1])};
#pragma unroll
    for (int t = 0; t < 2; ++t)
#pragma unroll
        for (int s = 0; s < 2; ++s) {
            auto a0 = __builtin_amdgcn_permlane32_swap(zf[0][2 * t + s][0], zf[0][2 * t + s][2], false, false);
            auto a1 = __builtin_amdgcn_permlane32_swap(zf[0][2 * t + s][1], zf[0][2 * t + s][3], false, false);
            const unsigned zc[4] = {a0[0], a1[0], a0[1], a1[1]};
            unsigned p[4];
#pragma unroll
            for (int e = 0; e < 4; ++e)
                p[e] = pk2(bflo(zc[e]) * (acc[0][t][8 * s + 2 * e] + bias[t]), bfhi(zc[e]) * (acc[0][t][8 * s + 2 * e + 1] + bias[t]));
            auto s0 = __builtin_amdgcn_permlane32_swap(p[0], p[2], false, false);
            auto s1 = __builtin_amdgcn_permlane32_swap(p[1], p[3], false, false);
            u32x4 v = {s0[0], s1[0], s0[1], s1[1]};
            *(u32x4*)(ws + WS_CAT + ((size_t)(tt * 2 + t) * CAT_KS + 32 + fs0 + s) * 1024 + lane * 16) = v;
        }
    STAMP(9);
    STAMP_FLUSH(1, ws);
}

__global__ __launch_bounds__(512, 2) void k2_kernel(const char* __restrict__ wsp, const __hip_bfloat16* __restrict__ bsp, char* __restrict__ ws,
                                                    const char* pf_x, const char* pf_wo, const char* pf_wout) {
    extern __shared__ __attribute__((aligned(16))) char smem[];
    const unsigned lds0 = (unsigned)(uintptr_t)(LAS char*)smem;
    const int wid = __builtin_amdgcn_readfirstlane(threadIdx.x >> 6);
    const int b = blockIdx.x;
    const int head = (b & 7) * 4 + (b >> 6);
    if (wid < 4) att_role(head, (b >> 3) & 7, wid, ws, lds0);
    else spg_role(b, head, wid - 4, wsp, bsp, ws, lds0, pf_x, pf_wo, pf_wout);
}

constexpr int K3_D = 6, K3_RING = K3_D * 4096, K3_LDS = 4 * K3_RING;

__global__ __launch_bounds__(256, 1) void k3_kernel(const char* __restrict__ x, const char* __restrict__ wo, const char* __restrict__ wout,
                                                    const char* __restrict__ ws, char* __restrict__ out) {
    extern __shared__ __attribute__((aligned(16))) char smem[];
    const unsigned lds0 = (unsigned)(uintptr_t)(LAS char*)smem;
    const int lane = threadIdx.x & 63;
    const int kq = __builtin_amdgcn_readfirstlane(threadIdx.x >> 6);
    const int b = blockIdx.x, xc = b & 7, rr = b >> 3;
    const int tt = xc * 4 + (rr & 3), nt = rr >> 2;
    STAMP_DECL; STAMP(0); STAMPC(14);
    const unsigned l16 = lane * 16;
    const unsigned bvoff = ((lane >> 2) & 7) * 1024 + (lane >> 5) * 64 + (lane & 3) * 16;
    const unsigned ring = lds0 + kq * K3_RING;
    const char* ab = ws + WS_CAT + ((size_t)tt * 2 * CAT_KS + kq * 22) * 1024;
    auto issue = [&](int s, unsigned dst) {
        const int k = (kq * 22 + s) * 16;
        const char* bs = (k < 512 ? wo + (size_t)k * 1024 : wout + (size_t)(k - 512) * 1024) + nt * 128;
        glds16(ab + s * 1024, l16, dst);
        glds16(bs, bvoff, dst + 2048);
        glds16(ab + (CAT_KS + s) * 1024, l16, dst + 1024);
        glds16(bs + 8192, bvoff, dst + 3072);
    };
    const size_t o = (size_t)(tt * 64 + (kq >> 1) * 32) * 1024 + (nt * 64 + (kq & 1) * 32) * 2;
    const unsigned xo = (lane & 31) * 1024 + (lane >> 5) * 16;
    u32x4 xv[2];
    GLD16(xv[0], xo, x + o, 0); GLD16(xv[1], xo, x + o, 32);
#pragma unroll 1
    for (int s = 0; s < K3_D; ++s) issue(s, ring + s * 4096);
    STAMP(1);
    f32x16 acc[2][2] = {{splat16(0.f), splat16(0.f)}, {splat16(0.f), splat16(0.f)}};
    const unsigned aa0 = ring + (kq >> 1) * 1024 + l16, aa1 = aa0 ^ 1024;
    const unsigned ba0 = ring + 2048 + (lane >> 5) * 1024 + (kq & 1) * 512 + ((lane & 15) >> 2) * 64 + ((lane >> 4) & 1) * 32 + (lane & 3) * 8, ba1 = ba0 ^ 512;
    bf16x8 fr[2][4];
#define K3READ(F, so) do { F[0] = ldsfrag(aa0 + (so)); F[1] = ldsfrag(aa1 + (so)); F[2] = trfrag(ba0 + (so), ba0 + (so) + 256); F[3] = trfrag(ba1 + (so), ba1 + (so) + 256); } while (0)
#define K3LW(F) asm volatile("s_waitcnt lgkmcnt(0)" : "+v"(F[0]), "+v"(F[1]), "+v"(F[2]), "+v"(F[3]) :: "memory")
#define K3MMA(F) do { acc[0][0] = MFMA32(F[2], F[0], acc[0][0]); acc[0][1] = MFMA32(F[3], F[0], acc[0][1]); acc[1][0] = MFMA32(F[2], F[1], acc[1][0]); acc[1][1] = MFMA32(F[3], F[1], acc[1][1]); } while (0)
#define K3NEXT(x) ((x) == (K3_D - 1) * 4096 ? 0u : (x) + 4096u)
    WAITV(20);
    K3READ(fr[0], 0);
    unsigned so = 0;
#pragma unroll 1
    for (int s = 0; s < 16; s += 2) {
        const unsigned s1 = K3NEXT(so), s2 = K3NEXT(s1);
        K3LW(fr[0]); WAITV(16); K3READ(fr[1], s1); SB(); issue(s + K3_D, ring + so); K3MMA(fr[0]); SB();
        K3LW(fr[1]); WAITV(16); K3READ(fr[0], s2); SB(); issue(s + 1 + K3_D, ring + s1); K3MMA(fr[1]); SB();
        so = s2;
    }
    STAMP(2);
#define K3TAIL(n, i) do { WAITV(n); K3READ(fr[(i + 1) & 1], ((i + 1) % K3_D) * 4096); SB(); K3MMA(fr[i & 1]); SB(); } while (0)
    K3TAIL(16, 16); K3TAIL(12, 17); K3TAIL(8, 18); K3TAIL(4, 19); K3TAIL(0, 20);
    K3MMA(fr[1]);
    STAMP(3);
#pragma unroll
    for (int j = 1; j < 4; ++j)
#pragma unroll
        for (int q = 0; q < 4; ++q) {
            const f32x16& t = acc[j >> 1][j & 1];
            f32x4 v = {t[4 * q], t[4 * q + 1], t[4 * q + 2], t[4 * q + 3]};
            *(LAS f32x4*)(uintptr_t)(ring + (j * 4 + q) * 1024 + l16) = v;
        }
    __syncthreads();
    STAMP(4);
#pragma unroll
    for (int j = 1; j < 4; ++j)
#pragma unroll
        for (int q = 0; q < 4; ++q) {
            f32x4 v = *(LAS f32x4*)(uintptr_t)(lds0 + (kq ^ j) * K3_RING + (j * 4 + q) * 1024 + l16);
#pragma unroll
            for (int e = 0; e < 4; ++e) acc[0][0][4 * q + e] += v[e];
        }
    asm volatile("" : "+v"(xv[0]), "+v"(xv[1]));
#pragma unroll
    for (int s = 0; s < 2; ++s) {
        auto a0 = __builtin_amdgcn_permlane32_swap(xv[s][0], xv[s][2], false, false);
        auto a1 = __builtin_amdgcn_permlane32_swap(xv[s][1], xv[s][3], false, false);
        const unsigned xc4[4] = {a0[0], a1[0], a0[1], a1[1]};
        unsigned p[4];
#pragma unroll
        for (int e = 0; e < 4; ++e) p[e] = pk2(bflo(xc4[e]) + acc[0][0][8 * s + 2 * e], bfhi(xc4[e]) + acc[0][0][8 * s + 2 * e + 1]);
        auto s0 = __builtin_amdgcn_permlane32_swap(p[0], p[2], false, false);
        auto s1 = __builtin_amdgcn_permlane32_swap(p[1], p[3], false, false);
        u32x4 v = {s0[0], s1[0], s0[1], s1[1]};
        *(u32x4*)(out + o + xo + s * 32) = v;
    }
    STAMP(5);
    STAMP_FLUSH(2, ws);
}

extern "C" void kernel_launch(void* const* d_in, const int* in_sizes, int n_in, void* d_out, int out_size, void* d_ws, size_t ws_size,
                              hipStream_t stream) {
    const char* x = (const char*)d_in[0];
    const char* wqkv = (const char*)d_in[1];
    const char* wo = (const char*)d_in[2];
    const char* win = (const char*)d_in[4];
    const char* wsp = (const char*)d_in[5];
    const __hip_bfloat16* bsp = (const __hip_bfloat16*)d_in[6];
    const char* wout = (const char*)d_in[7];
    char* ws = (char*)d_ws;
    (void)hipFuncSetAttribute((const void*)k1_kernel, hipFuncAttributeMaxDynamicSharedMemorySize, K1_LDS);
    (void)hipFuncSetAttribute((const void*)k2_kernel, hipFuncAttributeMaxDynamicSharedMemorySize, K2_LDS);
    (void)hipFuncSetAttribute((const void*)k3_kernel, hipFuncAttributeMaxDynamicSharedMemorySize, K3_LDS);
    hipLaunchKernelGGL(k1_kernel, dim3(256), dim3(512), K1_LDS, stream, x, wqkv, win, ws);
    hipLaunchKernelGGL(k2_kernel, dim3(256), dim3(512), K2_LDS, stream, wsp, bsp, ws, x, wo, wout);
    hipLaunchKernelGGL(k3_kernel, dim3(256), dim3(256), K3_LDS, stream, x, wo, wout, (const char*)ws, (char*)d_out);
}
```

```cpp
#include <hip/hip_runtime.h>
#include <hip/hip_bf16.h>
#include <stdint.h>

typedef __attribute__((ext_vector_type(2))) __bf16 bf16x2;
typedef __attribute__((ext_vector_type(8))) __bf16 bf16x8;
typedef __attribute__((ext_vector_type(16))) __bf16 bf16x16;
typedef __attribute__((ext_vector_type(2))) float f32x2;
typedef __attribute__((ext_vector_type(4))) float f32x4;
typedef __attribute__((ext_vector_type(8))) float f32x8;
typedef __attribute__((ext_vector_type(16))) float f32x16;
typedef __attribute__((ext_vector_type(2))) unsigned u32x2;
typedef __attribute__((ext_vector_type(4))) unsigned u32x4;
typedef __attribute__((ext_vector_type(4))) short s16x4;
#define LAS __attribute__((address_space(3)))
#define DI __device__ __forceinline__
#define SB() __builtin_amdgcn_sched_barrier(0)
#define PIN(x) asm volatile("" : "+v"(x))
#define WAITV(n) asm volatile("s_waitcnt vmcnt(" #n ")" ::: "memory")
#define MFMA16(a, b, c) __builtin_amdgcn_mfma_f32_16x16x32_bf16(a, b, c, 0, 0, 0)
#define MFMA32(a, b, c) __builtin_amdgcn_mfma_f32_32x32x16_bf16(a, b, c, 0, 0, 0)

constexpr int S_ = 2048, D_ = 512, FH = 896;
constexpr size_t WS_Q = 0, WS_K = 2u << 20, WS_V = 4u << 20, WS_Z1 = 6u << 20, WS_Z2 = 10u << 20, WS_Z2N = 14u << 20,
                 WS_CAT = 18u << 20, WS_FLAG = 24u << 20;
constexpr int FLAG_REP = 32, FLAG_STRIDE = 4352;
constexpr int CAT_KS = 88;

#define STAMP_DECL
#define STAMP(i)
#define STAMPC(i)
#define STAMP_FLUSH(kern, wsp_)

#define STFLAG "sc1"
#define GST16(voff, val, sbase) asm volatile("global_store_dwordx4 %0, %1, %2 " STFLAG "\n\ts_nop 1" :: "v"(voff), "v"(val), "s"(sbase) : "memory")

#define GLD16(dst, voff, sbase, IMM) asm volatile("global_load_dwordx4 %0, %1, %2 offset:" #IMM : "=v"(dst) : "v"(voff), "s"(sbase) : "memory")

DI void glds16(const void* sbase, unsigned voff, unsigned ldsdst) {
    unsigned keep;
    asm volatile("s_mov_b32 %0, m0\n\ts_mov_b32 m0, %3\n\ts_nop 0\n\tglobal_load_lds_dwordx4 %1, %2\n\ts_mov_b32 m0, %0"
                 : "=&s"(keep) : "v"(voff), "s"(sbase), "s"(ldsdst) : "memory");
}
DI unsigned pk2(float a, float b) { f32x2 v = {a, b}; return __builtin_bit_cast(unsigned, __builtin_convertvector(v, bf16x2)); }
DI float bflo(unsigned u) { return __uint_as_float(u << 16); }
DI float bfhi(unsigned u) { return __uint_as_float(u & 0xffff0000u); }
DI s16x4 trread(unsigned addr) { return __builtin_amdgcn_ds_read_tr16_b64_v4i16((LAS s16x4*)(uintptr_t)addr); }
DI bf16x8 trfrag(unsigned addr0, unsigned addr1) {
    s16x4 lo = trread(addr0), hi = trread(addr1);
    return __builtin_bit_cast(bf16x8, __builtin_shufflevector(lo, hi, 0, 1, 2, 3, 4, 5, 6, 7));
}
DI bf16x8 ldsfrag(unsigned addr) { return *(const LAS bf16x8*)(uintptr_t)addr; }
DI float gelu_t(float u) {
    const float k1 = -2.3022081985f;
    float w = u * __builtin_fmaf(u * u, k1 * 0.044715f, k1);
    float e = __builtin_amdgcn_exp2f(w);
    return u * __builtin_amdgcn_rcpf(1.0f + e);
}

constexpr int K1_MISC = 9 * 16384;
constexpr int K1_SUM = K1_MISC;
constexpr int K1_CS = K1_MISC + 1024;
constexpr int K1_PRIV = K1_MISC + 1024 + 2048;
constexpr int K1_LDS = K1_PRIV + 8 * 256;
enum { EPI_Q = 0, EPI_K = 1, EPI_V = 2, EPI_Z1 = 3, EPI_Z2 = 4 };
DI f32x16 splat16(float v) { f32x16 r; for (int i = 0; i < 16; ++i) r[i] = v; return r; }

DI float dot2m(unsigned a, unsigned m1, unsigned m2, float c) {
    const unsigned b = (a & m1) | m2;
    return __builtin_amdgcn_fdot2_f32_bf16(__builtin_bit_cast(bf16x2, a), __builtin_bit_cast(bf16x2, b), c, false);
}

template <bool ORI2, bool GELU>
DI void k1_body(const char* __restrict__ x, const char* __restrict__ wb  , int rp, char* __restrict__ dst  ,
                unsigned dvoff, unsigned jstride, unsigned pstride, float oscale, char* __restrict__ ws, unsigned lds0, u32x4 junk) {
    constexpr int LDW = GELU ? 3584 : 3072, NP = GELU ? 3 : 4;
    const int tid = threadIdx.x, lane = tid & 63;
    const int wid = __builtin_amdgcn_readfirstlane(tid >> 6);
    const int wm = wid & 3, wn = wid >> 2;
    const int r = lane & 31, h = lane >> 5;
    STAMP_DECL; STAMP(0); STAMPC(14);
    const char* xa = x + (size_t)rp * 131072 + wid * 16384;
    unsigned xvoff[2];
#pragma unroll
    for (int i = 0; i < 2; ++i) xvoff[i] = (i * 8 + (lane >> 3)) * 1024 + (((lane & 7) ^ ((4 * i + (lane >> 4)) & 7)) << 4);
    const char* wa = wb + (size_t)wid * 16 * LDW;
    const unsigned wvoff = ((lane >> 2) & 7) * LDW + (lane >> 5) * 64 + (lane & 3) * 16;
    const unsigned sdst = lds0 + wid * 2048;
#define ISSX(c, slot) do { glds16(xa + (c) * 128, xvoff[0], sdst + (slot) * 16384); glds16(xa + (c) * 128, xvoff[1], sdst + (slot) * 16384 + 1024); } while (0)
#define ISSW1(wp, st, i, sl) glds16((wp) + (size_t)((st) * 128 + (i) * 8) * LDW, wvoff, (sl) + (i) * 1024)
#define ISSW(wp, st, slot) do { ISSW1(wp, st, 0, sdst + (slot) * 16384); ISSW1(wp, st, 1, sdst + (slot) * 16384); } while (0)
    ISSX(0, 0); ISSX(1, 1); ISSW(wa, 0, 2);
    ISSX(2, 4); ISSX(3, 5); ISSW(wa, 1, 6);
    ISSX(4, 7); ISSX(5, 8); ISSW(wa, 2, 3);
    STAMP(1);
    unsigned xad[4], bad[4];
#pragma unroll
    for (int t = 0; t < 4; ++t) {
        xad[t] = lds0 + (wm * 32 + r) * 128 + (((2 * (t ^ wm) + h) ^ ((r >> 1) & 7)) << 4);
        bad[t] = lds0 + (2 * (t ^ wm) + h) * 1024 + wn * 512 + ((lane & 15) >> 2) * 64 + ((lane >> 4) & 1) * 32 + (lane & 3) * 8;
    }
    const unsigned selw = ((lane & 15) == ((lane >> 4) & 1)) ? 0x3f803f80u : 0u;
    u32x4 selu = {selw, selw, selw, selw};
    const bf16x8 sel = __builtin_bit_cast(bf16x8, selu);
    const unsigned m1 = wn ? 0u : 0xffffffffu, m2 = wn ? 0x3f803f80u : 0u;
    bf16x8 af[32];
    f32x16 acc[2];
    f32x4 csum = {0, 0, 0, 0};
    float st0 = 0.f, st1 = 0.f;
#define MM(bf, a, c) (ORI2 ? MFMA32(a, bf, c) : MFMA32(bf, a, c))
#define BFR(i, off) trfrag(bad[(i) & 3] + (off) + (((i) >> 2) & 1) * 8192, bad[(i) & 3] + (off) + (((i) >> 2) & 1) * 8192 + 256)
#define DOTS(i0) do { _Pragma("unroll") for (int i = i0; i < i0 + 2; ++i) { const u32x4 u = __builtin_bit_cast(u32x4, af[i]); \
            st0 = dot2m(u[0], m1, m2, st0); st1 = dot2m(u[1], m1, m2, st1); st0 = dot2m(u[2], m1, m2, st0); st1 = dot2m(u[3], m1, m2, st1); } \
        PIN(st0); PIN(st1); SB(); } while (0)
#define STAGE0(st, sx0, sx1, sb, G0, G1, G2) do { \
        bf16x8 bf[8]; \
        _Pragma("unroll") for (int i = 0; i < 8; ++i) { af[st * 8 + i] = ldsfrag(xad[i & 3] + (i < 4 ? sx0 : sx1) * 16384); bf[i] = BFR(i, sb * 16384); } \
        SB(); G0; SB(); \
        _Pragma("unroll") for (int i = 0; i < 8; ++i) { \
            if (st == 0 && i < 2) acc[i & 1] = MM(bf[i], af[i], splat16(0.f)); else acc[i & 1] = MM(bf[i], af[st * 8 + i], acc[i & 1]); \
            if ((i & 3) == 0) csum = MFMA16(sel, bf[i], csum); \
            if (i == 4) { PIN(acc[0]); PIN(acc[1]); SB(); G1; SB(); } } \
        PIN(acc[0]); PIN(acc[1]); PIN(csum); SB(); \
        DOTS(st * 8); DOTS(st * 8 + 2); G2; SB(); DOTS(st * 8 + 4); DOTS(st * 8 + 6); \
    } while (0)
#define BAR(n) asm volatile("s_waitcnt vmcnt(" #n ") lgkmcnt(0)\n\ts_barrier" ::: "memory")
    const char* w1 = wa + 128;
    BAR(12); STAMP(2);
    asm volatile("" :: "v"(junk));
    STAGE0(0, 0, 1, 2, {}, {}, {});
    BAR(6); STAMP(3);
    STAGE0(1, 4, 5, 6, ISSX(6, 0), ISSX(7, 1), ISSW(wa, 3, 2));
    BAR(6); STAMP(4);
    STAGE0(2, 7, 8, 3, ISSW(w1, 0, 4), ISSW(w1, 1, 5), ISSW(w1, 2, 6));
    BAR(6); STAMP(5);
    STAGE0(3, 0, 1, 2, ISSW(w1, 3, 7), {}, {});
    const unsigned csw = lds0 + K1_CS + wn * 512 + (lane & 15) * 16 + wm * 4;
    const unsigned csr = lds0 + K1_CS + wn * 512 + r * 16;
    const unsigned priv = lds0 + K1_PRIV + wid * 256;
#define CSWRITE(par) do { if (lane < 16) { *(LAS float*)(uintptr_t)(csw + (par)) = csum[0]; *(LAS float*)(uintptr_t)(csw + (par) + 256) = csum[1]; } } while (0)
    {
        float t = st0 + st1;
        auto rr = __builtin_amdgcn_permlane32_swap(__float_as_uint(t), __float_as_uint(t), false, false);
        t = __uint_as_float(rr[0]) + __uint_as_float(rr[1]);
        *(LAS float*)(uintptr_t)(lds0 + K1_SUM + wn * 512 + (wm * 32 + r) * 4) = t;
        CSWRITE(0);
    }
    BAR(0); STAMP(6);
    float rs, nm;
    {
        const float ss = *(LAS float*)(uintptr_t)(lds0 + K1_SUM + (wm * 32 + r) * 4), sm = *(LAS float*)(uintptr_t)(lds0 + K1_SUM + 512 + (wm * 32 + r) * 4);
        const float mean = sm * (1.0f / 512.0f);
        const float var = fmaxf(ss * (1.0f / 512.0f) - mean * mean, 0.0f);
        rs = __builtin_amdgcn_rsqf(var + 1e-5f) * oscale;
        nm = -mean * rs;
        if (ORI2) *(LAS float*)(uintptr_t)(priv + h * 128 + r * 4) = h ? nm : rs;
    }
    f32x4 ea, eb; float cs, ylo; unsigned pk[4];
#define EPI_BEGIN(par) do { f32x4 c4 = *(LAS f32x4*)(uintptr_t)(csr + (par)); cs = (c4[0] + c4[1]) + (c4[2] + c4[3]); \
        if (!ORI2) *(LAS float*)(uintptr_t)(priv + r * 4) = cs; } while (0)
#define EPI_LOAD(q) do { ea = *(LAS f32x4*)(uintptr_t)(priv + (q) * 32 + h * 16); if (ORI2) eb = *(LAS f32x4*)(uintptr_t)(priv + 128 + (q) * 32 + h * 16); } while (0)
#define EPI_ELEM(PV, e) do { float y_ = ORI2 ? __builtin_fmaf(eb[(e) & 3], cs, PV[e] * ea[(e) & 3]) : __builtin_fmaf(nm, ea[(e) & 3], PV[e] * rs); \
        if (GELU) y_ = gelu_t(y_); if ((e) & 1) { pk[((e) >> 1) & 3] = pk2(ylo, y_); PIN(pk[((e) >> 1) & 3]); } else { ylo = y_; PIN(ylo); } } while (0)
#define EPI_STORE(j) do { auto s0_ = __builtin_amdgcn_permlane32_swap(pk[0], pk[2], false, false); auto s1_ = __builtin_amdgcn_permlane32_swap(pk[1], pk[3], false, false); \
        u32x4 v_ = {s0_[0], s1_[0], s0_[1], s1_[1]}; GST16(dvoff, v_, dst + (j) * jstride); } while (0)
#pragma unroll
    for (int t = 0; t < 4; ++t) bad[t] += 65536;
    unsigned par = 0, nslot = sdst;
    const char* wnx = wa + 256;
    constexpr int PD = 4;
#pragma unroll 1
    for (int p = 1; p < NP; ++p) {
        f32x16 pv = acc[0] + acc[1]; PIN(pv);
        const bool more = p + 1 < NP;
        bf16x8 bf[PD + 1];
#pragma unroll
        for (int i = 0; i < PD; ++i) bf[i] = BFR(i, 0);
        SB();
#pragma unroll
        for (int i = 0; i < 32; ++i) {
            if (i + PD < 32) bf[(i + PD) % (PD + 1)] = BFR(i + PD, ((i + PD) >> 3) * 16384);
            if (i < 2) acc[i & 1] = MM(bf[i], af[i], splat16(0.f)); else acc[i & 1] = MM(bf[i % (PD + 1)], af[i], acc[i & 1]);
            PIN(acc[i & 1]);
            if ((i & 3) == 0) { if (i == 0) { f32x4 z4 = {0, 0, 0, 0}; csum = MFMA16(sel, bf[0], z4); } else csum = MFMA16(sel, bf[i % (PD + 1)], csum); PIN(csum); }
            if (i < 16 && (i & 1) && more) ISSW1(wnx, i >> 2, (i >> 1) & 1, nslot + (i >> 2) * 16384);
            if (i == 0) EPI_BEGIN(par);
            if (i >= 3) {
                const int u = i - 3;
                if (u == 14 || u == 28) EPI_STORE(u / 14 - 1);
                if (u % 7 == 0 && u < 28) EPI_LOAD(u / 7);
                if (u % 7 >= 3) EPI_ELEM(pv, (u / 7) * 4 + (u % 7) - 3);
            }
            SB();
        }
        par ^= 1024;
        CSWRITE(par);
        BAR(2);
        if (p == 1) STAMP(7); else if (p == 2) STAMP(8); else STAMP(9);
#pragma unroll
        for (int t = 0; t < 4; ++t) bad[t] ^= 65536;
        nslot ^= 65536; wnx += 128; dst += pstride;
    }
    EPI_BEGIN(par);
    const f32x16 pl = acc[0] + acc[1];
#pragma unroll
    for (int q = 0; q < 4; ++q) {
        EPI_LOAD(q);
#pragma unroll
        for (int e = 0; e < 4; ++e) EPI_ELEM(pl, q * 4 + e);
        if (q & 1) EPI_STORE(q >> 1);
    }
    STAMP(12); WAITV(0); STAMP(13);
    STAMP_FLUSH(0, ws);
}

__global__ __launch_bounds__(512, 2) void k1_kernel(const char* __restrict__ x, const char* __restrict__ wqkv, const char* __restrict__ win, char* __restrict__ ws) {
    extern __shared__ __attribute__((aligned(16))) char smem[];
    const unsigned lds0 = (unsigned)(uintptr_t)(LAS char*)smem;
    const int b = blockIdx.x, xc = b & 7, rr = b >> 3;
    const int rp = (xc & 1) * 8 + (rr & 7), ct = (xc >> 1) * 4 + (rr >> 3);
    const int lane = threadIdx.x & 63, wid = __builtin_amdgcn_readfirstlane(threadIdx.x >> 6);
    const int rt = rp * 4 + (wid & 3), wn = wid >> 2;
    u32x4 junk = {0, 0, 0, 0};
    {
        uint64_t pc0; unsigned len;
        asm volatile("s_getpc_b64 %0\nk1_code_begin:\n\ts_mov_b32 %1, k3_code_end-k1_code_begin" : "=s"(pc0), "=s"(len));
        pc0 &= ~(uint64_t)127;
        const unsigned off = (wid * 32 + rr) * 1024;
        if (len < (256u << 10) && off + 1024 <= len) GLD16(junk, lane * 16, (const char*)(pc0 + off), 0);
    }
    if (threadIdx.x < FLAG_REP) *(unsigned*)(ws + WS_FLAG + threadIdx.x * FLAG_STRIDE + b * 4) = 0u;
    if (ct < 4) {
        const int head0 = (ct & 1) * 16 + wn * 2;
        k1_body<false, false>(x, wqkv + ct * 512, rp, ws + (ct < 2 ? WS_Q : WS_K) + ((size_t)head0 * 64 + rt) * 1024, lane * 16, 65536, 4 * 65536,
                              ct < 2 ? 0.25f * 1.4426950408889634f : 1.0f, ws, lds0, junk);
    } else if (ct < 6) {
        const int head0 = (ct & 1) * 16 + wn * 2;
        k1_body<true, false>(x, wqkv + ct * 512, rp, ws + WS_V + ((size_t)head0 * 64 + rt) * 1024, ((lane >> 4) & 1) * 65536 + (lane >> 5) * 256 + (lane & 15) * 16, 512, 4 * 65536,
                             1.0f, ws, lds0, junk);
    } else if (ct < 11) {
        const int c0 = min((ct - 6) * 192, 704);
        k1_body<false, true>(x, win + c0 * 2, rp, ws + WS_Z1 + ((size_t)rt * 56 + (c0 >> 4) + wn * 2) * 1024, lane * 16, 1024, 4096, 1.0f, ws, lds0, junk);
    } else {
        const int c0 = min((ct - 11) * 192, 704);
        k1_body<true, true>(x, win + (FH + c0) * 2, rp, ws + WS_Z2 + ((size_t)rt * 4 * FH + c0 + wn * 32) * 16, (lane >> 5) * (FH * 16) + (lane & 31) * 16, 2 * FH * 16, 1024,
                            1.0f, ws, lds0, junk);
    }
}

constexpr int K2_K = 0, K2_V = 62 * 1024, K2_W = 126 * 1024, K2_ONES = K2_W + 32768, K2_MISC = K2_ONES + 1024, K2_LDS = K2_MISC + 1024;
enum { SY_KVA = 0, SY_KVB = 4, SY_KVC = 8, SY_P24 = 12, SY_LN = 16, SY_LNST = 20, SY_RED = 24, SY_FLAG = 28, SY_P30 = 32, SY_CMB = 36 };
constexpr int SPIN_MAX = 1 << 20;
constexpr unsigned FLAG_TAG = 0x5eed0001u;
constexpr float REF_OFF = 64.0f;

DI void lds_inc(unsigned addr) { __hip_atomic_fetch_add((LAS unsigned*)(uintptr_t)addr, 1u, __ATOMIC_RELAXED, __HIP_MEMORY_SCOPE_WORKGROUP); }
DI void lds_wait(unsigned addr, unsigned n) {
#pragma unroll 1
    for (int it = 0; it < SPIN_MAX && __builtin_amdgcn_readfirstlane(__hip_atomic_load((LAS unsigned*)(uintptr_t)addr, __ATOMIC_RELAXED, __HIP_MEMORY_SCOPE_WORKGROUP)) < n; ++it)
        __builtin_amdgcn_s_sleep(1);
    asm volatile("" ::: "memory");
}
DI bf16x16 lo8(const f32x16& x) { f32x8 t = __builtin_shufflevector(x, x, 0, 1, 2, 3, 4, 5, 6, 7); return __builtin_bit_cast(bf16x16, t); }
DI bf16x16 hi8(const f32x16& x) { f32x8 t = __builtin_shufflevector(x, x, 8, 9, 10, 11, 12, 13, 14, 15); return __builtin_bit_cast(bf16x16, t); }
#define SMF(a, b, c) __builtin_amdgcn_smfmac_f32_32x32x32_bf16(a, b, c, sidx, 0, 0)
#define GLD16S(dst, voff, sbase) asm volatile("global_load_dwordx4 %0, %1, %2 sc1" : "=v"(dst) : "v"(voff), "s"(sbase) : "memory")

DI float max16(const f32x16& s) {
    float a = fmaxf(fmaxf(s[0], s[1]), s[2]), b = fmaxf(fmaxf(s[3], s[4]), s[5]);
    a = fmaxf(fmaxf(a, s[6]), s[7]); b = fmaxf(fmaxf(b, s[8]), s[9]);
    a = fmaxf(fmaxf(a, s[10]), s[11]); b = fmaxf(fmaxf(b, s[12]), s[13]);
    a = fmaxf(fmaxf(a, s[14]), s[15]);
    return fmaxf(a, b);
}
DI float xhalf_max(float m) {
    auto rr = __builtin_amdgcn_permlane32_swap(__float_as_uint(m), __float_as_uint(m), false, false);
    return fmaxf(__uint_as_float(rr[0]), __uint_as_float(rr[1]));
}

DI void att_exact(const char* kg, const char* vg, bf16x8 q, f32x16& o_out) {
    const int lane = threadIdx.x & 63;
    const int sidx = (int)0xDDDDDDDDu;
    f32x16 z = splat16(0.f);
    float m = -INFINITY;
    for (int t = 0; t < 64; ++t) {
        bf16x8 kf = *(const bf16x8*)(kg + t * 1024 + lane * 16);
        f32x16 s = MFMA32(kf, q, z);
        m = fmaxf(m, max16(s));
    }
    m = xhalf_max(m);
    f32x16 c = splat16(-m), o = z;
    const bool isv = (lane & 16) == 0;
    u32x4 onesu = {0x3f803f80u, 0x3f803f80u, 0x3f803f80u, 0x3f803f80u};
    const unsigned vo = (lane >> 5) * 256 + (lane & 15) * 16;
    for (int t = 0; t < 64; ++t) {
        bf16x8 kf = *(const bf16x8*)(kg + t * 1024 + lane * 16);
        u32x4 v0 = *(const u32x4*)(vg + t * 1024 + vo), v1 = *(const u32x4*)(vg + t * 1024 + 512 + vo);
        if (!isv) { v0 = onesu; v1 = onesu; }
        f32x16 s = MFMA32(kf, q, c);
#pragma unroll
        for (int i = 0; i < 16; ++i) s[i] = __builtin_amdgcn_exp2f(s[i]);
        o = SMF(__builtin_bit_cast(bf16x8, v0), lo8(s), o);
        o = SMF(__builtin_bit_cast(bf16x8, v1), hi8(s), o);
    }
    o_out = o;
}

DI void att_store(const f32x16& o, char* dst, int lane) {
    const float rl = __builtin_amdgcn_rcpf(o[8]);
    unsigned p0 = pk2(o[0] * rl, o[1] * rl), p1 = pk2(o[2] * rl, o[3] * rl), p2 = pk2(o[4] * rl, o[5] * rl), p3 = pk2(o[6] * rl, o[7] * rl);
    auto s0 = __builtin_amdgcn_permlane32_swap(p0, p2, false, false);
    auto s1 = __builtin_amdgcn_permlane32_swap(p1, p3, false, false);
    u32x4 v = {s0[0], s1[0], s0[1], s1[1]};
    *(u32x4*)(dst + lane * 16) = v;
}
DI bool att_bad(const f32x16& o) {
    float a = fmaxf(fmaxf(fabsf(o[0]), fabsf(o[1])), fmaxf(fabsf(o[2]), fabsf(o[3])));
    float b = fmaxf(fmaxf(fabsf(o[4]), fabsf(o[5])), fmaxf(fabsf(o[6]), fabsf(o[7])));
    float s = (o[0] + o[1] + o[2] + o[3]) + (o[4] + o[5] + o[6] + o[7]);
    bool ok = (o[8] < 0x1p120f) && (o[8] > 0x1p-80f) && (fmaxf(a, b) < 0x1p124f) && (s == s);
    return __any(!ok);
}

DI void att_role(int head, int qb, int w, char* __restrict__ ws, unsigned lds0) {
    const int lane = threadIdx.x & 63;
    const int qh = w & 1, kh = w >> 1;
    const char* kg = ws + WS_K + (size_t)head * 65536;
    const char* vg = ws + WS_V + (size_t)head * 65536;
    const char* qg = ws + WS_Q + (size_t)head * 65536 + (size_t)(qb * 8 + qh * 4) * 1024;
    const unsigned l16 = lane * 16;
    const unsigned vo = (lane >> 5) * 256 + (lane & 15) * 16;
    u32x4 qr[4], k0, k1;
    STAMP_DECL; STAMP(0); STAMPC(14);
    const unsigned qo = l16 + kh * 2048, qo2 = l16 + (kh ^ 1) * 2048;
    GLD16(k0, l16, kg + kh * 32768, 0); GLD16(k1, l16, kg + (kh ^ 1) * 32768, 0);
    GLD16(qr[0], qo, qg, 0); GLD16(qr[1], qo, qg, 1024); GLD16(qr[2], qo2, qg, 0); GLD16(qr[3], qo2, qg, 1024);
    const char* ksrc = (qh ? vg : kg + 1024) + kh * 32768;
    const unsigned kdst = lds0 + (qh ? K2_V + kh * 32768 : K2_K + kh * 31744);
#pragma unroll
    for (int i = 0; i < 5; ++i) glds16(ksrc + i * 1024, l16, kdst + i * 1024);
    *(LAS unsigned*)(uintptr_t)(lds0 + K2_ONES + threadIdx.x * 4) = ((threadIdx.x >> 2) & 11) == 0 ? 0x3f803f80u : 0u;
    asm volatile("s_waitcnt lgkmcnt(0)\n\ts_barrier" ::: "memory");
#pragma unroll
    for (int i = 5; i < 13; ++i) glds16(ksrc + i * 1024, l16, kdst + i * 1024);
    STAMP(1);
    asm volatile("s_waitcnt vmcnt(13)" : "+v"(qr[0]), "+v"(qr[1]), "+v"(qr[2]), "+v"(qr[3]), "+v"(k0), "+v"(k1) :: "memory");
    STAMP(2);
#define QF(i) __builtin_bit_cast(bf16x8, qr[i])
    int sidx = (int)0xDDDDDDDDu; PIN(sidx);
    f32x16 cc[4];
#pragma unroll
    for (int i = 0; i < 4; i += 2) {
        f32x16 z = splat16(0.f);
        f32x16 s0 = MFMA32(__builtin_bit_cast(bf16x8, k0), QF(i), z), s1 = MFMA32(__builtin_bit_cast(bf16x8, k1), QF(i), z);
        f32x16 s2 = MFMA32(__builtin_bit_cast(bf16x8, k0), QF(i + 1), z), s3 = MFMA32(__builtin_bit_cast(bf16x8, k1), QF(i + 1), z);
        PIN(s0); PIN(s1); PIN(s2); PIN(s3);
        float mA = xhalf_max(fmaxf(max16(s0), max16(s1))), mB = xhalf_max(fmaxf(max16(s2), max16(s3)));
        cc[i] = splat16(-(mA + REF_OFF)); cc[i + 1] = splat16(-(mB + REF_OFF));
        PIN(cc[i]); PIN(cc[i + 1]);
    }
    bf16x8 kf = __builtin_bit_cast(bf16x8, k0);
    f32x16 oo[4] = {splat16(0.f), splat16(0.f), splat16(0.f), splat16(0.f)}, sA, sB = splat16(0.f);
    sA = MFMA32(kf, QF(0), cc[0]);
    WAITV(8); lds_inc(lds0 + K2_MISC + SY_KVA);
    unsigned kaddr = lds0 + K2_K + kh * 31744 + l16;
    unsigned vaddr = (lane & 16) ? (lds0 + K2_ONES + vo) : (lds0 + K2_V + kh * 32768 + vo);
    const unsigned vinc = (lane & 16) ? 0u : 1024u;
    lds_wait(lds0 + K2_MISC + SY_KVA, 4 * 64);
    bf16x8 v0 = ldsfrag(vaddr), v1 = v0;
    STAMP(3);
#define EXPS(X, a, b) do { _Pragma("unroll") for (int i_ = a; i_ < b; ++i_) X[i_] = __builtin_amdgcn_exp2f(X[i_]); PIN(X); SB(); } while (0)
#define HSTEP(X, Y, i, A1, A2, A3) do { \
        EXPS(X, 0, 2); \
        oo[(i + 3) & 3] = SMF(v1, hi8(Y), oo[(i + 3) & 3]); PIN(oo[(i + 3) & 3]); SB(); A1; SB(); \
        EXPS(X, 2, 5); \
        Y = MFMA32(kf, QF((i + 1) & 3), cc[(i + 1) & 3]); PIN(Y); SB(); A2; SB(); \
        EXPS(X, 5, 11); \
        oo[i] = SMF(v0, lo8(X), oo[i]); PIN(oo[i]); SB(); A3; SB(); \
        EXPS(X, 11, 16); \
    } while (0)
#pragma unroll 1
    for (int seg = 0; seg < 5; ++seg) {
        int n = 4;
        if (seg == 1) { n = 8; STAMP(4); WAITV(0); lds_inc(lds0 + K2_MISC + SY_KVB); lds_wait(lds0 + K2_MISC + SY_KVB, 4 * 64); STAMP(5); STAMPC(10); }
        else if (seg == 2) { n = 13; STAMP(6); STAMPC(11); lds_wait(lds0 + K2_MISC + SY_KVC, 4 * 64); STAMP(7); STAMPC(12); }
        else if (seg == 3) { n = 6; asm volatile("s_waitcnt lgkmcnt(0)" ::: "memory"); lds_inc(lds0 + K2_MISC + SY_P24); }
        else if (seg == 4) { n = 1; asm volatile("s_waitcnt lgkmcnt(0)" ::: "memory"); lds_inc(lds0 + K2_MISC + SY_P30); }
#pragma unroll 1
        for (int i = 0; i < n; ++i) {
            HSTEP(sA, sB, 0, { v1 = ldsfrag(vaddr + 512); vaddr += vinc; }, {}, {});
            HSTEP(sB, sA, 1, {}, {}, {});
            HSTEP(sA, sB, 2, {}, { kf = ldsfrag(kaddr); kaddr += 1024; }, {});
            HSTEP(sB, sA, 3, {}, {}, { v0 = ldsfrag(vaddr); });
        }
    }
    oo[3] = SMF(v1, hi8(sB), oo[3]);
    STAMP(8); STAMPC(13);
    {
        auto cbase = [&](int ww) { return lds0 + ((ww & 1) ? K2_V + (ww >> 1) * 32768 + 25 * 1024 : K2_K + (ww >> 1) * 31744 + 24 * 1024); };
        lds_wait(lds0 + K2_MISC + SY_P30, 4 * 64);
        const unsigned wa = cbase(w), ra = cbase(w ^ 2);
#pragma unroll
        for (int i = 0; i < 2; ++i) {
            f32x4 a = {oo[2 + i][0], oo[2 + i][1], oo[2 + i][2], oo[2 + i][3]}, b = {oo[2 + i][4], oo[2 + i][5], oo[2 + i][6], oo[2 + i][7]};
            *(LAS f32x4*)(uintptr_t)(wa + i * 2048 + l16) = a;
            *(LAS f32x4*)(uintptr_t)(wa + i * 2048 + 1024 + l16) = b;
            *(LAS float*)(uintptr_t)(wa + 4096 + i * 256 + lane * 4) = oo[2 + i][8];
        }
        asm volatile("s_waitcnt lgkmcnt(0)" ::: "memory");
        lds_inc(lds0 + K2_MISC + SY_CMB + qh * 4);
        lds_wait(lds0 + K2_MISC + SY_CMB + qh * 4, 2 * 64);
#pragma unroll
        for (int i = 0; i < 2; ++i) {
            f32x4 a = *(LAS f32x4*)(uintptr_t)(ra + i * 2048 + l16), b = *(LAS f32x4*)(uintptr_t)(ra + i * 2048 + 1024 + l16);
            float l = *(LAS float*)(uintptr_t)(ra + 4096 + i * 256 + lane * 4);
#pragma unroll
            for (int e = 0; e < 4; ++e) { oo[i][e] += a[e]; oo[i][4 + e] += b[e]; }
            oo[i][8] += l;
        }
    }
    if (__builtin_expect(att_bad(oo[0]), 0)) att_exact(kg, vg, QF(0), oo[0]);
    if (__builtin_expect(att_bad(oo[1]), 0)) att_exact(kg, vg, QF(1), oo[1]);
    char* dst = ws + WS_CAT + ((size_t)(qb * 8 + qh * 4 + kh * 2) * CAT_KS + head) * 1024;
    att_store(oo[0], dst, lane);
    att_store(oo[1], dst + CAT_KS * 1024, lane);
    STAMP(9);
    STAMP_FLUSH(1, ws);
}

template <int CTRL> DI float dppf(float x) { return __builtin_bit_cast(float, __builtin_amdgcn_mov_dpp(__builtin_bit_cast(int, x), CTRL, 0xf, 0xf, true)); }
DI float fold32(float a, float b) {
    auto r = __builtin_amdgcn_permlane32_swap(__float_as_uint(a), __float_as_uint(b), false, false);
    return __uint_as_float(r[0]) + __uint_as_float(r[1]);
}
DI float fold16(float a, float b) {
    auto r = __builtin_amdgcn_permlane16_swap(__float_as_uint(a), __float_as_uint(b), false, false);
    return __uint_as_float(r[0]) + __uint_as_float(r[1]);
}
DI float rowsum16(float v) { v += dppf<0xB1>(v); v += dppf<0x4E>(v); v += dppf<0x124>(v); v += dppf<0x128>(v); return v; }

DI void spg_role(int b, int head, int w, const char* __restrict__ wsp, const __hip_bfloat16* __restrict__ bsp, char* __restrict__ ws, unsigned lds0,
                 const char* pf_x, const char* pf_wo, const char* pf_wout) {
    const int lane = threadIdx.x & 63;
    const int xc = b & 7, rr = b >> 3;
    const int tt = xc * 4 + (rr & 3), ft = rr >> 2;
    const bool active = ft < 7;
    STAMP_DECL; STAMP(0); STAMPC(14);
    __builtin_amdgcn_s_setprio(2);
    const unsigned wring = lds0 + K2_W + w * 8192;
    const char* wbase = wsp + (size_t)tt * 64 * 4096 + w * 1024;
    unsigned wvoff[2];
#pragma unroll
    for (int p = 0; p < 2; ++p) wvoff[p] = (lane >> 3) * 4096 + (((lane & 7) ^ ((4 * p + (lane >> 4)) & 7)) << 4);
    const int rot = 0;
#define CROT(c) (((c) + rot) & 7)
#define ISSUEW(c, i) glds16(wbase + (size_t)(i) * 32768 + CROT(c) * 128, wvoff[(i) & 1], wring + (i) * 1024)
    {
        const char* src = ws + WS_Z2 + (size_t)b * FH * 16;
        char* dst = ws + WS_Z2N + (size_t)b * FH * 16;
        u32x4 zc[4], z0;
        const unsigned o0 = (w * 224 + lane) * 16;
        const unsigned o3 = (w * 224 + 192 + (lane & 31)) * 16;
        const unsigned zero = 0;
        GLD16(zc[0], o0, src, 0); GLD16(zc[1], o0, src, 1024); GLD16(zc[2], o0, src, 2048); GLD16(zc[3], o3, src, 0); GLD16(z0, zero, src, 0);
        *(LAS unsigned*)(uintptr_t)(lds0 + K2_MISC + (threadIdx.x - 256) * 4) = 0u;
        asm volatile("s_waitcnt lgkmcnt(0)\n\ts_barrier" ::: "memory");
        asm volatile("s_waitcnt vmcnt(0)" : "+v"(zc[0]), "+v"(zc[1]), "+v"(zc[2]), "+v"(zc[3]), "+v"(z0) :: "memory");
        STAMP(1);
        float sh[8], s1[8], s2[8];
#pragma unroll
        for (int e = 0; e < 4; ++e) { sh[2 * e] = bflo(z0[e]); sh[2 * e + 1] = bfhi(z0[e]); }
        const float m3 = lane < 32 ? 1.f : 0.f;
#pragma unroll
        for (int e = 0; e < 8; ++e) {
            float a = ((e & 1) ? bfhi(zc[0][e >> 1]) : bflo(zc[0][e >> 1])) - sh[e];
            float bb = ((e & 1) ? bfhi(zc[1][e >> 1]) : bflo(zc[1][e >> 1])) - sh[e];
            float c = ((e & 1) ? bfhi(zc[2][e >> 1]) : bflo(zc[2][e >> 1])) - sh[e];
            float d = (((e & 1) ? bfhi(zc[3][e >> 1]) : bflo(zc[3][e >> 1])) - sh[e]) * m3;
            s1[e] = (a + bb) + (c + d);
            s2[e] = (a * a + bb * bb) + (c * c + d * d);
        }
        f32x4 q;
#pragma unroll
        for (int i = 0; i < 4; ++i) q[i] = rowsum16(fold16(fold32(s1[i], s2[i]), fold32(s1[i + 4], s2[i + 4])));
        LAS float* part = (LAS float*)(uintptr_t)(lds0 + K2_MISC + 256);
        *(LAS f32x4*)(part + w * 16 + (lane >> 4) * 4) = q;
        asm volatile("s_waitcnt lgkmcnt(0)" ::: "memory");
        lds_inc(lds0 + K2_MISC + SY_LN);
        lds_wait(lds0 + K2_MISC + SY_LN, 4 * 64);
        STAMP(2);
        float rs[8], nm[8];
        {
            f32x4 t[4];
#pragma unroll
            for (int i = 0; i < 4; ++i) t[i] = (*(LAS f32x4*)(part + i * 4) + *(LAS f32x4*)(part + 16 + i * 4)) + (*(LAS f32x4*)(part + 32 + i * 4) + *(LAS f32x4*)(part + 48 + i * 4));
#pragma unroll
            for (int e = 0; e < 8; ++e) {
                float mean = t[e >> 2][e & 3] * (1.0f / FH);
                float var = fmaxf(t[2 + (e >> 2)][e & 3] * (1.0f / FH) - mean * mean, 0.f);
                rs[e] = __builtin_amdgcn_rsqf(var + 1e-5f);
                nm[e] = -(mean + sh[e]) * rs[e];
            }
        }
#pragma unroll
        for (int j = 0; j < 4; ++j) {
            u32x4 v;
#pragma unroll
            for (int e = 0; e < 4; ++e) v[e] = pk2(__builtin_fmaf(bflo(zc[j][e]), rs[2 * e], nm[2 * e]), __builtin_fmaf(bfhi(zc[j][e]), rs[2 * e + 1], nm[2 * e + 1]));
            const unsigned o = j < 3 ? o0 + j * 1024 : o3;
            asm volatile("global_store_dwordx4 %0, %1, %2 sc1\n\ts_nop 1" :: "v"(o), "v"(v), "s"(dst) : "memory");
        }
        STAMP(3);
        WAITV(0);
        STAMP(4);
        unsigned old = 0;
        if (lane == 0) old = __hip_atomic_fetch_add((LAS unsigned*)(uintptr_t)(lds0 + K2_MISC + SY_LNST), 1u, __ATOMIC_RELAXED, __HIP_MEMORY_SCOPE_WORKGROUP);
        old = __builtin_amdgcn_readfirstlane(old);
        if (old == 3 && lane < FLAG_REP) __hip_atomic_store((unsigned*)(ws + WS_FLAG + lane * FLAG_STRIDE) + b, FLAG_TAG, __ATOMIC_RELAXED, __HIP_MEMORY_SCOPE_AGENT);
    }
    {
        const char* src = ws + ((w & 1) ? WS_V : WS_K) + (size_t)head * 65536 + (w >> 1) * 32768;
        const unsigned dst = lds0 + ((w & 1) ? K2_V + (w >> 1) * 32768 : K2_K + (w >> 1) * 31744 - 1024);
#pragma unroll 1
        for (int t = 13; t < 32; ++t) glds16(src + t * 1024, lane * 16, dst + t * 1024);
#pragma unroll
        for (int i = 0; i < 8; ++i) ISSUEW(0, i);
        WAITV(8);
        lds_inc(lds0 + K2_MISC + SY_KVC);
    }
    if (!active) {
        lds_wait(lds0 + K2_MISC + SY_P24, 4 * 64);
        const int id = (rr & 3) * 4 + w;
        const unsigned lo = lane * 16;
        u32x4 t;
#pragma unroll 4
        for (int i = 0; i < 32; ++i) GLD16(t, lo, pf_wo + (size_t)(id * 32 + i) * 1024, 0);
#pragma unroll 4
        for (int i = 0; i < 56; ++i) { GLD16(t, lo, pf_wout + (size_t)(id * 56 + i) * 1024, 0); if ((i & 3) == 3) WAITV(40); }
#pragma unroll 4
        for (int i = 0; i < 16; ++i) { GLD16(t, lo, pf_x + (size_t)(xc * 256 + id * 16 + i) * 1024, 0); if ((i & 3) == 3) WAITV(40); }
        WAITV(0);
        STAMP_FLUSH(1, ws); return;
    }
    const char* zb = ws + WS_Z2N + ((size_t)w * 64 * FH + ft * 128) * 16;
    unsigned zvoff[4];
#pragma unroll
    for (int j = 0; j < 4; ++j) { zvoff[j] = (lane >> 5) * (FH * 16) + (j ^ w) * 512 + (lane & 31) * 16; PIN(zvoff[j]); }
    u32x4 zf[4][4];
#define ZPTR(c, ks) (zb + (size_t)(CROT(c) * 4 + (ks)) * (2 * FH * 16))
    f32x16 acc[4][2];
#pragma unroll
    for (int j = 0; j < 4; ++j) { acc[j][0] = splat16(0.f); acc[j][1] = splat16(0.f); PIN(acc[j][0]); PIN(acc[j][1]); }
    unsigned waddr[4];
#pragma unroll
    for (int ks = 0; ks < 4; ++ks) { waddr[ks] = wring + (lane & 31) * 128 + (((ks * 2 + (lane >> 5)) ^ ((lane >> 1) & 7)) << 4); PIN(waddr[ks]); }
    if (w == 0) {
        const char* fl = ws + WS_FLAG + (size_t)(b >> 3) * FLAG_STRIDE;
        const unsigned fo = lane * 16;
#pragma unroll 1
        for (int it = 0; it < SPIN_MAX; ++it) {
            u32x4 f;
            asm volatile("global_load_dwordx4 %0, %1, %2 sc1\n\ts_waitcnt vmcnt(0)" : "=&v"(f) : "v"(fo), "s"(fl) : "memory");
            if (__all(f[0] == FLAG_TAG && f[1] == FLAG_TAG && f[2] == FLAG_TAG && f[3] == FLAG_TAG)) break;
            __builtin_amdgcn_s_sleep(4);
        }
        lds_inc(lds0 + K2_MISC + SY_FLAG);
    } else lds_wait(lds0 + K2_MISC + SY_FLAG, 64);
    STAMP(5);
#pragma unroll
    for (int ks = 0; ks < 4; ++ks)
#pragma unroll
        for (int j = 0; j < 4; ++j) GLD16S(zf[ks][j], zvoff[j], ZPTR(0, ks));
    __builtin_amdgcn_s_setprio(0);
#define ZWAIT(n, ks) asm volatile("s_waitcnt vmcnt(" #n ")" : "+v"(zf[ks][0]), "+v"(zf[ks][1]), "+v"(zf[ks][2]), "+v"(zf[ks][3]) :: "memory")
#define KSTEP(ks, MORE) do { _Pragma("unroll") for (int j_ = 0; j_ < 4; ++j_) { \
        acc[j_][0] = MFMA32(__builtin_bit_cast(bf16x8, zf[ks][j_]), wf[0][ks], acc[j_][0]); PIN(acc[j_][0]); SB(); \
        if (MORE && ks == 0) ISSUEW(c + 1, 2 * j_); \
        acc[j_][1] = MFMA32(__builtin_bit_cast(bf16x8, zf[ks][j_]), wf[1][ks], acc[j_][1]); PIN(acc[j_][1]); SB(); \
        if (MORE && ks == 0) ISSUEW(c + 1, 2 * j_ + 1); \
        if (MORE) GLD16S(zf[ks][j_], zvoff[j_], ZPTR(c + 1, ks)); } } while (0)
#define WREAD() _Pragma("unroll") for (int ks = 0; ks < 4; ++ks) { wf[0][ks] = ldsfrag(waddr[ks]); wf[1][ks] = ldsfrag(waddr[ks] + 4096); } \
        asm volatile("s_waitcnt lgkmcnt(0)" : "+v"(wf[0][0]), "+v"(wf[0][1]), "+v"(wf[0][2]), "+v"(wf[0][3]), "+v"(wf[1][0]), "+v"(wf[1][1]), "+v"(wf[1][2]), "+v"(wf[1][3]) :: "memory")
#pragma unroll 1
    for (int c = 0; c < 7; ++c) {
        bf16x8 wf[2][4];
        ZWAIT(12, 0);
        WREAD();
        KSTEP(0, true);
        ZWAIT(20, 1); KSTEP(1, true);
        ZWAIT(20, 2); KSTEP(2, true);
        ZWAIT(20, 3); KSTEP(3, true);
    }
    const int fs0 = ft * 8 + w * 2;
    const char* z1p = ws + WS_Z1 + ((size_t)tt * 2 * 56 + fs0) * 1024;
    const char* bp = (const char*)bsp + tt * 128;
    const unsigned l16 = lane * 16, bo = (lane & 31) * 2;
    unsigned braw[2];
    {
        const int c = 7; (void)c;
        bf16x8 wf[2][4];
        ZWAIT(12, 0);
        WREAD();
        KSTEP(0, false);
        GLD16(zf[0][0], l16, z1p, 0); GLD16(zf[0][1], l16, z1p, 1024); GLD16(zf[0][2], l16, z1p + 56 * 1024, 0); GLD16(zf[0][3], l16, z1p + 56 * 1024, 1024);
        asm volatile("global_load_ushort %0, %1, %2" : "=v"(braw[0]) : "v"(bo), "s"(bp) : "memory");
        asm volatile("global_load_ushort %0, %1, %2 offset:64" : "=v"(braw[1]) : "v"(bo), "s"(bp) : "memory");
        ZWAIT(14, 1); KSTEP(1, false);
        ZWAIT(10, 2); KSTEP(2, false);
        ZWAIT(6, 3); KSTEP(3, false);
    }
    STAMP(6);
    __builtin_amdgcn_s_setprio(2);
    lds_wait(lds0 + K2_MISC + SY_P24, 4 * 64);
    STAMP(7);
    auto blk = [&](int dstw, int j) { return lds0 + ((dstw & 1) ? K2_V + (dstw >> 1) * 32768 : K2_K + (dstw >> 1) * 31744) + (j - 1) * 8192 + lane * 16; };
#pragma unroll
    for (int j = 1; j < 4; ++j) {
        const unsigned a = blk(j ^ w, j);
#pragma unroll
        for (int t = 0; t < 2; ++t)
#pragma unroll
            for (int q = 0; q < 4; ++q) {
                f32x4 v = {acc[j][t][4 * q], acc[j][t][4 * q + 1], acc[j][t][4 * q + 2], acc[j][t][4 * q + 3]};
                *(LAS f32x4*)(uintptr_t)(a + (t * 4 + q) * 1024) = v;
            }
    }
    asm volatile("s_waitcnt lgkmcnt(0)" ::: "memory");
    lds_inc(lds0 + K2_MISC + SY_RED);
    lds_wait(lds0 + K2_MISC + SY_RED, 4 * 64);
    STAMP(8);
#pragma unroll
    for (int j = 1; j < 4; ++j) {
        const unsigned a = blk(w, j);
#pragma unroll
        for (int t = 0; t < 2; ++t)
#pragma unroll
            for (int q = 0; q < 4; ++q) {
                f32x4 v = *(LAS f32x4*)(uintptr_t)(a + (t * 4 + q) * 1024);
#pragma unroll
                for (int e = 0; e < 4; ++e) acc[0][t][4 * q + e] += v[e];
            }
    }
    asm volatile("s_waitcnt vmcnt(0)" : "+v"(zf[0][0]), "+v"(zf[0][1]), "+v"(zf[0][2]), "+v"(zf[0][3]), "+v"(braw[0]), "+v"(braw[1]) :: "memory");
    const float bias[2] = {bflo(braw[0]), bflo(braw[1])};
#pragma unroll
    for (int t = 0; t < 2; ++t)
#pragma unroll
        for (int s = 0; s < 2; ++s) {
            auto a0 = __builtin_amdgcn_permlane32_swap(zf[0][2 * t + s][0], zf[0][2 * t + s][2], false, false);
            auto a1 = __builtin_amdgcn_permlane32_swap(zf[0][2 * t + s][1], zf[0][2 * t + s][3], false, false);
            const unsigned zc[4] = {a0[0], a1[0], a0[1], a1[1]};
            unsigned p[4];
#pragma unroll
            for (int e = 0; e < 4; ++e)
                p[e] = pk2(bflo(zc[e]) * (acc[0][t][8 * s + 2 * e] + bias[t]), bfhi(zc[e]) * (acc[0][t][8 * s + 2 * e + 1] + bias[t]));
            auto s0 = __builtin_amdgcn_permlane32_swap(p[0], p[2], false, false);
            auto s1 = __builtin_amdgcn_permlane32_swap(p[1], p[3], false, false);
            u32x4 v = {s0[0], s1[0], s0[1], s1[1]};
            *(u32x4*)(ws + WS_CAT + ((size_t)(tt * 2 + t) * CAT_KS + 32 + fs0 + s) * 1024 + lane * 16) = v;
        }
    STAMP(9);
    STAMP_FLUSH(1, ws);
}

__global__ __launch_bounds__(512, 2) void k2_kernel(const char* __restrict__ wsp, const __hip_bfloat16* __restrict__ bsp, char* __restrict__ ws,
                                                    const char* pf_x, const char* pf_wo, const char* pf_wout) {
    extern __shared__ __attribute__((aligned(16))) char smem[];
    const unsigned lds0 = (unsigned)(uintptr_t)(LAS char*)smem;
    const int wid = __builtin_amdgcn_readfirstlane(threadIdx.x >> 6);
    const int b = blockIdx.x;
    const int head = (b & 7) * 4 + (b >> 6);
    if (wid < 4) att_role(head, (b >> 3) & 7, wid, ws, lds0);
    else spg_role(b, head, wid - 4, wsp, bsp, ws, lds0, pf_x, pf_wo, pf_wout);
}

constexpr int K3_D = 6, K3_RING = K3_D * 4096, K3_LDS = 4 * K3_RING;

__global__ __launch_bounds__(256, 1) void k3_kernel(const char* __restrict__ x, const char* __restrict__ wo, const char* __restrict__ wout,
                                                    const char* __restrict__ ws, char* __restrict__ out) {
    extern __shared__ __attribute__((aligned(16))) char smem[];
    const unsigned lds0 = (unsigned)(uintptr_t)(LAS char*)smem;
    const int lane = threadIdx.x & 63;
    const int kq = __builtin_amdgcn_readfirstlane(threadIdx.x >> 6);
    const int b = blockIdx.x, xc = b & 7, rr = b >> 3;
    const int tt = xc * 4 + (rr & 3), nt = rr >> 2;
    STAMP_DECL; STAMP(0); STAMPC(14);
    const unsigned l16 = lane * 16;
    const unsigned bvoff = ((lane >> 2) & 7) * 1024 + (lane >> 5) * 64 + (lane & 3) * 16;
    const unsigned ring = lds0 + kq * K3_RING;
    const char* ab = ws + WS_CAT + ((size_t)tt * 2 * CAT_KS + kq * 22) * 1024;
    auto issue = [&](int s, unsigned dst) {
        const int k = (kq * 22 + s) * 16;
        const char* bs = (k < 512 ? wo + (size_t)k * 1024 : wout + (size_t)(k - 512) * 1024) + nt * 128;
        glds16(ab + s * 1024, l16, dst);
        glds16(bs, bvoff, dst + 2048);
        glds16(ab + (CAT_KS + s) * 1024, l16, dst + 1024);
        glds16(bs + 8192, bvoff, dst + 3072);
    };
    const size_t o = (size_t)(tt * 64 + (kq >> 1) * 32) * 1024 + (nt * 64 + (kq & 1) * 32) * 2;
    const unsigned xo = (lane & 31) * 1024 + (lane >> 5) * 16;
    u32x4 xv[2];
    GLD16(xv[0], xo, x + o, 0); GLD16(xv[1], xo, x + o, 32);
#pragma unroll 1
    for (int s = 0; s < K3_D; ++s) issue(s, ring + s * 4096);
    STAMP(1);
    f32x16 acc[2][2] = {{splat16(0.f), splat16(0.f)}, {splat16(0.f), splat16(0.f)}};
    const unsigned aa0 = ring + (kq >> 1) * 1024 + l16, aa1 = aa0 ^ 1024;
    const unsigned ba0 = ring + 2048 + (lane >> 5) * 1024 + (kq & 1) * 512 + ((lane & 15) >> 2) * 64 + ((lane >> 4) & 1) * 32 + (lane & 3) * 8, ba1 = ba0 ^ 512;
    bf16x8 fr[2][4];
#define K3READ(F, so) do { F[0] = ldsfrag(aa0 + (so)); F[1] = ldsfrag(aa1 + (so)); F[2] = trfrag(ba0 + (so), ba0 + (so) + 256); F[3] = trfrag(ba1 + (so), ba1 + (so) + 256); } while (0)
#define K3LW(F) asm volatile("s_waitcnt lgkmcnt(0)" : "+v"(F[0]), "+v"(F[1]), "+v"(F[2]), "+v"(F[3]) :: "memory")
#define K3MMA(F) do { acc[0][0] = MFMA32(F[2], F[0], acc[0][0]); acc[0][1] = MFMA32(F[3], F[0], acc[0][1]); acc[1][0] = MFMA32(F[2], F[1], acc[1][0]); acc[1][1] = MFMA32(F[3], F[1], acc[1][1]); } while (0)
#define K3NEXT(x) ((x) == (K3_D - 1) * 4096 ? 0u : (x) + 4096u)
    WAITV(20);
    K3READ(fr[0], 0);
    unsigned so = 0;
#pragma unroll 1
    for (int s = 0; s < 16; s += 2) {
        const unsigned s1 = K3NEXT(so), s2 = K3NEXT(s1);
        K3LW(fr[0]); WAITV(16); K3READ(fr[1], s1); SB(); issue(s + K3_D, ring + so); K3MMA(fr[0]); SB();
        K3LW(fr[1]); WAITV(16); K3READ(fr[0], s2); SB(); issue(s + 1 + K3_D, ring + s1); K3MMA(fr[1]); SB();
        so = s2;
    }
    STAMP(2);
#define K3TAIL(n, i) do { WAITV(n); K3READ(fr[(i + 1) & 1], ((i + 1) % K3_D) * 4096); SB(); K3MMA(fr[i & 1]); SB(); } while (0)
    K3TAIL(16, 16); K3TAIL(12, 17); K3TAIL(8, 18); K3TAIL(4, 19); K3TAIL(0, 20);
    K3MMA(fr[1]);
    STAMP(3);
#pragma unroll
    for (int j = 1; j < 4; ++j)
#pragma unroll
        for (int q = 0; q < 4; ++q) {
            const f32x16& t = acc[j >> 1][j & 1];
            f32x4 v = {t[4 * q], t[4 * q + 1], t[4 * q + 2], t[4 * q + 3]};
            *(LAS f32x4*)(uintptr_t)(ring + (j * 4 + q) * 1024 + l16) = v;
        }
    __syncthreads();
    STAMP(4);
#pragma unroll
    for (int j = 1; j < 4; ++j)
#pragma unroll
        for (int q = 0; q < 4; ++q) {
            f32x4 v = *(LAS f32x4*)(uintptr_t)(lds0 + (kq ^ j) * K3_RING + (j * 4 + q) * 1024 + l16);
#pragma unroll
            for (int e = 0; e < 4; ++e) acc[0][0][4 * q + e] += v[e];
        }
    asm volatile("" : "+v"(xv[0]), "+v"(xv[1]));
#pragma unroll
    for (int s = 0; s < 2; ++s) {
        auto a0 = __builtin_amdgcn_permlane32_swap(xv[s][0], xv[s][2], false, false);
        auto a1 = __builtin_amdgcn_permlane32_swap(xv[s][1], xv[s][3], false, false);
        const unsigned xc4[4] = {a0[0], a1[0], a0[1], a1[1]};
        unsigned p[4];
#pragma unroll
        for (int e = 0; e < 4; ++e) p[e] = pk2(bflo(xc4[e]) + acc[0][0][8 * s + 2 * e], bfhi(xc4[e]) + acc[0][0][8 * s + 2 * e + 1]);
        auto s0 = __builtin_amdgcn_permlane32_swap(p[0], p[2], false, false);
        auto s1 = __builtin_amdgcn_permlane32_swap(p[1], p[3], false, false);
        u32x4 v = {s0[0], s1[0], s0[1], s1[1]};
        *(u32x4*)(out + o + xo + s * 32) = v;
    }
    STAMP(5);
    STAMP_FLUSH(2, ws);
    asm volatile("k3_code_end:");
}

extern "C" void kernel_launch(void* const* d_in, const int* in_sizes, int n_in, void* d_out, int out_size, void* d_ws, size_t ws_size,
                              hipStream_t stream) {
    const char* x = (const char*)d_in[0];
    const char* wqkv = (const char*)d_in[1];
    const char* wo = (const char*)d_in[2];
    const char* win = (const char*)d_in[4];
    const char* wsp = (const char*)d_in[5];
    const __hip_bfloat16* bsp = (const __hip_bfloat16*)d_in[6];
    const char* wout = (const char*)d_in[7];
    char* ws = (char*)d_ws;
    (void)hipFuncSetAttribute((const void*)k1_kernel, hipFuncAttributeMaxDynamicSharedMemorySize, K1_LDS);
    (void)hipFuncSetAttribute((const void*)k2_kernel, hipFuncAttributeMaxDynamicSharedMemorySize, K2_LDS);
    (void)hipFuncSetAttribute((const void*)k3_kernel, hipFuncAttributeMaxDynamicSharedMemorySize, K3_LDS);
    hipLaunchKernelGGL(k1_kernel, dim3(256), dim3(512), K1_LDS, stream, x, wqkv, win, ws);
    hipLaunchKernelGGL(k2_kernel, dim3(256), dim3(512), K2_LDS, stream, wsp, bsp, ws, x, wo, wout);
    hipLaunchKernelGGL(k3_kernel, dim3(256), dim3(256), K3_LDS, stream, x, wo, wout, (const char*)ws, (char*)d_out);
}
```

```cpp
#include <hip/hip_runtime.h>
#include <hip/hip_bf16.h>
#include <stdint.h>

typedef __attribute__((ext_vector_type(2))) __bf16 bf16x2;
typedef __attribute__((ext_vector_type(8))) __bf16 bf16x8;
typedef __attribute__((ext_vector_type(16))) __bf16 bf16x16;
typedef __attribute__((ext_vector_type(2))) float f32x2;
typedef __attribute__((ext_vector_type(4))) float f32x4;
typedef __attribute__((ext_vector_type(8))) float f32x8;
typedef __attribute__((ext_vector_type(16))) float f32x16;
typedef __attribute__((ext_vector_type(2))) unsigned u32x2;
typedef __attribute__((ext_vector_type(4))) unsigned u32x4;
typedef __attribute__((ext_vector_type(4))) short s16x4;
#define LAS __attribute__((address_space(3)))
#define DI __device__ __forceinline__
#define SB() __builtin_amdgcn_sched_barrier(0)
#define PIN(x) asm volatile("" : "+v"(x))
#define WAITV(n) asm volatile("s_waitcnt vmcnt(" #n ")" ::: "memory")
#define MFMA16(a, b, c) __builtin_amdgcn_mfma_f32_16x16x32_bf16(a, b, c, 0, 0, 0)
#define MFMA32(a, b, c) __builtin_amdgcn_mfma_f32_32x32x16_bf16(a, b, c, 0, 0, 0)

constexpr int S_ = 2048, D_ = 512, FH = 896;
constexpr size_t WS_Q = 0, WS_K = 2u << 20, WS_V = 4u << 20, WS_Z1 = 6u << 20, WS_Z2 = 10u << 20, WS_Z2N = 14u << 20,
                 WS_CAT = 18u << 20, WS_FLAG = 24u << 20;
constexpr int FLAG_REP = 32, FLAG_STRIDE = 4352;
constexpr int CAT_KS = 88;

#define STAMP_DECL
#define STAMP(i)
#define STAMPC(i)
#define STAMP_FLUSH(kern, wsp_)

#define STFLAG "sc1"
#define GST16(voff, val, sbase) asm volatile("global_store_dwordx4 %0, %1, %2 " STFLAG "\n\ts_nop 1" :: "v"(voff), "v"(val), "s"(sbase) : "memory")

#define GLD16(dst, voff, sbase, IMM) asm volatile("global_load_dwordx4 %0, %1, %2 offset:" #IMM : "=v"(dst) : "v"(voff), "s"(sbase) : "memory")

DI void glds16(const void* sbase, unsigned voff, unsigned ldsdst) {
    unsigned keep;
    asm volatile("s_mov_b32 %0, m0\n\ts_mov_b32 m0, %3\n\ts_nop 0\n\tglobal_load_lds_dwordx4 %1, %2\n\ts_mov_b32 m0, %0"
                 : "=&s"(keep) : "v"(voff), "s"(sbase), "s"(ldsdst) : "memory");
}
DI unsigned pk2(float a, float b) { f32x2 v = {a, b}; return __builtin_bit_cast(unsigned, __builtin_convertvector(v, bf16x2)); }
DI float bflo(unsigned u) { return __uint_as_float(u << 16); }
DI float bfhi(unsigned u) { return __uint_as_float(u & 0xffff0000u); }
DI s16x4 trread(unsigned addr) { return __builtin_amdgcn_ds_read_tr16_b64_v4i16((LAS s16x4*)(uintptr_t)addr); }
DI bf16x8 trfrag(unsigned addr0, unsigned addr1) {
    s16x4 lo = trread(addr0), hi = trread(addr1);
    return __builtin_bit_cast(bf16x8, __builtin_shufflevector(lo, hi, 0, 1, 2, 3, 4, 5, 6, 7));
}
DI bf16x8 ldsfrag(unsigned addr) { return *(const LAS bf16x8*)(uintptr_t)addr; }
DI float gelu_t(float u) {
    const float k1 = -2.3022081985f;
    float w = u * __builtin_fmaf(u * u, k1 * 0.044715f, k1);
    float e = __builtin_amdgcn_exp2f(w);
    return u * __builtin_amdgcn_rcpf(1.0f + e);
}

constexpr int K1_MISC = 9 * 16384;
constexpr int K1_SUM = K1_MISC;
constexpr int K1_CS = K1_MISC + 1024;
constexpr int K1_PRIV = K1_MISC + 1024 + 2048;
constexpr int K1_JUNK = K1_PRIV + 8 * 256;
constexpr int K1_LDS = K1_JUNK + 1024;
enum { EPI_Q = 0, EPI_K = 1, EPI_V = 2, EPI_Z1 = 3, EPI_Z2 = 4 };
DI f32x16 splat16(float v) { f32x16 r; for (int i = 0; i < 16; ++i) r[i] = v; return r; }

DI float dot2m(unsigned a, unsigned m1, unsigned m2, float c) {
    const unsigned b = (a & m1) | m2;
    return __builtin_amdgcn_fdot2_f32_bf16(__builtin_bit_cast(bf16x2, a), __builtin_bit_cast(bf16x2, b), c, false);
}

template <bool ORI2, bool GELU>
DI void k1_body(const char* __restrict__ x, const char* __restrict__ wb  , int rp, char* __restrict__ dst  ,
                unsigned dvoff, unsigned jstride, unsigned pstride, float oscale, char* __restrict__ ws, unsigned lds0) {
    constexpr int LDW = GELU ? 3584 : 3072, NP = GELU ? 3 : 4;
    const int tid = threadIdx.x, lane = tid & 63;
    const int wid = __builtin_amdgcn_readfirstlane(tid >> 6);
    const int wm = wid & 3, wn = wid >> 2;
    const int r = lane & 31, h = lane >> 5;
    STAMP_DECL; STAMP(0); STAMPC(14);
    const char* xa = x + (size_t)rp * 131072 + wid * 16384;
    unsigned xvoff[2];
#pragma unroll
    for (int i = 0; i < 2; ++i) xvoff[i] = (i * 8 + (lane >> 3)) * 1024 + (((lane & 7) ^ ((4 * i + (lane >> 4)) & 7)) << 4);
    const char* wa = wb + (size_t)wid * 16 * LDW;
    const unsigned wvoff = ((lane >> 2) & 7) * LDW + (lane >> 5) * 64 + (lane & 3) * 16;
    const unsigned sdst = lds0 + wid * 2048;
#define ISSX(c, slot) do { glds16(xa + (c) * 128, xvoff[0], sdst + (slot) * 16384); glds16(xa + (c) * 128, xvoff[1], sdst + (slot) * 16384 + 1024); } while (0)
#define ISSW1(wp, st, i, sl) glds16((wp) + (size_t)((st) * 128 + (i) * 8) * LDW, wvoff, (sl) + (i) * 1024)
#define ISSW(wp, st, slot) do { ISSW1(wp, st, 0, sdst + (slot) * 16384); ISSW1(wp, st, 1, sdst + (slot) * 16384); } while (0)
    ISSX(0, 0); ISSX(1, 1); ISSW(wa, 0, 2);
    ISSX(2, 4); ISSX(3, 5); ISSW(wa, 1, 6);
    ISSX(4, 7); ISSX(5, 8); ISSW(wa, 2, 3);
    STAMP(1);
    unsigned xad[4], bad[4];
#pragma unroll
    for (int t = 0; t < 4; ++t) {
        xad[t] = lds0 + (wm * 32 + r) * 128 + (((2 * (t ^ wm) + h) ^ ((r >> 1) & 7)) << 4);
        bad[t] = lds0 + (2 * (t ^ wm) + h) * 1024 + wn * 512 + ((lane & 15) >> 2) * 64 + ((lane >> 4) & 1) * 32 + (lane & 3) * 8;
    }
    const unsigned selw = ((lane & 15) == ((lane >> 4) & 1)) ? 0x3f803f80u : 0u;
    u32x4 selu = {selw, selw, selw, selw};
    const bf16x8 sel = __builtin_bit_cast(bf16x8, selu);
    const unsigned m1 = wn ? 0u : 0xffffffffu, m2 = wn ? 0x3f803f80u : 0u;
    bf16x8 af[32];
    f32x16 acc[2];
    f32x4 csum = {0, 0, 0, 0};
    float st0 = 0.f, st1 = 0.f;
#define MM(bf, a, c) (ORI2 ? MFMA32(a, bf, c) : MFMA32(bf, a, c))
#define BFR(i, off) trfrag(bad[(i) & 3] + (off) + (((i) >> 2) & 1) * 8192, bad[(i) & 3] + (off) + (((i) >> 2) & 1) * 8192 + 256)
#define DOTS(i0) do { _Pragma("unroll") for (int i = i0; i < i0 + 2; ++i) { const u32x4 u = __builtin_bit_cast(u32x4, af[i]); \
            st0 = dot2m(u[0], m1, m2, st0); st1 = dot2m(u[1], m1, m2, st1); st0 = dot2m(u[2], m1, m2, st0); st1 = dot2m(u[3], m1, m2, st1); } \
        PIN(st0); PIN(st1); SB(); } while (0)
#define STAGE0(st, sx0, sx1, sb, G0, G1, G2) do { \
        bf16x8 bf[8]; \
        _Pragma("unroll") for (int i = 0; i < 8; ++i) { af[st * 8 + i] = ldsfrag(xad[i & 3] + (i < 4 ? sx0 : sx1) * 16384); bf[i] = BFR(i, sb * 16384); } \
        SB(); G0; SB(); \
        _Pragma("unroll") for (int i = 0; i < 8; ++i) { \
            if (st == 0 && i < 2) acc[i & 1] = MM(bf[i], af[i], splat16(0.f)); else acc[i & 1] = MM(bf[i], af[st * 8 + i], acc[i & 1]); \
            if ((i & 3) == 0) csum = MFMA16(sel, bf[i], csum); \
            if (i == 4) { PIN(acc[0]); PIN(acc[1]); SB(); G1; SB(); } } \
        PIN(acc[0]); PIN(acc[1]); PIN(csum); SB(); \
        DOTS(st * 8); DOTS(st * 8 + 2); G2; SB(); DOTS(st * 8 + 4); DOTS(st * 8 + 6); \
    } while (0)
#define BAR(n) asm volatile("s_waitcnt vmcnt(" #n ") lgkmcnt(0)\n\ts_barrier" ::: "memory")
    const char* w1 = wa + 128;
    BAR(12); STAMP(2);
    STAGE0(0, 0, 1, 2, {}, {}, {});
    BAR(6); STAMP(3);
    STAGE0(1, 4, 5, 6, ISSX(6, 0), ISSX(7, 1), ISSW(wa, 3, 2));
    BAR(6); STAMP(4);
    STAGE0(2, 7, 8, 3, ISSW(w1, 0, 4), ISSW(w1, 1, 5), ISSW(w1, 2, 6));
    BAR(6); STAMP(5);
    STAGE0(3, 0, 1, 2, ISSW(w1, 3, 7), {}, {});
    const unsigned csw = lds0 + K1_CS + wn * 512 + (lane & 15) * 16 + wm * 4;
    const unsigned csr = lds0 + K1_CS + wn * 512 + r * 16;
    const unsigned priv = lds0 + K1_PRIV + wid * 256;
#define CSWRITE(par) do { if (lane < 16) { *(LAS float*)(uintptr_t)(csw + (par)) = csum[0]; *(LAS float*)(uintptr_t)(csw + (par) + 256) = csum[1]; } } while (0)
    {
        float t = st0 + st1;
        auto rr = __builtin_amdgcn_permlane32_swap(__float_as_uint(t), __float_as_uint(t), false, false);
        t = __uint_as_float(rr[0]) + __uint_as_float(rr[1]);
        *(LAS float*)(uintptr_t)(lds0 + K1_SUM + wn * 512 + (wm * 32 + r) * 4) = t;
        CSWRITE(0);
    }
    BAR(0); STAMP(6);
    float rs, nm;
    {
        const float ss = *(LAS float*)(uintptr_t)(lds0 + K1_SUM + (wm * 32 + r) * 4), sm = *(LAS float*)(uintptr_t)(lds0 + K1_SUM + 512 + (wm * 32 + r) * 4);
        const float mean = sm * (1.0f / 512.0f);
        const float var = fmaxf(ss * (1.0f / 512.0f) - mean * mean, 0.0f);
        rs = __builtin_amdgcn_rsqf(var + 1e-5f) * oscale;
        nm = -mean * rs;
        if (ORI2) *(LAS float*)(uintptr_t)(priv + h * 128 + r * 4) = h ? nm : rs;
    }
    f32x4 ea, eb; float cs, ylo; unsigned pk[4];
#define EPI_BEGIN(par) do { f32x4 c4 = *(LAS f32x4*)(uintptr_t)(csr + (par)); cs = (c4[0] + c4[1]) + (c4[2] + c4[3]); \
        if (!ORI2) *(LAS float*)(uintptr_t)(priv + r * 4) = cs; } while (0)
#define EPI_LOAD(q) do { ea = *(LAS f32x4*)(uintptr_t)(priv + (q) * 32 + h * 16); if (ORI2) eb = *(LAS f32x4*)(uintptr_t)(priv + 128 + (q) * 32 + h * 16); } while (0)
#define EPI_ELEM(PV, e) do { float y_ = ORI2 ? __builtin_fmaf(eb[(e) & 3], cs, PV[e] * ea[(e) & 3]) : __builtin_fmaf(nm, ea[(e) & 3], PV[e] * rs); \
        if (GELU) y_ = gelu_t(y_); if ((e) & 1) { pk[((e) >> 1) & 3] = pk2(ylo, y_); PIN(pk[((e) >> 1) & 3]); } else { ylo = y_; PIN(ylo); } } while (0)
#define EPI_STORE(j) do { auto s0_ = __builtin_amdgcn_permlane32_swap(pk[0], pk[2], false, false); auto s1_ = __builtin_amdgcn_permlane32_swap(pk[1], pk[3], false, false); \
        u32x4 v_ = {s0_[0], s1_[0], s0_[1], s1_[1]}; GST16(dvoff, v_, dst + (j) * jstride); } while (0)
#pragma unroll
    for (int t = 0; t < 4; ++t) bad[t] += 65536;
    unsigned par = 0, nslot = sdst;
    const char* wnx = wa + 256;
    constexpr int PD = 4;
#pragma unroll 1
    for (int p = 1; p < NP; ++p) {
        f32x16 pv = acc[0] + acc[1]; PIN(pv);
        const bool more = p + 1 < NP;
        bf16x8 bf[PD + 1];
#pragma unroll
        for (int i = 0; i < PD; ++i) bf[i] = BFR(i, 0);
        SB();
#pragma unroll
        for (int i = 0; i < 32; ++i) {
            if (i + PD < 32) bf[(i + PD) % (PD + 1)] = BFR(i + PD, ((i + PD) >> 3) * 16384);
            if (i < 2) acc[i & 1] = MM(bf[i], af[i], splat16(0.f)); else acc[i & 1] = MM(bf[i % (PD + 1)], af[i], acc[i & 1]);
            PIN(acc[i & 1]);
            if ((i & 3) == 0) { if (i == 0) { f32x4 z4 = {0, 0, 0, 0}; csum = MFMA16(sel, bf[0], z4); } else csum = MFMA16(sel, bf[i % (PD + 1)], csum); PIN(csum); }
            if (i < 16 && (i & 1) && more) ISSW1(wnx, i >> 2, (i >> 1) & 1, nslot + (i >> 2) * 16384);
            if (i == 0) EPI_BEGIN(par);
            if (i >= 3) {
                const int u = i - 3;
                if (u == 14 || u == 28) EPI_STORE(u / 14 - 1);
                if (u % 7 == 0 && u < 28) EPI_LOAD(u / 7);
                if (u % 7 >= 3) EPI_ELEM(pv, (u / 7) * 4 + (u % 7) - 3);
            }
            SB();
        }
        par ^= 1024;
        CSWRITE(par);
        BAR(2);
        if (p == 1) STAMP(7); else if (p == 2) STAMP(8); else STAMP(9);
#pragma unroll
        for (int t = 0; t < 4; ++t) bad[t] ^= 65536;
        nslot ^= 65536; wnx += 128; dst += pstride;
    }
    EPI_BEGIN(par);
    const f32x16 pl = acc[0] + acc[1];
#pragma unroll
    for (int q = 0; q < 4; ++q) {
        EPI_LOAD(q);
#pragma unroll
        for (int e = 0; e < 4; ++e) EPI_ELEM(pl, q * 4 + e);
        if (q & 1) EPI_STORE(q >> 1);
    }
    STAMP(12); WAITV(0); STAMP(13);
    STAMP_FLUSH(0, ws);
}

__global__ __launch_bounds__(512, 2) void k1_kernel(const char* __restrict__ x, const char* __restrict__ wqkv, const char* __restrict__ win, char* __restrict__ ws,
                                                    const char* pf_wsp, const char* pf_out, const char* pf_wo, const char* pf_wout) {
    extern __shared__ __attribute__((aligned(16))) char smem[];
    const unsigned lds0 = (unsigned)(uintptr_t)(LAS char*)smem;
    const int b = blockIdx.x, xc = b & 7, rr = b >> 3;
    const int rp = (xc & 1) * 8 + (rr & 7), ct = (xc >> 1) * 4 + (rr >> 3);
    const int lane = threadIdx.x & 63, wid = __builtin_amdgcn_readfirstlane(threadIdx.x >> 6);
    const int rt = rp * 4 + (wid & 3), wn = wid >> 2;
    if (threadIdx.x < FLAG_REP) *(unsigned*)(ws + WS_FLAG + threadIdx.x * FLAG_STRIDE + b * 4) = 0u;
    if (ct < 4) {
        const int head0 = (ct & 1) * 16 + wn * 2;
        k1_body<false, false>(x, wqkv + ct * 512, rp, ws + (ct < 2 ? WS_Q : WS_K) + ((size_t)head0 * 64 + rt) * 1024, lane * 16, 65536, 4 * 65536,
                              ct < 2 ? 0.25f * 1.4426950408889634f : 1.0f, ws, lds0);
    } else if (ct < 6) {
        const int head0 = (ct & 1) * 16 + wn * 2;
        k1_body<true, false>(x, wqkv + ct * 512, rp, ws + WS_V + ((size_t)head0 * 64 + rt) * 1024, ((lane >> 4) & 1) * 65536 + (lane >> 5) * 256 + (lane & 15) * 16, 512, 4 * 65536,
                             1.0f, ws, lds0);
    } else if (ct < 11) {
        const int c0 = min((ct - 6) * 192, 704);
        k1_body<false, true>(x, win + c0 * 2, rp, ws + WS_Z1 + ((size_t)rt * 56 + (c0 >> 4) + wn * 2) * 1024, lane * 16, 1024, 4096, 1.0f, ws, lds0);
    } else {
        const int c0 = min((ct - 11) * 192, 704);
        k1_body<true, true>(x, win + (FH + c0) * 2, rp, ws + WS_Z2 + ((size_t)rt * 4 * FH + c0 + wn * 32) * 16, (lane >> 5) * (FH * 16) + (lane & 31) * 16, 2 * FH * 16, 1024,
                            1.0f, ws, lds0);
    }
}

constexpr int K2_K = 0, K2_V = 62 * 1024, K2_W = 126 * 1024, K2_ONES = K2_W + 32768, K2_MISC = K2_ONES + 1024, K2_LDS = K2_MISC + 1024;
enum { SY_KVA = 0, SY_KVB = 4, SY_KVC = 8, SY_P24 = 12, SY_LN = 16, SY_LNST = 20, SY_RED = 24, SY_FLAG = 28, SY_P30 = 32, SY_CMB = 36 };
constexpr int SPIN_MAX = 1 << 20;
constexpr unsigned FLAG_TAG = 0x5eed0001u;
constexpr float REF_OFF = 64.0f;

DI void lds_inc(unsigned addr) { __hip_atomic_fetch_add((LAS unsigned*)(uintptr_t)addr, 1u, __ATOMIC_RELAXED, __HIP_MEMORY_SCOPE_WORKGROUP); }
DI void lds_wait(unsigned addr, unsigned n) {
#pragma unroll 1
    for (int it = 0; it < SPIN_MAX && __builtin_amdgcn_readfirstlane(__hip_atomic_load((LAS unsigned*)(uintptr_t)addr, __ATOMIC_RELAXED, __HIP_MEMORY_SCOPE_WORKGROUP)) < n; ++it)
        __builtin_amdgcn_s_sleep(1);
    asm volatile("" ::: "memory");
}
DI bf16x16 lo8(const f32x16& x) { f32x8 t = __builtin_shufflevector(x, x, 0, 1, 2, 3, 4, 5, 6, 7); return __builtin_bit_cast(bf16x16, t); }
DI bf16x16 hi8(const f32x16& x) { f32x8 t = __builtin_shufflevector(x, x, 8, 9, 10, 11, 12, 13, 14, 15); return __builtin_bit_cast(bf16x16, t); }
#define SMF(a, b, c) __builtin_amdgcn_smfmac_f32_32x32x32_bf16(a, b, c, sidx, 0, 0)
#define GLD16S(dst, voff, sbase) asm volatile("global_load_dwordx4 %0, %1, %2 sc1" : "=v"(dst) : "v"(voff), "s"(sbase) : "memory")

DI float max16(const f32x16& s) {
    float a = fmaxf(fmaxf(s[0], s[1]), s[2]), b = fmaxf(fmaxf(s[3], s[4]), s[5]);
    a = fmaxf(fmaxf(a, s[6]), s[7]); b = fmaxf(fmaxf(b, s[8]), s[9]);
    a = fmaxf(fmaxf(a, s[10]), s[11]); b = fmaxf(fmaxf(b, s[12]), s[13]);
    a = fmaxf(fmaxf(a, s[14]), s[15]);
    return fmaxf(a, b);
}
DI float xhalf_max(float m) {
    auto rr = __builtin_amdgcn_permlane32_swap(__float_as_uint(m), __float_as_uint(m), false, false);
    return fmaxf(__uint_as_float(rr[0]), __uint_as_float(rr[1]));
}

DI void att_exact(const char* kg, const char* vg, bf16x8 q, f32x16& o_out) {
    const int lane = threadIdx.x & 63;
    const int sidx = (int)0xDDDDDDDDu;
    f32x16 z = splat16(0.f);
    float m = -INFINITY;
    for (int t = 0; t < 64; ++t) {
        bf16x8 kf = *(const bf16x8*)(kg + t * 1024 + lane * 16);
        f32x16 s = MFMA32(kf, q, z);
        m = fmaxf(m, max16(s));
    }
    m = xhalf_max(m);
    f32x16 c = splat16(-m), o = z;
    const bool isv = (lane & 16) == 0;
    u32x4 onesu = {0x3f803f80u, 0x3f803f80u, 0x3f803f80u, 0x3f803f80u};
    const unsigned vo = (lane >> 5) * 256 + (lane & 15) * 16;
    for (int t = 0; t < 64; ++t) {
        bf16x8 kf = *(const bf16x8*)(kg + t * 1024 + lane * 16);
        u32x4 v0 = *(const u32x4*)(vg + t * 1024 + vo), v1 = *(const u32x4*)(vg + t * 1024 + 512 + vo);
        if (!isv) { v0 = onesu; v1 = onesu; }
        f32x16 s = MFMA32(kf, q, c);
#pragma unroll
        for (int i = 0; i < 16; ++i) s[i] = __builtin_amdgcn_exp2f(s[i]);
        o = SMF(__builtin_bit_cast(bf16x8, v0), lo8(s), o);
        o = SMF(__builtin_bit_cast(bf16x8, v1), hi8(s), o);
    }
    o_out = o;
}

DI void att_store(const f32x16& o, char* dst, int lane) {
    const float rl = __builtin_amdgcn_rcpf(o[8]);
    unsigned p0 = pk2(o[0] * rl, o[1] * rl), p1 = pk2(o[2] * rl, o[3] * rl), p2 = pk2(o[4] * rl, o[5] * rl), p3 = pk2(o[6] * rl, o[7] * rl);
    auto s0 = __builtin_amdgcn_permlane32_swap(p0, p2, false, false);
    auto s1 = __builtin_amdgcn_permlane32_swap(p1, p3, false, false);
    u32x4 v = {s0[0], s1[0], s0[1], s1[1]};
    *(u32x4*)(dst + lane * 16) = v;
}
DI bool att_bad(const f32x16& o) {
    float a = fmaxf(fmaxf(fabsf(o[0]), fabsf(o[1])), fmaxf(fabsf(o[2]), fabsf(o[3])));
    float b = fmaxf(fmaxf(fabsf(o[4]), fabsf(o[5])), fmaxf(fabsf(o[6]), fabsf(o[7])));
    float s = (o[0] + o[1] + o[2] + o[3]) + (o[4] + o[5] + o[6] + o[7]);
    bool ok = (o[8] < 0x1p120f) && (o[8] > 0x1p-80f) && (fmaxf(a, b) < 0x1p124f) && (s == s);
    return __any(!ok);
}

DI void att_role(int head, int qb, int w, char* __restrict__ ws, unsigned lds0) {
    const int lane = threadIdx.x & 63;
    const int qh = w & 1, kh = w >> 1;
    const char* kg = ws + WS_K + (size_t)head * 65536;
    const char* vg = ws + WS_V + (size_t)head * 65536;
    const char* qg = ws + WS_Q + (size_t)head * 65536 + (size_t)(qb * 8 + qh * 4) * 1024;
    const unsigned l16 = lane * 16;
    const unsigned vo = (lane >> 5) * 256 + (lane & 15) * 16;
    u32x4 qr[4], k0, k1;
    STAMP_DECL; STAMP(0); STAMPC(14);
    const unsigned qo = l16 + kh * 2048, qo2 = l16 + (kh ^ 1) * 2048;
    GLD16(k0, l16, kg + kh * 32768, 0); GLD16(k1, l16, kg + (kh ^ 1) * 32768, 0);
    GLD16(qr[0], qo, qg, 0); GLD16(qr[1], qo, qg, 1024); GLD16(qr[2], qo2, qg, 0); GLD16(qr[3], qo2, qg, 1024);
    const char* ksrc = (qh ? vg : kg + 1024) + kh * 32768;
    const unsigned kdst = lds0 + (qh ? K2_V + kh * 32768 : K2_K + kh * 31744);
#pragma unroll
    for (int i = 0; i < 5; ++i) glds16(ksrc + i * 1024, l16, kdst + i * 1024);
    *(LAS unsigned*)(uintptr_t)(lds0 + K2_ONES + threadIdx.x * 4) = ((threadIdx.x >> 2) & 11) == 0 ? 0x3f803f80u : 0u;
    asm volatile("s_waitcnt lgkmcnt(0)\n\ts_barrier" ::: "memory");
#pragma unroll
    for (int i = 5; i < 13; ++i) glds16(ksrc + i * 1024, l16, kdst + i * 1024);
    STAMP(1);
    asm volatile("s_waitcnt vmcnt(13)" : "+v"(qr[0]), "+v"(qr[1]), "+v"(qr[2]), "+v"(qr[3]), "+v"(k0), "+v"(k1) :: "memory");
    STAMP(2);
#define QF(i) __builtin_bit_cast(bf16x8, qr[i])
    int sidx = (int)0xDDDDDDDDu; PIN(sidx);
    f32x16 cc[4];
#pragma unroll
    for (int i = 0; i < 4; i += 2) {
        f32x16 z = splat16(0.f);
        f32x16 s0 = MFMA32(__builtin_bit_cast(bf16x8, k0), QF(i), z), s1 = MFMA32(__builtin_bit_cast(bf16x8, k1), QF(i), z);
        f32x16 s2 = MFMA32(__builtin_bit_cast(bf16x8, k0), QF(i + 1), z), s3 = MFMA32(__builtin_bit_cast(bf16x8, k1), QF(i + 1), z);
        PIN(s0); PIN(s1); PIN(s2); PIN(s3);
        float mA = xhalf_max(fmaxf(max16(s0), max16(s1))), mB = xhalf_max(fmaxf(max16(s2), max16(s3)));
        cc[i] = splat16(-(mA + REF_OFF)); cc[i + 1] = splat16(-(mB + REF_OFF));
        PIN(cc[i]); PIN(cc[i + 1]);
    }
    bf16x8 kf = __builtin_bit_cast(bf16x8, k0);
    f32x16 oo[4] = {splat16(0.f), splat16(0.f), splat16(0.f), splat16(0.f)}, sA, sB = splat16(0.f);
    sA = MFMA32(kf, QF(0), cc[0]);
    WAITV(8); lds_inc(lds0 + K2_MISC + SY_KVA);
    unsigned kaddr = lds0 + K2_K + kh * 31744 + l16;
    unsigned vaddr = (lane & 16) ? (lds0 + K2_ONES + vo) : (lds0 + K2_V + kh * 32768 + vo);
    const unsigned vinc = (lane & 16) ? 0u : 1024u;
    lds_wait(lds0 + K2_MISC + SY_KVA, 4 * 64);
    bf16x8 v0 = ldsfrag(vaddr), v1 = v0;
    STAMP(3);
#define EXPS(X, a, b) do { _Pragma("unroll") for (int i_ = a; i_ < b; ++i_) X[i_] = __builtin_amdgcn_exp2f(X[i_]); PIN(X); SB(); } while (0)
#define HSTEP(X, Y, i, A1, A2, A3) do { \
        EXPS(X, 0, 2); \
        oo[(i + 3) & 3] = SMF(v1, hi8(Y), oo[(i + 3) & 3]); PIN(oo[(i + 3) & 3]); SB(); A1; SB(); \
        EXPS(X, 2, 5); \
        Y = MFMA32(kf, QF((i + 1) & 3), cc[(i + 1) & 3]); PIN(Y); SB(); A2; SB(); \
        EXPS(X, 5, 11); \
        oo[i] = SMF(v0, lo8(X), oo[i]); PIN(oo[i]); SB(); A3; SB(); \
        EXPS(X, 11, 16); \
    } while (0)
#pragma unroll 1
    for (int seg = 0; seg < 5; ++seg) {
        int n = 4;
        if (seg == 1) { n = 8; STAMP(4); WAITV(0); lds_inc(lds0 + K2_MISC + SY_KVB); lds_wait(lds0 + K2_MISC + SY_KVB, 4 * 64); STAMP(5); STAMPC(10); }
        else if (seg == 2) { n = 13; STAMP(6); STAMPC(11); lds_wait(lds0 + K2_MISC + SY_KVC, 4 * 64); STAMP(7); STAMPC(12); }
        else if (seg == 3) { n = 6; asm volatile("s_waitcnt lgkmcnt(0)" ::: "memory"); lds_inc(lds0 + K2_MISC + SY_P24); }
        else if (seg == 4) { n = 1; asm volatile("s_waitcnt lgkmcnt(0)" ::: "memory"); lds_inc(lds0 + K2_MISC + SY_P30); }
#pragma unroll 1
        for (int i = 0; i < n; ++i) {
            HSTEP(sA, sB, 0, { v1 = ldsfrag(vaddr + 512); vaddr += vinc; }, {}, {});
            HSTEP(sB, sA, 1, {}, {}, {});
            HSTEP(sA, sB, 2, {}, { kf = ldsfrag(kaddr); kaddr += 1024; }, {});
            HSTEP(sB, sA, 3, {}, {}, { v0 = ldsfrag(vaddr); });
        }
    }
    oo[3] = SMF(v1, hi8(sB), oo[3]);
    STAMP(8); STAMPC(13);
    {
        auto cbase = [&](int ww) { return lds0 + ((ww & 1) ? K2_V + (ww >> 1) * 32768 + 25 * 1024 : K2_K + (ww >> 1) * 31744 + 24 * 1024); };
        lds_wait(lds0 + K2_MISC + SY_P30, 4 * 64);
        const unsigned wa = cbase(w), ra = cbase(w ^ 2);
#pragma unroll
        for (int i = 0; i < 2; ++i) {
            f32x4 a = {oo[2 + i][0], oo[2 + i][1], oo[2 + i][2], oo[2 + i][3]}, b = {oo[2 + i][4], oo[2 + i][5], oo[2 + i][6], oo[2 + i][7]};
            *(LAS f32x4*)(uintptr_t)(wa + i * 2048 + l16) = a;
            *(LAS f32x4*)(uintptr_t)(wa + i * 2048 + 1024 + l16) = b;
            *(LAS float*)(uintptr_t)(wa + 4096 + i * 256 + lane * 4) = oo[2 + i][8];
        }
        asm volatile("s_waitcnt lgkmcnt(0)" ::: "memory");
        lds_inc(lds0 + K2_MISC + SY_CMB + qh * 4);
        lds_wait(lds0 + K2_MISC + SY_CMB + qh * 4, 2 * 64);
#pragma unroll
        for (int i = 0; i < 2; ++i) {
            f32x4 a = *(LAS f32x4*)(uintptr_t)(ra + i * 2048 + l16), b = *(LAS f32x4*)(uintptr_t)(ra + i * 2048 + 1024 + l16);
            float l = *(LAS float*)(uintptr_t)(ra + 4096 + i * 256 + lane * 4);
#pragma unroll
            for (int e = 0; e < 4; ++e) { oo[i][e] += a[e]; oo[i][4 + e] += b[e]; }
            oo[i][8] += l;
        }
    }
    if (__builtin_expect(att_bad(oo[0]), 0)) att_exact(kg, vg, QF(0), oo[0]);
    if (__builtin_expect(att_bad(oo[1]), 0)) att_exact(kg, vg, QF(1), oo[1]);
    char* dst = ws + WS_CAT + ((size_t)(qb * 8 + qh * 4 + kh * 2) * CAT_KS + head) * 1024;
    att_store(oo[0], dst, lane);
    att_store(oo[1], dst + CAT_KS * 1024, lane);
    STAMP(9);
    STAMP_FLUSH(1, ws);
}

template <int CTRL> DI float dppf(float x) { return __builtin_bit_cast(float, __builtin_amdgcn_mov_dpp(__builtin_bit_cast(int, x), CTRL, 0xf, 0xf, true)); }
DI float fold32(float a, float b) {
    auto r = __builtin_amdgcn_permlane32_swap(__float_as_uint(a), __float_as_uint(b), false, false);
    return __uint_as_float(r[0]) + __uint_as_float(r[1]);
}
DI float fold16(float a, float b) {
    auto r = __builtin_amdgcn_permlane16_swap(__float_as_uint(a), __float_as_uint(b), false, false);
    return __uint_as_float(r[0]) + __uint_as_float(r[1]);
}
DI float rowsum16(float v) { v += dppf<0xB1>(v); v += dppf<0x4E>(v); v += dppf<0x124>(v); v += dppf<0x128>(v); return v; }

DI void spg_role(int b, int head, int w, const char* __restrict__ wsp, const __hip_bfloat16* __restrict__ bsp, char* __restrict__ ws, unsigned lds0,
                 const char* pf_x, const char* pf_wo, const char* pf_wout) {
    const int lane = threadIdx.x & 63;
    const int xc = b & 7, rr = b >> 3;
    const int tt = xc * 4 + (rr & 3), ft = rr >> 2;
    const bool active = ft < 7;
    STAMP_DECL; STAMP(0); STAMPC(14);
    __builtin_amdgcn_s_setprio(2);
    const unsigned wring = lds0 + K2_W + w * 8192;
    const char* wbase = wsp + (size_t)tt * 64 * 4096 + w * 1024;
    unsigned wvoff[2];
#pragma unroll
    for (int p = 0; p < 2; ++p) wvoff[p] = (lane >> 3) * 4096 + (((lane & 7) ^ ((4 * p + (lane >> 4)) & 7)) << 4);
    const int rot = 0;
#define CROT(c) (((c) + rot) & 7)
#define ISSUEW(c, i) glds16(wbase + (size_t)(i) * 32768 + CROT(c) * 128, wvoff[(i) & 1], wring + (i) * 1024)
    {
        const char* src = ws + WS_Z2 + (size_t)b * FH * 16;
        char* dst = ws + WS_Z2N + (size_t)b * FH * 16;
        u32x4 zc[4], z0;
        const unsigned o0 = (w * 224 + lane) * 16;
        const unsigned o3 = (w * 224 + 192 + (lane & 31)) * 16;
        const unsigned zero = 0;
        GLD16(zc[0], o0, src, 0); GLD16(zc[1], o0, src, 1024); GLD16(zc[2], o0, src, 2048); GLD16(zc[3], o3, src, 0); GLD16(z0, zero, src, 0);
        *(LAS unsigned*)(uintptr_t)(lds0 + K2_MISC + (threadIdx.x - 256) * 4) = 0u;
        asm volatile("s_waitcnt lgkmcnt(0)\n\ts_barrier" ::: "memory");
        asm volatile("s_waitcnt vmcnt(0)" : "+v"(zc[0]), "+v"(zc[1]), "+v"(zc[2]), "+v"(zc[3]), "+v"(z0) :: "memory");
        STAMP(1);
        float sh[8], s1[8], s2[8];
#pragma unroll
        for (int e = 0; e < 4; ++e) { sh[2 * e] = bflo(z0[e]); sh[2 * e + 1] = bfhi(z0[e]); }
        const float m3 = lane < 32 ? 1.f : 0.f;
#pragma unroll
        for (int e = 0; e < 8; ++e) {
            float a = ((e & 1) ? bfhi(zc[0][e >> 1]) : bflo(zc[0][e >> 1])) - sh[e];
            float bb = ((e & 1) ? bfhi(zc[1][e >> 1]) : bflo(zc[1][e >> 1])) - sh[e];
            float c = ((e & 1) ? bfhi(zc[2][e >> 1]) : bflo(zc[2][e >> 1])) - sh[e];
            float d = (((e & 1) ? bfhi(zc[3][e >> 1]) : bflo(zc[3][e >> 1])) - sh[e]) * m3;
            s1[e] = (a + bb) + (c + d);
            s2[e] = (a * a + bb * bb) + (c * c + d * d);
        }
        f32x4 q;
#pragma unroll
        for (int i = 0; i < 4; ++i) q[i] = rowsum16(fold16(fold32(s1[i], s2[i]), fold32(s1[i + 4], s2[i + 4])));
        LAS float* part = (LAS float*)(uintptr_t)(lds0 + K2_MISC + 256);
        *(LAS f32x4*)(part + w * 16 + (lane >> 4) * 4) = q;
        asm volatile("s_waitcnt lgkmcnt(0)" ::: "memory");
        lds_inc(lds0 + K2_MISC + SY_LN);
        lds_wait(lds0 + K2_MISC + SY_LN, 4 * 64);
        STAMP(2);
        float rs[8], nm[8];
        {
            f32x4 t[4];
#pragma unroll
            for (int i = 0; i < 4; ++i) t[i] = (*(LAS f32x4*)(part + i * 4) + *(LAS f32x4*)(part + 16 + i * 4)) + (*(LAS f32x4*)(part + 32 + i * 4) + *(LAS f32x4*)(part + 48 + i * 4));
#pragma unroll
            for (int e = 0; e < 8; ++e) {
                float mean = t[e >> 2][e & 3] * (1.0f / FH);
                float var = fmaxf(t[2 + (e >> 2)][e & 3] * (1.0f / FH) - mean * mean, 0.f);
                rs[e] = __builtin_amdgcn_rsqf(var + 1e-5f);
                nm[e] = -(mean + sh[e]) * rs[e];
            }
        }
#pragma unroll
        for (int j = 0; j < 4; ++j) {
            u32x4 v;
#pragma unroll
            for (int e = 0; e < 4; ++e) v[e] = pk2(__builtin_fmaf(bflo(zc[j][e]), rs[2 * e], nm[2 * e]), __builtin_fmaf(bfhi(zc[j][e]), rs[2 * e + 1], nm[2 * e + 1]));
            const unsigned o = j < 3 ? o0 + j * 1024 : o3;
            asm volatile("global_store_dwordx4 %0, %1, %2 sc1\n\ts_nop 1" :: "v"(o), "v"(v), "s"(dst) : "memory");
        }
        STAMP(3);
        WAITV(0);
        STAMP(4);
        unsigned old = 0;
        if (lane == 0) old = __hip_atomic_fetch_add((LAS unsigned*)(uintptr_t)(lds0 + K2_MISC + SY_LNST), 1u, __ATOMIC_RELAXED, __HIP_MEMORY_SCOPE_WORKGROUP);
        old = __builtin_amdgcn_readfirstlane(old);
        if (old == 3 && lane < FLAG_REP) __hip_atomic_store((unsigned*)(ws + WS_FLAG + lane * FLAG_STRIDE) + b, FLAG_TAG, __ATOMIC_RELAXED, __HIP_MEMORY_SCOPE_AGENT);
    }
    {
        const char* src = ws + ((w & 1) ? WS_V : WS_K) + (size_t)head * 65536 + (w >> 1) * 32768;
        const unsigned dst = lds0 + ((w & 1) ? K2_V + (w >> 1) * 32768 : K2_K + (w >> 1) * 31744 - 1024);
#pragma unroll 1
        for (int t = 13; t < 32; ++t) glds16(src + t * 1024, lane * 16, dst + t * 1024);
#pragma unroll
        for (int i = 0; i < 8; ++i) ISSUEW(0, i);
        WAITV(8);
        lds_inc(lds0 + K2_MISC + SY_KVC);
    }
    if (!active) {
        lds_wait(lds0 + K2_MISC + SY_P24, 4 * 64);
        const int id = (rr & 3) * 4 + w;
        const unsigned lo = lane * 16;
        u32x4 t;
#pragma unroll 4
        for (int i = 0; i < 32; ++i) GLD16(t, lo, pf_wo + (size_t)(id * 32 + i) * 1024, 0);
#pragma unroll 4
        for (int i = 0; i < 56; ++i) { GLD16(t, lo, pf_wout + (size_t)(id * 56 + i) * 1024, 0); if ((i & 3) == 3) WAITV(40); }
#pragma unroll 4
        for (int i = 0; i < 16; ++i) { GLD16(t, lo, pf_x + (size_t)(xc * 256 + id * 16 + i) * 1024, 0); if ((i & 3) == 3) WAITV(40); }
        WAITV(0);
        STAMP_FLUSH(1, ws); return;
    }
    const char* zb = ws + WS_Z2N + ((size_t)w * 64 * FH + ft * 128) * 16;
    unsigned zvoff[4];
#pragma unroll
    for (int j = 0; j < 4; ++j) { zvoff[j] = (lane >> 5) * (FH * 16) + (j ^ w) * 512 + (lane & 31) * 16; PIN(zvoff[j]); }
    u32x4 zf[4][4];
#define ZPTR(c, ks) (zb + (size_t)(CROT(c) * 4 + (ks)) * (2 * FH * 16))
    f32x16 acc[4][2];
#pragma unroll
    for (int j = 0; j < 4; ++j) { acc[j][0] = splat16(0.f); acc[j][1] = splat16(0.f); PIN(acc[j][0]); PIN(acc[j][1]); }
    unsigned waddr[4];
#pragma unroll
    for (int ks = 0; ks < 4; ++ks) { waddr[ks] = wring + (lane & 31) * 128 + (((ks * 2 + (lane >> 5)) ^ ((lane >> 1) & 7)) << 4); PIN(waddr[ks]); }
    if (w == 0) {
        const char* fl = ws + WS_FLAG + (size_t)(b >> 3) * FLAG_STRIDE;
        const unsigned fo = lane * 16;
#pragma unroll 1
        for (int it = 0; it < SPIN_MAX; ++it) {
            u32x4 f;
            asm volatile("global_load_dwordx4 %0, %1, %2 sc1\n\ts_waitcnt vmcnt(0)" : "=&v"(f) : "v"(fo), "s"(fl) : "memory");
            if (__all(f[0] == FLAG_TAG && f[1] == FLAG_TAG && f[2] == FLAG_TAG && f[3] == FLAG_TAG)) break;
            __builtin_amdgcn_s_sleep(4);
        }
        lds_inc(lds0 + K2_MISC + SY_FLAG);
    } else lds_wait(lds0 + K2_MISC + SY_FLAG, 64);
    STAMP(5);
#pragma unroll
    for (int ks = 0; ks < 4; ++ks)
#pragma unroll
        for (int j = 0; j < 4; ++j) GLD16S(zf[ks][j], zvoff[j], ZPTR(0, ks));
    __builtin_amdgcn_s_setprio(0);
#define ZWAIT(n, ks) asm volatile("s_waitcnt vmcnt(" #n ")" : "+v"(zf[ks][0]), "+v"(zf[ks][1]), "+v"(zf[ks][2]), "+v"(zf[ks][3]) :: "memory")
#define KSTEP(ks, MORE) do { _Pragma("unroll") for (int j_ = 0; j_ < 4; ++j_) { \
        acc[j_][0] = MFMA32(__builtin_bit_cast(bf16x8, zf[ks][j_]), wf[0][ks], acc[j_][0]); PIN(acc[j_][0]); SB(); \
        if (MORE && ks == 0) ISSUEW(c + 1, 2 * j_); \
        acc[j_][1] = MFMA32(__builtin_bit_cast(bf16x8, zf[ks][j_]), wf[1][ks], acc[j_][1]); PIN(acc[j_][1]); SB(); \
        if (MORE && ks == 0) ISSUEW(c + 1, 2 * j_ + 1); \
        if (MORE) GLD16S(zf[ks][j_], zvoff[j_], ZPTR(c + 1, ks)); } } while (0)
#define WREAD() _Pragma("unroll") for (int ks = 0; ks < 4; ++ks) { wf[0][ks] = ldsfrag(waddr[ks]); wf[1][ks] = ldsfrag(waddr[ks] + 4096); } \
        asm volatile("s_waitcnt lgkmcnt(0)" : "+v"(wf[0][0]), "+v"(wf[0][1]), "+v"(wf[0][2]), "+v"(wf[0][3]), "+v"(wf[1][0]), "+v"(wf[1][1]), "+v"(wf[1][2]), "+v"(wf[1][3]) :: "memory")
#pragma unroll 1
    for (int c = 0; c < 7; ++c) {
        bf16x8 wf[2][4];
        ZWAIT(12, 0);
        WREAD();
        KSTEP(0, true);
        ZWAIT(20, 1); KSTEP(1, true);
        ZWAIT(20, 2); KSTEP(2, true);
        ZWAIT(20, 3); KSTEP(3, true);
    }
    const int fs0 = ft * 8 + w * 2;
    const char* z1p = ws + WS_Z1 + ((size_t)tt * 2 * 56 + fs0) * 1024;
    const char* bp = (const char*)bsp + tt * 128;
    const unsigned l16 = lane * 16, bo = (lane & 31) * 2;
    unsigned braw[2];
    {
        const int c = 7; (void)c;
        bf16x8 wf[2][4];
        ZWAIT(12, 0);
        WREAD();
        KSTEP(0, false);
        GLD16(zf[0][0], l16, z1p, 0); GLD16(zf[0][1], l16, z1p, 1024); GLD16(zf[0][2], l16, z1p + 56 * 1024, 0); GLD16(zf[0][3], l16, z1p + 56 * 1024, 1024);
        asm volatile("global_load_ushort %0, %1, %2" : "=v"(braw[0]) : "v"(bo), "s"(bp) : "memory");
        asm volatile("global_load_ushort %0, %1, %2 offset:64" : "=v"(braw[1]) : "v"(bo), "s"(bp) : "memory");
        ZWAIT(14, 1); KSTEP(1, false);
        ZWAIT(10, 2); KSTEP(2, false);
        ZWAIT(6, 3); KSTEP(3, false);
    }
    STAMP(6);
    __builtin_amdgcn_s_setprio(2);
    lds_wait(lds0 + K2_MISC + SY_P24, 4 * 64);
    STAMP(7);
    auto blk = [&](int dstw, int j) { return lds0 + ((dstw & 1) ? K2_V + (dstw >> 1) * 32768 : K2_K + (dstw >> 1) * 31744) + (j - 1) * 8192 + lane * 16; };
#pragma unroll
    for (int j = 1; j < 4; ++j) {
        const unsigned a = blk(j ^ w, j);
#pragma unroll
        for (int t = 0; t < 2; ++t)
#pragma unroll
            for (int q = 0; q < 4; ++q) {
                f32x4 v = {acc[j][t][4 * q], acc[j][t][4 * q + 1], acc[j][t][4 * q + 2], acc[j][t][4 * q + 3]};
                *(LAS f32x4*)(uintptr_t)(a + (t * 4 + q) * 1024) = v;
            }
    }
    asm volatile("s_waitcnt lgkmcnt(0)" ::: "memory");
    lds_inc(lds0 + K2_MISC + SY_RED);
    lds_wait(lds0 + K2_MISC + SY_RED, 4 * 64);
    STAMP(8);
#pragma unroll
    for (int j = 1; j < 4; ++j) {
        const unsigned a = blk(w, j);
#pragma unroll
        for (int t = 0; t < 2; ++t)
#pragma unroll
            for (int q = 0; q < 4; ++q) {
                f32x4 v = *(LAS f32x4*)(uintptr_t)(a + (t * 4 + q) * 1024);
#pragma unroll
                for (int e = 0; e < 4; ++e) acc[0][t][4 * q + e] += v[e];
            }
    }
    asm volatile("s_waitcnt vmcnt(0)" : "+v"(zf[0][0]), "+v"(zf[0][1]), "+v"(zf[0][2]), "+v"(zf[0][3]), "+v"(braw[0]), "+v"(braw[1]) :: "memory");
    const float bias[2] = {bflo(braw[0]), bflo(braw[1])};
#pragma unroll
    for (int t = 0; t < 2; ++t)
#pragma unroll
        for (int s = 0; s < 2; ++s) {
            auto a0 = __builtin_amdgcn_permlane32_swap(zf[0][2 * t + s][0], zf[0][2 * t + s][2], false, false);
            auto a1 = __builtin_amdgcn_permlane32_swap(zf[0][2 * t + s][1], zf[0][2 * t + s][3], false, false);
            const unsigned zc[4] = {a0[0], a1[0], a0[1], a1[1]};
            unsigned p[4];
#pragma unroll
            for (int e = 0; e < 4; ++e)
                p[e] = pk2(bflo(zc[e]) * (acc[0][t][8 * s + 2 * e] + bias[t]), bfhi(zc[e]) * (acc[0][t][8 * s + 2 * e + 1] + bias[t]));
            auto s0 = __builtin_amdgcn_permlane32_swap(p[0], p[2], false, false);
            auto s1 = __builtin_amdgcn_permlane32_swap(p[1], p[3], false, false);
            u32x4 v = {s0[0], s1[0], s0[1], s1[1]};
            *(u32x4*)(ws + WS_CAT + ((size_t)(tt * 2 + t) * CAT_KS + 32 + fs0 + s) * 1024 + lane * 16) = v;
        }
    STAMP(9);
    STAMP_FLUSH(1, ws);
}

__global__ __launch_bounds__(512, 2) void k2_kernel(const char* __restrict__ wsp, const __hip_bfloat16* __restrict__ bsp, char* __restrict__ ws,
                                                    const char* pf_x, const char* pf_wo, const char* pf_wout) {
    extern __shared__ __attribute__((aligned(16))) char smem[];
    const unsigned lds0 = (unsigned)(uintptr_t)(LAS char*)smem;
    const int wid = __builtin_amdgcn_readfirstlane(threadIdx.x >> 6);
    const int b = blockIdx.x;
    const int head = (b & 7) * 4 + (b >> 6);
    if (wid < 4) att_role(head, (b >> 3) & 7, wid, ws, lds0);
    else spg_role(b, head, wid - 4, wsp, bsp, ws, lds0, pf_x, pf_wo, pf_wout);
}

constexpr int K3_D = 6, K3_RING = K3_D * 4096, K3_LDS = 4 * K3_RING;

__global__ __launch_bounds__(256, 1) void k3_kernel(const char* __restrict__ x, const char* __restrict__ wo, const char* __restrict__ wout,
                                                    const char* __restrict__ ws, char* __restrict__ out) {
    extern __shared__ __attribute__((aligned(16))) char smem[];
    const unsigned lds0 = (unsigned)(uintptr_t)(LAS char*)smem;
    const int lane = threadIdx.x & 63;
    const int kq = __builtin_amdgcn_readfirstlane(threadIdx.x >> 6);
    const int b = blockIdx.x, xc = b & 7, rr = b >> 3;
    const int tt = xc * 4 + (rr & 3), nt = rr >> 2;
    STAMP_DECL; STAMP(0); STAMPC(14);
    const unsigned l16 = lane * 16;
    const unsigned bvoff = ((lane >> 2) & 7) * 1024 + (lane >> 5) * 64 + (lane & 3) * 16;
    const unsigned ring = lds0 + kq * K3_RING;
    const char* ab = ws + WS_CAT + ((size_t)tt * 2 * CAT_KS + kq * 22) * 1024;
    auto issue = [&](int s, unsigned dst) {
        const int k = (kq * 22 + s) * 16;
        const char* bs = (k < 512 ? wo + (size_t)k * 1024 : wout + (size_t)(k - 512) * 1024) + nt * 128;
        glds16(ab + s * 1024, l16, dst);
        glds16(bs, bvoff, dst + 2048);
        glds16(ab + (CAT_KS + s) * 1024, l16, dst + 1024);
        glds16(bs + 8192, bvoff, dst + 3072);
    };
    const size_t o = (size_t)(tt * 64 + (kq >> 1) * 32) * 1024 + (nt * 64 + (kq & 1) * 32) * 2;
    const unsigned xo = (lane & 31) * 1024 + (lane >> 5) * 16;
    u32x4 xv[2];
    GLD16(xv[0], xo, x + o, 0); GLD16(xv[1], xo, x + o, 32);
#pragma unroll 1
    for (int s = 0; s < K3_D; ++s) issue(s, ring + s * 4096);
    STAMP(1);
    f32x16 acc[2][2] = {{splat16(0.f), splat16(0.f)}, {splat16(0.f), splat16(0.f)}};
    const unsigned aa0 = ring + (kq >> 1) * 1024 + l16, aa1 = aa0 ^ 1024;
    const unsigned ba0 = ring + 2048 + (lane >> 5) * 1024 + (kq & 1) * 512 + ((lane & 15) >> 2) * 64 + ((lane >> 4) & 1) * 32 + (lane & 3) * 8, ba1 = ba0 ^ 512;
    bf16x8 fr[2][4];
#define K3READ(F, so) do { F[0] = ldsfrag(aa0 + (so)); F[1] = ldsfrag(aa1 + (so)); F[2] = trfrag(ba0 + (so), ba0 + (so) + 256); F[3] = trfrag(ba1 + (so), ba1 + (so) + 256); } while (0)
#define K3LW(F) asm volatile("s_waitcnt lgkmcnt(0)" : "+v"(F[0]), "+v"(F[1]), "+v"(F[2]), "+v"(F[3]) :: "memory")
#define K3MMA(F) do { acc[0][0] = MFMA32(F[2], F[0], acc[0][0]); acc[0][1] = MFMA32(F[3], F[0], acc[0][1]); acc[1][0] = MFMA32(F[2], F[1], acc[1][0]); acc[1][1] = MFMA32(F[3], F[1], acc[1][1]); } while (0)
#define K3NEXT(x) ((x) == (K3_D - 1) * 4096 ? 0u : (x) + 4096u)
    WAITV(20);
    K3READ(fr[0], 0);
    unsigned so = 0;
#pragma unroll 1
    for (int s = 0; s < 16; s += 2) {
        const unsigned s1 = K3NEXT(so), s2 = K3NEXT(s1);
        K3LW(fr[0]); WAITV(16); K3READ(fr[1], s1); SB(); issue(s + K3_D, ring + so); K3MMA(fr[0]); SB();
        K3LW(fr[1]); WAITV(16); K3READ(fr[0], s2); SB(); issue(s + 1 + K3_D, ring + s1); K3MMA(fr[1]); SB();
        so = s2;
    }
    STAMP(2);
#define K3TAIL(n, i) do { WAITV(n); K3READ(fr[(i + 1) & 1], ((i + 1) % K3_D) * 4096); SB(); K3MMA(fr[i & 1]); SB(); } while (0)
    K3TAIL(16, 16); K3TAIL(12, 17); K3TAIL(8, 18); K3TAIL(4, 19); K3TAIL(0, 20);
    K3MMA(fr[1]);
    STAMP(3);
#pragma unroll
    for (int j = 1; j < 4; ++j)
#pragma unroll
        for (int q = 0; q < 4; ++q) {
            const f32x16& t = acc[j >> 1][j & 1];
            f32x4 v = {t[4 * q], t[4 * q + 1], t[4 * q + 2], t[4 * q + 3]};
            *(LAS f32x4*)(uintptr_t)(ring + (j * 4 + q) * 1024 + l16) = v;
        }
    __syncthreads();
    STAMP(4);
#pragma unroll
    for (int j = 1; j < 4; ++j)
#pragma unroll
        for (int q = 0; q < 4; ++q) {
            f32x4 v = *(LAS f32x4*)(uintptr_t)(lds0 + (kq ^ j) * K3_RING + (j * 4 + q) * 1024 + l16);
#pragma unroll
            for (int e = 0; e < 4; ++e) acc[0][0][4 * q + e] += v[e];
        }
    asm volatile("" : "+v"(xv[0]), "+v"(xv[1]));
#pragma unroll
    for (int s = 0; s < 2; ++s) {
        auto a0 = __builtin_amdgcn_permlane32_swap(xv[s][0], xv[s][2], false, false);
        auto a1 = __builtin_amdgcn_permlane32_swap(xv[s][1], xv[s][3], false, false);
        const unsigned xc4[4] = {a0[0], a1[0], a0[1], a1[1]};
        unsigned p[4];
#pragma unroll
        for (int e = 0; e < 4; ++e) p[e] = pk2(bflo(xc4[e]) + acc[0][0][8 * s + 2 * e], bfhi(xc4[e]) + acc[0][0][8 * s + 2 * e + 1]);
        auto s0 = __builtin_amdgcn_permlane32_swap(p[0], p[2], false, false);
        auto s1 = __builtin_amdgcn_permlane32_swap(p[1], p[3], false, false);
        u32x4 v = {s0[0], s1[0], s0[1], s1[1]};
        *(u32x4*)(out + o + xo + s * 32) = v;
    }
    STAMP(5);
    STAMP_FLUSH(2, ws);
}

extern "C" void kernel_launch(void* const* d_in, const int* in_sizes, int n_in, void* d_out, int out_size, void* d_ws, size_t ws_size,
                              hipStream_t stream) {
    const char* x = (const char*)d_in[0];
    const char* wqkv = (const char*)d_in[1];
    const char* wo = (const char*)d_in[2];
    const char* win = (const char*)d_in[4];
    const char* wsp = (const char*)d_in[5];
    const __hip_bfloat16* bsp = (const __hip_bfloat16*)d_in[6];
    const char* wout = (const char*)d_in[7];
    char* ws = (char*)d_ws;
    (void)hipFuncSetAttribute((const void*)k1_kernel, hipFuncAttributeMaxDynamicSharedMemorySize, K1_LDS);
    (void)hipFuncSetAttribute((const void*)k2_kernel, hipFuncAttributeMaxDynamicSharedMemorySize, K2_LDS);
    (void)hipFuncSetAttribute((const void*)k3_kernel, hipFuncAttributeMaxDynamicSharedMemorySize, K3_LDS);
    hipLaunchKernelGGL(k1_kernel, dim3(256), dim3(512), K1_LDS, stream, x, wqkv, win, ws, wsp, (const char*)d_out, wo, wout);
    hipLaunchKernelGGL(k2_kernel, dim3(256), dim3(512), K2_LDS, stream, wsp, bsp, ws, x, wo, wout);
    hipLaunchKernelGGL(k3_kernel, dim3(256), dim3(256), K3_LDS, stream, x, wo, wout, (const char*)ws, (char*)d_out);
}
```

```cpp
#include <hip/hip_runtime.h>
#include <hip/hip_bf16.h>
#include <stdint.h>

typedef __attribute__((ext_vector_type(2))) __bf16 bf16x2;
typedef __attribute__((ext_vector_type(8))) __bf16 bf16x8;
typedef __attribute__((ext_vector_type(16))) __bf16 bf16x16;
typedef __attribute__((ext_vector_type(2))) float f32x2;
typedef __attribute__((ext_vector_type(4))) float f32x4;
typedef __attribute__((ext_vector_type(8))) float f32x8;
typedef __attribute__((ext_vector_type(16))) float f32x16;
typedef __attribute__((ext_vector_type(2))) unsigned u32x2;
typedef __attribute__((ext_vector_type(4))) unsigned u32x4;
typedef __attribute__((ext_vector_type(4))) short s16x4;
#define LAS __attribute__((address_space(3)))
#define DI __device__ __forceinline__
#define SB() __builtin_amdgcn_sched_barrier(0)
#define PIN(x) asm volatile("" : "+v"(x))
#define WAITV(n) asm volatile("s_waitcnt vmcnt(" #n ")" ::: "memory")
#define MFMA16(a, b, c) __builtin_amdgcn_mfma_f32_16x16x32_bf16(a, b, c, 0, 0, 0)
#define MFMA32(a, b, c) __builtin_amdgcn_mfma_f32_32x32x16_bf16(a, b, c, 0, 0, 0)

constexpr int S_ = 2048, D_ = 512, FH = 896;
constexpr size_t WS_Q = 0, WS_K = 2u << 20, WS_V = 4u << 20, WS_Z1 = 6u << 20, WS_Z2 = 10u << 20, WS_Z2N = 14u << 20,
                 WS_CAT = 18u << 20, WS_FLAG = 24u << 20;
constexpr int FLAG_REP = 32, FLAG_STRIDE = 4352;
constexpr size_t WS_REC = WS_FLAG + (256u << 10);
constexpr int REC_STRIDE = 4352;
constexpr int CAT_KS = 88;

#define STAMP_DECL
#define STAMP(i)
#define STAMPC(i)
#define STAMP_FLUSH(kern, wsp_)

#define STFLAG "sc1"
#define GST16P(voff, val, sbase) asm volatile("global_store_dwordx4 %0, %1, %2\n\ts_nop 1" :: "v"(voff), "v"(val), "s"(sbase) : "memory")
#define GST16(voff, val, sbase) asm volatile("global_store_dwordx4 %0, %1, %2 " STFLAG "\n\ts_nop 1" :: "v"(voff), "v"(val), "s"(sbase) : "memory")

#define GLD16(dst, voff, sbase, IMM) asm volatile("global_load_dwordx4 %0, %1, %2 offset:" #IMM : "=v"(dst) : "v"(voff), "s"(sbase) : "memory")

DI void glds16(const void* sbase, unsigned voff, unsigned ldsdst) {
    unsigned keep;
    asm volatile("s_mov_b32 %0, m0\n\ts_mov_b32 m0, %3\n\ts_nop 0\n\tglobal_load_lds_dwordx4 %1, %2\n\ts_mov_b32 m0, %0"
                 : "=&s"(keep) : "v"(voff), "s"(sbase), "s"(ldsdst) : "memory");
}
DI unsigned pk2(float a, float b) { f32x2 v = {a, b}; return __builtin_bit_cast(unsigned, __builtin_convertvector(v, bf16x2)); }
DI float bflo(unsigned u) { return __uint_as_float(u << 16); }
DI float bfhi(unsigned u) { return __uint_as_float(u & 0xffff0000u); }
DI s16x4 trread(unsigned addr) { return __builtin_amdgcn_ds_read_tr16_b64_v4i16((LAS s16x4*)(uintptr_t)addr); }
DI bf16x8 trfrag(unsigned addr0, unsigned addr1) {
    s16x4 lo = trread(addr0), hi = trread(addr1);
    return __builtin_bit_cast(bf16x8, __builtin_shufflevector(lo, hi, 0, 1, 2, 3, 4, 5, 6, 7));
}
DI bf16x8 ldsfrag(unsigned addr) { return *(const LAS bf16x8*)(uintptr_t)addr; }
DI float gelu_t(float u) {
    const float k1 = -2.3022081985f;
    float w = u * __builtin_fmaf(u * u, k1 * 0.044715f, k1);
    float e = __builtin_amdgcn_exp2f(w);
    return u * __builtin_amdgcn_rcpf(1.0f + e);
}

constexpr int K1_MISC = 9 * 16384;
constexpr int K1_SUM = K1_MISC;
constexpr int K1_CS = K1_MISC + 1024;
constexpr int K1_PRIV = K1_MISC + 1024 + 2048;
constexpr int K1_JUNK = K1_PRIV + 8 * 256;
constexpr int K1_LDS = K1_JUNK + 1024;
enum { EPI_Q = 0, EPI_K = 1, EPI_V = 2, EPI_Z1 = 3, EPI_Z2 = 4 };
DI f32x16 splat16(float v) { f32x16 r; for (int i = 0; i < 16; ++i) r[i] = v; return r; }

#define BLDS(rsrc, voff, soff, SIMM, m0b, MIMM) do { unsigned t_; \
    asm volatile("s_add_u32 m0, %4, %c6\n\ts_add_u32 %0, %3, %c5\n\tbuffer_load_dwordx4 %1, %2, %0 offen lds" \
                 : "=&s"(t_) : "v"(voff), "s"(rsrc), "s"(soff), "s"(m0b), "i"(SIMM), "i"(MIMM) : "scc", "memory"); } while (0)

template <bool ORI2, bool GELU>
DI void k1_body(const char* __restrict__ x, const char* __restrict__ W, unsigned col0  , int rp, char* __restrict__ dst  ,
                unsigned dvoff, unsigned jstride, unsigned pstride, float oscale, char* __restrict__ ws, unsigned lds0) {
    constexpr int LDW = GELU ? 3584 : 3072, NP = GELU ? 3 : 4;
    const int tid = threadIdx.x, lane = tid & 63;
    const int wid = __builtin_amdgcn_readfirstlane(tid >> 6);
    const int wm = wid & 3, wn = wid >> 2;
    const int r = lane & 31, h = lane >> 5;
    STAMP_DECL; STAMP(0); STAMPC(14);
    const __amdgpu_buffer_rsrc_t xr = __builtin_amdgcn_make_buffer_rsrc((void*)x, (short)0, S_ * D_ * 2, 0x00020000);
    const __amdgpu_buffer_rsrc_t wr = __builtin_amdgcn_make_buffer_rsrc((void*)W, (short)0, D_ * LDW, 0x00020000);
    const unsigned xso = rp * 131072 + wid * 16384, wso = col0 + wid * 16 * LDW;
    unsigned xvoff[2];
#pragma unroll
    for (int i = 0; i < 2; ++i) xvoff[i] = (i * 8 + (lane >> 3)) * 1024 + (((lane & 7) ^ ((4 * i + (lane >> 4)) & 7)) << 4);
    const unsigned wvoff = ((lane >> 2) & 7) * LDW + (lane >> 5) * 64 + (lane & 3) * 16;
    const unsigned sdst = lds0 + wid * 2048;
#define ISSX(c, slot) do { BLDS(xr, xvoff[0], xso, (c) * 128, sdst, (slot) * 16384); BLDS(xr, xvoff[1], xso, (c) * 128, sdst, (slot) * 16384 + 1024); } while (0)
#define ISSW(pp, st, slot) do { BLDS(wr, wvoff, wso, (pp) * 128 + (st) * 128 * LDW, sdst, (slot) * 16384); BLDS(wr, wvoff, wso, (pp) * 128 + ((st) * 128 + 8) * LDW, sdst, (slot) * 16384 + 1024); } while (0)
    ISSX(0, 0); ISSX(1, 1); ISSW(0, 0, 2);
    ISSX(2, 4); ISSX(3, 5); ISSW(0, 1, 6);
    ISSX(4, 7); ISSX(5, 8); ISSW(0, 2, 3);
    STAMP(1);
    unsigned xad[4], bad[4];
#pragma unroll
    for (int t = 0; t < 4; ++t) {
        xad[t] = lds0 + (wm * 32 + r) * 128 + (((2 * (t ^ wm) + h) ^ ((r >> 1) & 7)) << 4);
        bad[t] = lds0 + (2 * (t ^ wm) + h) * 1024 + wn * 512 + ((lane & 15) >> 2) * 64 + ((lane >> 4) & 1) * 32 + (lane & 3) * 8;
    }
    const unsigned selw = ((lane & 15) == ((lane >> 4) & 1)) ? 0x3f803f80u : 0u;
    u32x4 selu = {selw, selw, selw, selw};
    const bf16x8 sel = __builtin_bit_cast(bf16x8, selu);
    bf16x8 af[32];
    f32x16 acc[2];
    f32x16 gram = splat16(0.f);
    f32x4 csum = {0, 0, 0, 0}, rsum = {0, 0, 0, 0};
#define MM(bf, a, c) (ORI2 ? MFMA32(a, bf, c) : MFMA32(bf, a, c))
#define BFR(i, off) trfrag(bad[(i) & 3] + (off) + (((i) >> 2) & 1) * 8192, bad[(i) & 3] + (off) + (((i) >> 2) & 1) * 8192 + 256)
#define STAGE0(st, sx0, sx1, sb, G0, G1, G2) do { \
        bf16x8 bf[8]; \
        _Pragma("unroll") for (int i = 0; i < 8; ++i) { af[st * 8 + i] = ldsfrag(xad[i & 3] + (i < 4 ? sx0 : sx1) * 16384); bf[i] = BFR(i, sb * 16384); } \
        SB(); G0; SB(); \
        _Pragma("unroll") for (int i = 0; i < 8; ++i) { \
            if (st == 0 && i < 2) acc[i & 1] = MM(bf[i], af[i], splat16(0.f)); else acc[i & 1] = MM(bf[i], af[st * 8 + i], acc[i & 1]); \
            if ((i & 3) == 0) csum = MFMA16(sel, bf[i], csum); \
            if (i == 3) { PIN(acc[0]); PIN(acc[1]); SB(); G1; SB(); } } \
        PIN(acc[0]); PIN(acc[1]); PIN(csum); SB(); G2; SB(); \
        if (wn == 0) { _Pragma("unroll") for (int i = 0; i < 8; ++i) gram = MFMA32(af[st * 8 + i], af[st * 8 + i], gram); PIN(gram); } \
        else { _Pragma("unroll") for (int i = 0; i < 8; ++i) rsum = MFMA16(sel, af[st * 8 + i], rsum); PIN(rsum); } \
        SB(); \
    } while (0)
#define BAR(n) asm volatile("s_waitcnt vmcnt(" #n ") lgkmcnt(0)\n\ts_barrier" ::: "memory")
    BAR(12); STAMP(2);
    STAGE0(0, 0, 1, 2, {}, {}, {});
    BAR(6); STAMP(3);
    STAGE0(1, 4, 5, 6, ISSX(6, 0), ISSX(7, 1), ISSW(0, 3, 2));
    BAR(6); STAMP(4);
    STAGE0(2, 7, 8, 3, ISSW(1, 0, 4), ISSW(1, 1, 5), ISSW(1, 2, 6));
    BAR(6); STAMP(5);
    STAGE0(3, 0, 1, 2, ISSW(1, 3, 7), {}, {});
    const unsigned csw = lds0 + K1_CS + wn * 512 + (lane & 15) * 16 + wm * 4;
    const unsigned csr = lds0 + K1_CS + wn * 512 + r * 16;
    const unsigned priv = lds0 + K1_PRIV + wid * 256;
#define CSWRITE(par) do { if (lane < 16) { *(LAS float*)(uintptr_t)(csw + (par)) = csum[0]; *(LAS float*)(uintptr_t)(csw + (par) + 256) = csum[1]; } } while (0)
    CSWRITE(0);
    if (wn == 0) {
        float t[4];
#pragma unroll
        for (int j = 0; j < 4; ++j) t[j] = (r & 2) ? ((r & 1) ? gram[4 * j + 3] : gram[4 * j + 2]) : ((r & 1) ? gram[4 * j + 1] : gram[4 * j]);
        const float dg = (r & 16) ? ((r & 8) ? t[3] : t[2]) : ((r & 8) ? t[1] : t[0]);
        if (((r >> 2) & 1) == h) *(LAS float*)(uintptr_t)(lds0 + K1_SUM + (wm * 32 + r) * 4) = dg;
    } else if (lane < 16) {
        *(LAS float*)(uintptr_t)(lds0 + K1_SUM + 512 + (wm * 32 + lane) * 4) = rsum[0];
        *(LAS float*)(uintptr_t)(lds0 + K1_SUM + 512 + (wm * 32 + 16 + lane) * 4) = rsum[1];
    }
    BAR(0); STAMP(6);
    float rs, nm;
    {
        const float ss = *(LAS float*)(uintptr_t)(lds0 + K1_SUM + (wm * 32 + r) * 4), sm = *(LAS float*)(uintptr_t)(lds0 + K1_SUM + 512 + (wm * 32 + r) * 4);
        const float mean = sm * (1.0f / 512.0f);
        const float var = fmaxf(ss * (1.0f / 512.0f) - mean * mean, 0.0f);
        rs = __builtin_amdgcn_rsqf(var + 1e-5f) * oscale;
        nm = -mean * rs;
        if (ORI2) *(LAS float*)(uintptr_t)(priv + h * 128 + r * 4) = h ? nm : rs;
    }
    f32x4 ea[2], eb[2], c4; float cs, ylo; unsigned pk[4];
#define EPI_BEGIN0(par) c4 = *(LAS f32x4*)(uintptr_t)(csr + (par))
#define EPI_BEGIN1() do { cs = (c4[0] + c4[1]) + (c4[2] + c4[3]); PIN(cs); if (!ORI2) *(LAS float*)(uintptr_t)(priv + r * 4) = cs; } while (0)
#define EPI_LOAD(q) do { ea[(q) & 1] = *(LAS f32x4*)(uintptr_t)(priv + (q) * 32 + h * 16); if (ORI2) eb[(q) & 1] = *(LAS f32x4*)(uintptr_t)(priv + 128 + (q) * 32 + h * 16); } while (0)
#define EPI_ELEM(PV, e) do { float y_ = ORI2 ? __builtin_fmaf(eb[((e) >> 2) & 1][(e) & 3], cs, PV[e] * ea[((e) >> 2) & 1][(e) & 3]) : __builtin_fmaf(nm, ea[((e) >> 2) & 1][(e) & 3], PV[e] * rs); \
        if (GELU) y_ = gelu_t(y_); if ((e) & 1) { pk[((e) >> 1) & 3] = pk2(ylo, y_); PIN(pk[((e) >> 1) & 3]); } else { ylo = y_; PIN(ylo); } } while (0)
#define EPI_STORE(j, PLAIN) do { auto s0_ = __builtin_amdgcn_permlane32_swap(pk[0], pk[2], false, false); auto s1_ = __builtin_amdgcn_permlane32_swap(pk[1], pk[3], false, false); \
        u32x4 v_ = {s0_[0], s1_[0], s0_[1], s1_[1]}; if (PLAIN) GST16P(dvoff, v_, dst + (j) * jstride); else GST16(dvoff, v_, dst + (j) * jstride); } while (0)
#pragma unroll
    for (int t = 0; t < 4; ++t) bad[t] += 65536;
    unsigned par = 0, nslot = sdst;
    unsigned wnx = wso + 256;
    constexpr int PD = 4;
#pragma unroll 1
    for (int p = 1; p < NP; ++p) {
        const bool more = p + 1 < NP;
        if (!more) wnx = 0x40000000u;
        bf16x8 bf[PD + 1];
#pragma unroll
        for (int i = 0; i < PD; ++i) bf[i] = BFR(i, 0);
        EPI_BEGIN0(par);
        f32x16 pv = acc[0] + acc[1]; PIN(pv);
        SB();
#pragma unroll
        for (int i = 0; i < 32; ++i) {
            if (i + PD < 32) bf[(i + PD) % (PD + 1)] = BFR(i + PD, ((i + PD) >> 3) * 16384);
            if (i < 2) acc[i & 1] = MM(bf[i], af[i], splat16(0.f)); else acc[i & 1] = MM(bf[i % (PD + 1)], af[i], acc[i & 1]);
            PIN(acc[i & 1]);
            if ((i & 3) == 0) { if (i == 0) { f32x4 z4 = {0, 0, 0, 0}; csum = MFMA16(sel, bf[0], z4); } else csum = MFMA16(sel, bf[i % (PD + 1)], csum); PIN(csum); }
            {
                constexpr int gs[8] = {0, 1, 2, 3, 4, 7, 10, 13};
#define K1G(g) if (i == gs[g]) BLDS(wr, wvoff, wnx, ((g >> 1) * 128 + (g & 1) * 8) * LDW, nslot, (g >> 1) * 16384 + (g & 1) * 1024);
                K1G(0) K1G(1) K1G(2) K1G(3) K1G(4) K1G(5) K1G(6) K1G(7)
            }
            if (i == 1) EPI_BEGIN1();
            if (i == 2 || i == 8 || i == 14 || i == 20) EPI_LOAD((i - 2) / 6);
            if (i >= 5 && i < 29 && (i - 5) % 3 != 2) EPI_ELEM(pv, ((i - 5) / 3) * 2 + (i - 5) % 3);
            if (i == 16 || i == 28) EPI_STORE(i == 28, NP == 4 && p == 1);
            SB();
        }
        par ^= 1024;
        CSWRITE(par);
        if (more) WAITV(2);
        asm volatile("s_waitcnt lgkmcnt(0)\n\ts_barrier" ::: "memory");
        if (p == 1) { STAMP(7); STAMPC(10); } else if (p == 2) { STAMP(8); STAMPC(11); } else STAMP(9);
#pragma unroll
        for (int t = 0; t < 4; ++t) bad[t] ^= 65536;
        nslot ^= 65536; wnx += 128; dst += pstride;
    }
    EPI_BEGIN0(par); EPI_BEGIN1();
    const f32x16 pl = acc[0] + acc[1];
    EPI_LOAD(0);
#pragma unroll
    for (int q = 0; q < 4; ++q) {
        if (q < 3) EPI_LOAD(q + 1);
#pragma unroll
        for (int e = 0; e < 4; ++e) EPI_ELEM(pl, q * 4 + e);
        if (q & 1) EPI_STORE(q >> 1, false);
    }
    STAMP(12);
    STAMP(13);
    STAMP_FLUSH(0, ws);
}

__global__ __launch_bounds__(512, 2) void k1_kernel(const char* __restrict__ x, const char* __restrict__ wqkv, const char* __restrict__ win, char* __restrict__ ws,
                                                    const char* pf_wsp, const char* pf_out, const char* pf_wo, const char* pf_wout) {
    extern __shared__ __attribute__((aligned(16))) char smem[];
    const unsigned lds0 = (unsigned)(uintptr_t)(LAS char*)smem;
    const int b = blockIdx.x, xc = b & 7, rr = b >> 3;
    const int rp = (xc & 1) * 8 + (rr & 7), ct = (xc >> 1) * 4 + (rr >> 3);
    const int lane = threadIdx.x & 63, wid = __builtin_amdgcn_readfirstlane(threadIdx.x >> 6);
    const int rt = rp * 4 + (wid & 3), wn = wid >> 2;
    if (threadIdx.x < FLAG_REP) *(unsigned*)(ws + WS_FLAG + threadIdx.x * FLAG_STRIDE + b * 4) = 0u;
    else if (threadIdx.x < FLAG_REP + 8) *(unsigned*)(ws + WS_REC + (b >> 3) * REC_STRIDE + ((b & 7) * 8 + threadIdx.x - FLAG_REP) * 4) = 0u;
    if (ct < 4) {
        const int head0 = (ct & 1) * 16 + wn * 2;
        k1_body<false, false>(x, wqkv, ct * 512, rp, ws + (ct < 2 ? WS_Q : WS_K) + ((size_t)head0 * 64 + rt) * 1024, lane * 16, 65536, 4 * 65536,
                              ct < 2 ? 0.25f * 1.4426950408889634f : 1.0f, ws, lds0);
    } else if (ct < 6) {
        const int head0 = (ct & 1) * 16 + wn * 2;
        k1_body<true, false>(x, wqkv, ct * 512, rp, ws + WS_V + ((size_t)head0 * 64 + rt) * 1024, ((lane >> 4) & 1) * 65536 + (lane >> 5) * 256 + (lane & 15) * 16, 512, 4 * 65536,
                             1.0f, ws, lds0);
    } else if (ct < 11) {
        const int c0 = min((ct - 6) * 192, 704);
        k1_body<false, true>(x, win, c0 * 2, rp, ws + WS_Z1 + ((size_t)rt * 56 + (c0 >> 4) + wn * 2) * 1024, lane * 16, 1024, 4096, 1.0f, ws, lds0);
    } else {
        const int c0 = min((ct - 11) * 192, 704);
        k1_body<true, true>(x, win, (FH + c0) * 2, rp, ws + WS_Z2 + ((size_t)rt * 4 * FH + c0 + wn * 32) * 16, (lane >> 5) * (FH * 16) + (lane & 31) * 16, 2 * FH * 16, 1024,
                            1.0f, ws, lds0);
    }
}

constexpr int K2_K = 0, K2_V = 62 * 1024, K2_W = 126 * 1024, K2_ONES = K2_W + 32768, K2_MISC = K2_ONES + 1024, K2_LDS = K2_MISC + 1024;
enum { SY_KVA = 0, SY_KVB = 4, SY_KVC = 8, SY_P24 = 12, SY_LN = 16, SY_LNST = 20, SY_RED = 24, SY_FLAG = 28, SY_P30 = 32, SY_CMB = 36  , SY_P16 = 44, SY_MAIN = 48, SY_FIN = 52 };
constexpr int SPIN_MAX = 1 << 20;
constexpr unsigned FLAG_TAG = 0x5eed0001u;
constexpr float REF_OFF = 64.0f;

DI void lds_inc(unsigned addr) { __hip_atomic_fetch_add((LAS unsigned*)(uintptr_t)addr, 1u, __ATOMIC_RELAXED, __HIP_MEMORY_SCOPE_WORKGROUP); }
template <int SLEEP = 1> DI void lds_wait(unsigned addr, unsigned n) {
#pragma unroll 1
    for (int it = 0; it < SPIN_MAX && __builtin_amdgcn_readfirstlane(__hip_atomic_load((LAS unsigned*)(uintptr_t)addr, __ATOMIC_RELAXED, __HIP_MEMORY_SCOPE_WORKGROUP)) < n; ++it)
        __builtin_amdgcn_s_sleep(SLEEP);
    asm volatile("" ::: "memory");
}
DI bf16x16 lo8(const f32x16& x) { f32x8 t = __builtin_shufflevector(x, x, 0, 1, 2, 3, 4, 5, 6, 7); return __builtin_bit_cast(bf16x16, t); }
DI bf16x16 hi8(const f32x16& x) { f32x8 t = __builtin_shufflevector(x, x, 8, 9, 10, 11, 12, 13, 14, 15); return __builtin_bit_cast(bf16x16, t); }
#define SMF(a, b, c) __builtin_amdgcn_smfmac_f32_32x32x32_bf16(a, b, c, sidx, 0, 0)
#define GLD16S(dst, voff, sbase) asm volatile("global_load_dwordx4 %0, %1, %2 sc1" : "=v"(dst) : "v"(voff), "s"(sbase) : "memory")

DI float max16(const f32x16& s) {
    float a = fmaxf(fmaxf(s[0], s[1]), s[2]), b = fmaxf(fmaxf(s[3], s[4]), s[5]);
    a = fmaxf(fmaxf(a, s[6]), s[7]); b = fmaxf(fmaxf(b, s[8]), s[9]);
    a = fmaxf(fmaxf(a, s[10]), s[11]); b = fmaxf(fmaxf(b, s[12]), s[13]);
    a = fmaxf(fmaxf(a, s[14]), s[15]);
    return fmaxf(a, b);
}
DI float xhalf_max(float m) {
    auto rr = __builtin_amdgcn_permlane32_swap(__float_as_uint(m), __float_as_uint(m), false, false);
    return fmaxf(__uint_as_float(rr[0]), __uint_as_float(rr[1]));
}

DI void att_exact(const char* kg, const char* vg, bf16x8 q, f32x16& o_out) {
    const int lane = threadIdx.x & 63;
    const int sidx = (int)0xDDDDDDDDu;
    f32x16 z = splat16(0.f);
    float m = -INFINITY;
    for (int t = 0; t < 64; ++t) {
        bf16x8 kf = *(const bf16x8*)(kg + t * 1024 + lane * 16);
        f32x16 s = MFMA32(kf, q, z);
        m = fmaxf(m, max16(s));
    }
    m = xhalf_max(m);
    f32x16 c = splat16(-m), o = z;
    const bool isv = (lane & 16) == 0;
    u32x4 onesu = {0x3f803f80u, 0x3f803f80u, 0x3f803f80u, 0x3f803f80u};
    const unsigned vo = (lane >> 5) * 256 + (lane & 15) * 16;
    for (int t = 0; t < 64; ++t) {
        bf16x8 kf = *(const bf16x8*)(kg + t * 1024 + lane * 16);
        u32x4 v0 = *(const u32x4*)(vg + t * 1024 + vo), v1 = *(const u32x4*)(vg + t * 1024 + 512 + vo);
        if (!isv) { v0 = onesu; v1 = onesu; }
        f32x16 s = MFMA32(kf, q, c);
#pragma unroll
        for (int i = 0; i < 16; ++i) s[i] = __builtin_amdgcn_exp2f(s[i]);
        o = SMF(__builtin_bit_cast(bf16x8, v0), lo8(s), o);
        o = SMF(__builtin_bit_cast(bf16x8, v1), hi8(s), o);
    }
    o_out = o;
}

DI void att_store(const f32x16& o, char* dst, int lane) {
    const float rl = __builtin_amdgcn_rcpf(o[8]);
    unsigned p0 = pk2(o[0] * rl, o[1] * rl), p1 = pk2(o[2] * rl, o[3] * rl), p2 = pk2(o[4] * rl, o[5] * rl), p3 = pk2(o[6] * rl, o[7] * rl);
    auto s0 = __builtin_amdgcn_permlane32_swap(p0, p2, false, false);
    auto s1 = __builtin_amdgcn_permlane32_swap(p1, p3, false, false);
    u32x4 v = {s0[0], s1[0], s0[1], s1[1]};
    const unsigned l16 = lane * 16;
    GST16(l16, v, dst);
}
DI bool att_bad(const f32x16& o) {
    float a = fmaxf(fmaxf(fabsf(o[0]), fabsf(o[1])), fmaxf(fabsf(o[2]), fabsf(o[3])));
    float b = fmaxf(fmaxf(fabsf(o[4]), fabsf(o[5])), fmaxf(fabsf(o[6]), fabsf(o[7])));
    float s = (o[0] + o[1] + o[2] + o[3]) + (o[4] + o[5] + o[6] + o[7]);
    bool ok = (o[8] < 0x1p120f) && (o[8] > 0x1p-80f) && (fmaxf(a, b) < 0x1p124f) && (s == s);
    return __any(!ok);
}

DI void att_role(int head, int qb, int w, char* __restrict__ ws, unsigned lds0) {
    const int lane = threadIdx.x & 63;
    const int qh = w & 1, kh = w >> 1;
    const char* kg = ws + WS_K + (size_t)head * 65536;
    const char* vg = ws + WS_V + (size_t)head * 65536;
    const char* qg = ws + WS_Q + (size_t)head * 65536 + (size_t)(qb * 8 + qh * 4) * 1024;
    const unsigned l16 = lane * 16;
    const unsigned vo = (lane >> 5) * 256 + (lane & 15) * 16;
    u32x4 qr[4], k0, k1;
    STAMP_DECL; STAMP(0); STAMPC(14);
    const unsigned qo = l16 + kh * 2048, qo2 = l16 + (kh ^ 1) * 2048;
    GLD16(k0, l16, kg + kh * 32768, 0); GLD16(k1, l16, kg + (kh ^ 1) * 32768, 0);
    GLD16(qr[0], qo, qg, 0); GLD16(qr[1], qo, qg, 1024); GLD16(qr[2], qo2, qg, 0); GLD16(qr[3], qo2, qg, 1024);
    const char* ksrc = (qh ? vg : kg + 1024) + kh * 32768;
    const unsigned kdst = lds0 + (qh ? K2_V + kh * 32768 : K2_K + kh * 31744);
#pragma unroll
    for (int i = 0; i < 5; ++i) glds16(ksrc + i * 1024, l16, kdst + i * 1024);
    *(LAS unsigned*)(uintptr_t)(lds0 + K2_ONES + threadIdx.x * 4) = ((threadIdx.x >> 2) & 11) == 0 ? 0x3f803f80u : 0u;
    asm volatile("s_waitcnt lgkmcnt(0)\n\ts_barrier" ::: "memory");
#pragma unroll
    for (int i = 5; i < 13; ++i) glds16(ksrc + i * 1024, l16, kdst + i * 1024);
    STAMP(1);
    asm volatile("s_waitcnt vmcnt(13)" : "+v"(qr[0]), "+v"(qr[1]), "+v"(qr[2]), "+v"(qr[3]), "+v"(k0), "+v"(k1) :: "memory");
    STAMP(2);
#define QF(i) __builtin_bit_cast(bf16x8, qr[i])
    int sidx = (int)0xDDDDDDDDu; PIN(sidx);
    f32x16 cc[4];
#pragma unroll
    for (int i = 0; i < 4; i += 2) {
        f32x16 z = splat16(0.f);
        f32x16 s0 = MFMA32(__builtin_bit_cast(bf16x8, k0), QF(i), z), s1 = MFMA32(__builtin_bit_cast(bf16x8, k1), QF(i), z);
        f32x16 s2 = MFMA32(__builtin_bit_cast(bf16x8, k0), QF(i + 1), z), s3 = MFMA32(__builtin_bit_cast(bf16x8, k1), QF(i + 1), z);
        PIN(s0); PIN(s1); PIN(s2); PIN(s3);
        float mA = xhalf_max(fmaxf(max16(s0), max16(s1))), mB = xhalf_max(fmaxf(max16(s2), max16(s3)));
        cc[i] = splat16(-(mA + REF_OFF)); cc[i + 1] = splat16(-(mB + REF_OFF));
        PIN(cc[i]); PIN(cc[i + 1]);
    }
    bf16x8 kf = __builtin_bit_cast(bf16x8, k0);
    f32x16 oo[4] = {splat16(0.f), splat16(0.f), splat16(0.f), splat16(0.f)}, sA, sB = splat16(0.f);
    sA = MFMA32(kf, QF(0), cc[0]);
    WAITV(8); lds_inc(lds0 + K2_MISC + SY_KVA);
    unsigned kaddr = lds0 + K2_K + kh * 31744 + l16;
    unsigned vaddr = (lane & 16) ? (lds0 + K2_ONES + vo) : (lds0 + K2_V + kh * 32768 + vo);
    const unsigned vinc = (lane & 16) ? 0u : 1024u;
    lds_wait(lds0 + K2_MISC + SY_KVA, 4 * 64);
    bf16x8 v0 = ldsfrag(vaddr), v1 = v0;
    STAMP(3);
#define EXPS(X, a, b) do { _Pragma("unroll") for (int i_ = a; i_ < b; ++i_) X[i_] = __builtin_amdgcn_exp2f(X[i_]); PIN(X); SB(); } while (0)
#define HSTEP(X, Y, i, A1, A2, A3) do { \
        EXPS(X, 0, 2); \
        oo[(i + 3) & 3] = SMF(v1, hi8(Y), oo[(i + 3) & 3]); PIN(oo[(i + 3) & 3]); SB(); A1; SB(); \
        EXPS(X, 2, 5); \
        Y = MFMA32(kf, QF((i + 1) & 3), cc[(i + 1) & 3]); PIN(Y); SB(); A2; SB(); \
        EXPS(X, 5, 11); \
        oo[i] = SMF(v0, lo8(X), oo[i]); PIN(oo[i]); SB(); A3; SB(); \
        EXPS(X, 11, 16); \
    } while (0)
#pragma unroll 1
    for (int seg = 0; seg < 6; ++seg) {
        int n = 4;
        if (seg == 1) { n = 8; STAMP(4); WAITV(0); lds_inc(lds0 + K2_MISC + SY_KVB); lds_wait(lds0 + K2_MISC + SY_KVB, 4 * 64); STAMP(5); STAMPC(10); }
        else if (seg == 2) { n = 5; STAMP(6); STAMPC(11); lds_wait(lds0 + K2_MISC + SY_KVC, 4 * 64); STAMP(7); STAMPC(12); }
        else if (seg >= 3) {
            n = seg == 3 ? 8 : seg == 4 ? 6 : 1;
            asm volatile("s_waitcnt lgkmcnt(0)" ::: "memory");
            lds_inc(lds0 + K2_MISC + (seg == 3 ? SY_P16 : seg == 4 ? SY_P24 : SY_P30));
        }
#pragma unroll 1
        for (int i = 0; i < n; ++i) {
            HSTEP(sA, sB, 0, { v1 = ldsfrag(vaddr + 512); vaddr += vinc; }, {}, {});
            HSTEP(sB, sA, 1, {}, {}, {});
            HSTEP(sA, sB, 2, {}, { kf = ldsfrag(kaddr); kaddr += 1024; }, {});
            HSTEP(sB, sA, 3, {}, {}, { v0 = ldsfrag(vaddr); });
        }
    }
    oo[3] = SMF(v1, hi8(sB), oo[3]);
    STAMP(8); STAMPC(13);
    {
        auto cbase = [&](int ww) { return lds0 + ((ww & 1) ? K2_V + (ww >> 1) * 32768 + 25 * 1024 : K2_K + (ww >> 1) * 31744 + 24 * 1024); };
        lds_wait(lds0 + K2_MISC + SY_P30, 4 * 64);
        const unsigned wa = cbase(w), ra = cbase(w ^ 2);
#pragma unroll
        for (int i = 0; i < 2; ++i) {
            f32x4 a = {oo[2 + i][0], oo[2 + i][1], oo[2 + i][2], oo[2 + i][3]}, b = {oo[2 + i][4], oo[2 + i][5], oo[2 + i][6], oo[2 + i][7]};
            *(LAS f32x4*)(uintptr_t)(wa + i * 2048 + l16) = a;
            *(LAS f32x4*)(uintptr_t)(wa + i * 2048 + 1024 + l16) = b;
            *(LAS float*)(uintptr_t)(wa + 4096 + i * 256 + lane * 4) = oo[2 + i][8];
        }
        asm volatile("s_waitcnt lgkmcnt(0)" ::: "memory");
        lds_inc(lds0 + K2_MISC + SY_CMB + qh * 4);
        lds_wait(lds0 + K2_MISC + SY_CMB + qh * 4, 2 * 64);
#pragma unroll
        for (int i = 0; i < 2; ++i) {
            f32x4 a = *(LAS f32x4*)(uintptr_t)(ra + i * 2048 + l16), b = *(LAS f32x4*)(uintptr_t)(ra + i * 2048 + 1024 + l16);
            float l = *(LAS float*)(uintptr_t)(ra + 4096 + i * 256 + lane * 4);
#pragma unroll
            for (int e = 0; e < 4; ++e) { oo[i][e] += a[e]; oo[i][4 + e] += b[e]; }
            oo[i][8] += l;
        }
    }
    if (__builtin_expect(att_bad(oo[0]), 0)) att_exact(kg, vg, QF(0), oo[0]);
    if (__builtin_expect(att_bad(oo[1]), 0)) att_exact(kg, vg, QF(1), oo[1]);
    char* dst = ws + WS_CAT + ((size_t)(qb * 8 + qh * 4 + kh * 2) * CAT_KS + head) * 1024;
    att_store(oo[0], dst, lane);
    att_store(oo[1], dst + CAT_KS * 1024, lane);
    WAITV(0);
    if (lane == 0) __hip_atomic_store((unsigned*)(ws + WS_REC + (qb * 4 + qh * 2 + kh) * REC_STRIDE) + head, FLAG_TAG, __ATOMIC_RELAXED, __HIP_MEMORY_SCOPE_AGENT);
    STAMP(9);
    STAMP_FLUSH(1, ws);
}

template <int CTRL> DI float dppf(float x) { return __builtin_bit_cast(float, __builtin_amdgcn_mov_dpp(__builtin_bit_cast(int, x), CTRL, 0xf, 0xf, true)); }
DI float fold32(float a, float b) {
    auto r = __builtin_amdgcn_permlane32_swap(__float_as_uint(a), __float_as_uint(b), false, false);
    return __uint_as_float(r[0]) + __uint_as_float(r[1]);
}
DI float fold16(float a, float b) {
    auto r = __builtin_amdgcn_permlane16_swap(__float_as_uint(a), __float_as_uint(b), false, false);
    return __uint_as_float(r[0]) + __uint_as_float(r[1]);
}
DI float rowsum16(float v) { v += dppf<0xB1>(v); v += dppf<0x4E>(v); v += dppf<0x124>(v); v += dppf<0x128>(v); return v; }

DI void spg_role(int b, int head, int w, const char* __restrict__ wsp, const __hip_bfloat16* __restrict__ bsp, char* __restrict__ ws, unsigned lds0,
                 const char* __restrict__ x, const char* __restrict__ wo, const char* __restrict__ wout, char* __restrict__ out) {
    const int lane = threadIdx.x & 63;
    const int xc = b & 7, rr = b >> 3;
    const int tt = xc * 4 + (rr & 3), ft = rr >> 2;
    const bool active = ft < 7;
    STAMP_DECL; STAMP(0); STAMPC(14);
    __builtin_amdgcn_s_setprio(2);
    const unsigned wring = lds0 + K2_W + w * 8192;
    const __amdgpu_buffer_rsrc_t wr = __builtin_amdgcn_make_buffer_rsrc((void*)wsp, (short)0, S_ * S_ * 2, 0x00020000);
    unsigned wso = tt * 64 * 4096 + w * 1024;
    unsigned wvoff[2];
#pragma unroll
    for (int p = 0; p < 2; ++p) wvoff[p] = (lane >> 3) * 4096 + (((lane & 7) ^ ((4 * p + (lane >> 4)) & 7)) << 4);
#define ISSUEW(i) BLDS(wr, wvoff[(i) & 1], wso, (i) * 32768, wring, (i) * 1024)
    {
        const char* src = ws + WS_Z2 + (size_t)b * FH * 16;
        char* dst = ws + WS_Z2N + (size_t)b * FH * 16;
        u32x4 zc[4], z0;
        const unsigned o0 = (w * 224 + lane) * 16;
        const unsigned o3 = (w * 224 + 192 + (lane & 31)) * 16;
        const unsigned zero = 0;
        GLD16(zc[0], o0, src, 0); GLD16(zc[1], o0, src, 1024); GLD16(zc[2], o0, src, 2048); GLD16(zc[3], o3, src, 0); GLD16(z0, zero, src, 0);
        *(LAS unsigned*)(uintptr_t)(lds0 + K2_MISC + (threadIdx.x - 256) * 4) = 0u;
        asm volatile("s_waitcnt lgkmcnt(0)\n\ts_barrier" ::: "memory");
        asm volatile("s_waitcnt vmcnt(0)" : "+v"(zc[0]), "+v"(zc[1]), "+v"(zc[2]), "+v"(zc[3]), "+v"(z0) :: "memory");
        STAMP(1);
        float sh[8], s1[8], s2[8];
#pragma unroll
        for (int e = 0; e < 4; ++e) { sh[2 * e] = bflo(z0[e]); sh[2 * e + 1] = bfhi(z0[e]); }
        const float m3 = lane < 32 ? 1.f : 0.f;
#pragma unroll
        for (int e = 0; e < 8; ++e) {
            float a = ((e & 1) ? bfhi(zc[0][e >> 1]) : bflo(zc[0][e >> 1])) - sh[e];
            float bb = ((e & 1) ? bfhi(zc[1][e >> 1]) : bflo(zc[1][e >> 1])) - sh[e];
            float c = ((e & 1) ? bfhi(zc[2][e >> 1]) : bflo(zc[2][e >> 1])) - sh[e];
            float d = (((e & 1) ? bfhi(zc[3][e >> 1]) : bflo(zc[3][e >> 1])) - sh[e]) * m3;
            s1[e] = (a + bb) + (c + d);
            s2[e] = (a * a + bb * bb) + (c * c + d * d);
        }
        f32x4 q;
#pragma unroll
        for (int i = 0; i < 4; ++i) q[i] = rowsum16(fold16(fold32(s1[i], s2[i]), fold32(s1[i + 4], s2[i + 4])));
        LAS float* part = (LAS float*)(uintptr_t)(lds0 + K2_MISC + 256);
        *(LAS f32x4*)(part + w * 16 + (lane >> 4) * 4) = q;
        asm volatile("s_waitcnt lgkmcnt(0)" ::: "memory");
        lds_inc(lds0 + K2_MISC + SY_LN);
        lds_wait(lds0 + K2_MISC + SY_LN, 4 * 64);
        STAMP(2);
        float rs[8], nm[8];
        {
            f32x4 t[4];
#pragma unroll
            for (int i = 0; i < 4; ++i) t[i] = (*(LAS f32x4*)(part + i * 4) + *(LAS f32x4*)(part + 16 + i * 4)) + (*(LAS f32x4*)(part + 32 + i * 4) + *(LAS f32x4*)(part + 48 + i * 4));
#pragma unroll
            for (int e = 0; e < 8; ++e) {
                float mean = t[e >> 2][e & 3] * (1.0f / FH);
                float var = fmaxf(t[2 + (e >> 2)][e & 3] * (1.0f / FH) - mean * mean, 0.f);
                rs[e] = __builtin_amdgcn_rsqf(var + 1e-5f);
                nm[e] = -(mean + sh[e]) * rs[e];
            }
        }
#pragma unroll
        for (int j = 0; j < 4; ++j) {
            u32x4 v;
#pragma unroll
            for (int e = 0; e < 4; ++e) v[e] = pk2(__builtin_fmaf(bflo(zc[j][e]), rs[2 * e], nm[2 * e]), __builtin_fmaf(bfhi(zc[j][e]), rs[2 * e + 1], nm[2 * e + 1]));
            const unsigned o = j < 3 ? o0 + j * 1024 : o3;
            asm volatile("global_store_dwordx4 %0, %1, %2 sc1\n\ts_nop 1" :: "v"(o), "v"(v), "s"(dst) : "memory");
        }
        STAMP(3);
        WAITV(0);
        STAMP(4);
        unsigned old = 0;
        if (lane == 0) old = __hip_atomic_fetch_add((LAS unsigned*)(uintptr_t)(lds0 + K2_MISC + SY_LNST), 1u, __ATOMIC_RELAXED, __HIP_MEMORY_SCOPE_WORKGROUP);
        old = __builtin_amdgcn_readfirstlane(old);
        if (old == 3 && lane < FLAG_REP) __hip_atomic_store((unsigned*)(ws + WS_FLAG + lane * FLAG_STRIDE) + b, FLAG_TAG, __ATOMIC_RELAXED, __HIP_MEMORY_SCOPE_AGENT);
    }
    ISSUEW(0); ISSUEW(1); ISSUEW(2); ISSUEW(3); ISSUEW(4); ISSUEW(5); ISSUEW(6); ISSUEW(7);
    if (w != 0) {
        const __amdgpu_buffer_rsrc_t kr = __builtin_amdgcn_make_buffer_rsrc((void*)(ws + WS_K), (short)0, 4u << 20, 0x00020000);
        const unsigned lo = lane * 16;
        const unsigned so = head * 65536 + (w & 1) * (unsigned)(WS_V - WS_K) + (w >> 1) * 32768, so0 = head * 65536 + (w - 1) * 6144;
        const unsigned d = lds0 + ((w & 1) ? K2_V + (w >> 1) * 32768 : K2_K + (w >> 1) * 31744 - 1024), d0 = lds0 + K2_K - 1024 + (w - 1) * 6144;
#define KV1(t) BLDS(kr, lo, so, (t) * 1024, d, (t) * 1024)
#define KV0(t) BLDS(kr, lo, so0, (t) * 1024, d0, (t) * 1024)
        KV1(13); KV0(13); KV1(14); KV1(15); KV0(14); KV1(16); KV1(17); KV0(15); KV1(18); KV1(19); KV0(16); KV1(20); KV1(21); KV0(17); KV1(22);
        KV1(23); KV0(18); KV1(24); KV1(25); KV0(19); KV1(26); KV1(27); KV1(28); KV1(29); KV1(30); KV1(31);
    }
    const unsigned l16 = lane * 16;
    auto reg = [&](int ww) { return lds0 + ((ww & 1) ? K2_V + (ww >> 1) * 32768 : K2_K + (ww >> 1) * 31744); };
    const char* rec = ws + WS_REC + tt * REC_STRIDE;
    if (active) {
        const char* zb = ws + WS_Z2N + ((size_t)w * 64 * FH + ft * 128) * 16;
        unsigned zvoff[4];
#pragma unroll
        for (int j = 0; j < 4; ++j) { zvoff[j] = (lane >> 5) * (FH * 16) + (j ^ w) * 512 + (lane & 31) * 16; PIN(zvoff[j]); }
        u32x4 zf[4][4];
#define ZPTR(c, ks) (zb + (size_t)((c) * 4 + (ks)) * (2 * FH * 16))
        f32x16 acc[4][2];
#pragma unroll
        for (int j = 0; j < 4; ++j) { acc[j][0] = splat16(0.f); acc[j][1] = splat16(0.f); PIN(acc[j][0]); PIN(acc[j][1]); }
        unsigned waddr[4];
#pragma unroll
        for (int ks = 0; ks < 4; ++ks) { waddr[ks] = wring + (lane & 31) * 128 + (((ks * 2 + (lane >> 5)) ^ ((lane >> 1) & 7)) << 4); PIN(waddr[ks]); }
        if (w == 0) {
            const char* fl = ws + WS_FLAG + (size_t)(b >> 3) * FLAG_STRIDE;
#pragma unroll 1
            for (int it = 0; it < SPIN_MAX; ++it) {
                u32x4 f;
                asm volatile("global_load_dwordx4 %0, %1, %2 sc1\n\ts_waitcnt vmcnt(0)" : "=&v"(f) : "v"(l16), "s"(fl) : "memory");
                if (__all(f[0] == FLAG_TAG && f[1] == FLAG_TAG && f[2] == FLAG_TAG && f[3] == FLAG_TAG)) break;
                __builtin_amdgcn_s_sleep(4);
            }
            lds_inc(lds0 + K2_MISC + SY_FLAG);
        } else lds_wait<4>(lds0 + K2_MISC + SY_FLAG, 64);
        STAMP(5);
#pragma unroll
        for (int ks = 0; ks < 4; ++ks)
#pragma unroll
            for (int j = 0; j < 4; ++j) GLD16S(zf[ks][j], zvoff[j], ZPTR(0, ks));
        WAITV(16);
        lds_inc(lds0 + K2_MISC + SY_KVC);
        __builtin_amdgcn_s_setprio(0);
#define ZWAIT(n, ks) asm volatile("s_waitcnt vmcnt(" #n ")" : "+v"(zf[ks][0]), "+v"(zf[ks][1]), "+v"(zf[ks][2]), "+v"(zf[ks][3]) :: "memory")
#define KPAIR(ks, j_, MORE) do { \
        acc[j_][0] = MFMA32(__builtin_bit_cast(bf16x8, zf[ks][j_]), wf[0][ks], acc[j_][0]); PIN(acc[j_][0]); SB(); \
        if (MORE && ks == 0) ISSUEW(2 * j_); \
        acc[j_][1] = MFMA32(__builtin_bit_cast(bf16x8, zf[ks][j_]), wf[1][ks], acc[j_][1]); PIN(acc[j_][1]); SB(); \
        if (MORE && ks == 0) ISSUEW(2 * j_ + 1); \
        if (MORE) GLD16S(zf[ks][j_], zvoff[j_], ZPTR(c + 1, ks)); } while (0)
#define KSTEP(ks, MORE) do { KPAIR(ks, 0, MORE); KPAIR(ks, 1, MORE); KPAIR(ks, 2, MORE); KPAIR(ks, 3, MORE); } while (0)
#define WREAD() _Pragma("unroll") for (int ks = 0; ks < 4; ++ks) { wf[0][ks] = ldsfrag(waddr[ks]); wf[1][ks] = ldsfrag(waddr[ks] + 4096); } \
        asm volatile("s_waitcnt lgkmcnt(0)" : "+v"(wf[0][0]), "+v"(wf[0][1]), "+v"(wf[0][2]), "+v"(wf[0][3]), "+v"(wf[1][0]), "+v"(wf[1][1]), "+v"(wf[1][2]), "+v"(wf[1][3]) :: "memory")
#pragma unroll 1
        for (int c = 0; c < 7; ++c) {
            bf16x8 wf[2][4];
            wso += 128;
            ZWAIT(12, 0);
            WREAD();
            KSTEP(0, true);
            ZWAIT(20, 1); KSTEP(1, true);
            ZWAIT(20, 2); KSTEP(2, true);
            ZWAIT(20, 3); KSTEP(3, true);
        }
        const int fs0 = ft * 8 + w * 2;
        const char* z1p = ws + WS_Z1 + ((size_t)tt * 2 * 56 + fs0) * 1024;
        const char* bp = (const char*)bsp + tt * 128;
        const unsigned bo = (lane & 31) * 2;
        unsigned braw[2];
        {
            const int c = 7; (void)c;
            bf16x8 wf[2][4];
            ZWAIT(12, 0);
            WREAD();
            lds_inc(lds0 + K2_MISC + SY_MAIN);
            KSTEP(0, false);
            GLD16(zf[0][0], l16, z1p, 0); GLD16(zf[0][1], l16, z1p, 1024); GLD16(zf[0][2], l16, z1p + 56 * 1024, 0); GLD16(zf[0][3], l16, z1p + 56 * 1024, 1024);
            asm volatile("global_load_ushort %0, %1, %2" : "=v"(braw[0]) : "v"(bo), "s"(bp) : "memory");
            asm volatile("global_load_ushort %0, %1, %2 offset:64" : "=v"(braw[1]) : "v"(bo), "s"(bp) : "memory");
            ZWAIT(14, 1); KSTEP(1, false);
            ZWAIT(10, 2); KSTEP(2, false);
            ZWAIT(6, 3); KSTEP(3, false);
        }
        STAMP(6);
        __builtin_amdgcn_s_setprio(2);
        lds_wait(lds0 + K2_MISC + SY_P16, 4 * 64);
        lds_wait(lds0 + K2_MISC + SY_MAIN, 4 * 64);
        auto blk = [&](int dstw, int j) { return (j < 3 ? reg(dstw) + (j - 1) * 8192 : lds0 + K2_W + dstw * 8192) + l16; };
#pragma unroll
        for (int j = 1; j < 4; ++j) {
            const unsigned a = blk(j ^ w, j);
#pragma unroll
            for (int t = 0; t < 2; ++t)
#pragma unroll
                for (int q = 0; q < 4; ++q) {
                    f32x4 v = {acc[j][t][4 * q], acc[j][t][4 * q + 1], acc[j][t][4 * q + 2], acc[j][t][4 * q + 3]};
                    *(LAS f32x4*)(uintptr_t)(a + (t * 4 + q) * 1024) = v;
                }
        }
        asm volatile("s_waitcnt lgkmcnt(0)" ::: "memory");
        lds_inc(lds0 + K2_MISC + SY_RED);
        lds_wait(lds0 + K2_MISC + SY_RED, 4 * 64);
#pragma unroll
        for (int j = 1; j < 4; ++j) {
            const unsigned a = blk(w, j);
#pragma unroll
            for (int t = 0; t < 2; ++t)
#pragma unroll
                for (int q = 0; q < 4; ++q) {
                    f32x4 v = *(LAS f32x4*)(uintptr_t)(a + (t * 4 + q) * 1024);
#pragma unroll
                    for (int e = 0; e < 4; ++e) acc[0][t][4 * q + e] += v[e];
                }
        }
        asm volatile("s_waitcnt vmcnt(0)" : "+v"(zf[0][0]), "+v"(zf[0][1]), "+v"(zf[0][2]), "+v"(zf[0][3]), "+v"(braw[0]), "+v"(braw[1]) :: "memory");
        const float bias[2] = {bflo(braw[0]), bflo(braw[1])};
#pragma unroll
        for (int t = 0; t < 2; ++t)
#pragma unroll
            for (int s = 0; s < 2; ++s) {
                auto a0 = __builtin_amdgcn_permlane32_swap(zf[0][2 * t + s][0], zf[0][2 * t + s][2], false, false);
                auto a1 = __builtin_amdgcn_permlane32_swap(zf[0][2 * t + s][1], zf[0][2 * t + s][3], false, false);
                const unsigned zc[4] = {a0[0], a1[0], a0[1], a1[1]};
                unsigned p[4];
#pragma unroll
                for (int e = 0; e < 4; ++e)
                    p[e] = pk2(bflo(zc[e]) * (acc[0][t][8 * s + 2 * e] + bias[t]), bfhi(zc[e]) * (acc[0][t][8 * s + 2 * e + 1] + bias[t]));
                auto s0 = __builtin_amdgcn_permlane32_swap(p[0], p[2], false, false);
                auto s1 = __builtin_amdgcn_permlane32_swap(p[1], p[3], false, false);
                u32x4 v = {s0[0], s1[0], s0[1], s1[1]};
                GST16(l16, v, ws + WS_CAT + ((size_t)(tt * 2 + t) * CAT_KS + 32 + fs0 + s) * 1024);
            }
        WAITV(0);
        if (lane == 0) __hip_atomic_store((unsigned*)rec + 32 + ft * 4 + w, FLAG_TAG, __ATOMIC_RELAXED, __HIP_MEMORY_SCOPE_AGENT);
        STAMP(7);
    } else {
        const int id = (rr & 3) * 4 + w;
        WAITV(0);
        lds_inc(lds0 + K2_MISC + SY_KVC);
#pragma unroll 1
        for (int i = 0; i < 64; ++i) { glds16(wsp + (size_t)(xc * 256 + id * 16 + (i & 15)) * 4096 + (i >> 4) * 1024, l16, wring); if ((i & 7) == 7) WAITV(32); }
        lds_wait<32>(lds0 + K2_MISC + SY_P16, 4 * 64);
#pragma unroll 1
        for (int i = 0; i < 56; ++i) { glds16(wout + (size_t)(id * 56 + i) * 1024, l16, wring); if ((i & 7) == 7) WAITV(32); }
#pragma unroll 1
        for (int i = 0; i < 32; ++i) { glds16(wo + (size_t)(id * 32 + i) * 1024, l16, wring); if ((i & 7) == 7) WAITV(32); }
#pragma unroll 1
        for (int i = 0; i < 16; ++i) glds16(x + (size_t)(xc * 256 + id * 16 + i) * 1024, l16, wring);
        WAITV(0);
    }
    {
        const __amdgpu_buffer_rsrc_t hr = __builtin_amdgcn_make_buffer_rsrc((void*)wout, (short)0, FH * 1024, 0x00020000);
        const __amdgpu_buffer_rsrc_t orr = __builtin_amdgcn_make_buffer_rsrc((void*)wo, (short)0, D_ * 1024, 0x00020000);
        const unsigned bvoff = ((lane >> 2) & 7) * 1024 + (lane >> 5) * 64 + (lane & 3) * 16;
        const unsigned hso = ft * 128 + w * 14 * 16384, oso = ft * 128 + w * 8 * 16384;
        const unsigned rg = reg(w);
#define FB(rs, so, s, base, off) do { BLDS(rs, bvoff, so, (s) * 16384, base, off); BLDS(rs, bvoff, so, (s) * 16384 + 8192, base, (off) + 1024); } while (0)
        FB(hr, hso, 0, rg, 0); FB(hr, hso, 1, rg, 2048); FB(hr, hso, 2, rg, 4096); FB(hr, hso, 3, rg, 6144);
        FB(hr, hso, 4, rg, 8192); FB(hr, hso, 5, rg, 10240); FB(hr, hso, 6, rg, 12288); FB(hr, hso, 7, rg, 14336);
        FB(hr, hso, 8, wring, 0); FB(hr, hso, 9, wring, 2048); FB(hr, hso, 10, wring, 4096); FB(hr, hso, 11, wring, 6144);
        const size_t oo_ = (size_t)(tt * 64 + (w >> 1) * 32) * 1024 + (ft * 64 + (w & 1) * 32) * 2;
        const unsigned xo = (lane & 31) * 1024 + (lane >> 5) * 16;
        u32x4 xv[2];
        GLD16(xv[0], xo, x + oo_, 0); GLD16(xv[1], xo, x + oo_, 32);
        lds_wait<8>(lds0 + K2_MISC + SY_P24, 4 * 64);
        FB(hr, hso, 12, rg, 16384); FB(hr, hso, 13, rg, 18432);
        const unsigned bl = (lane >> 5) * 1024 + (w & 1) * 512 + ((lane & 15) >> 2) * 64 + ((lane >> 4) & 1) * 32 + (lane & 3) * 8;
        const unsigned br0 = rg + bl, br1 = rg + (bl ^ 512), bw0 = wring + bl, bw1 = wring + (bl ^ 512);
        const char* a0p = ws + WS_CAT + (size_t)(tt * 2 + (w >> 1)) * CAT_KS * 1024;
        const char* a1p = ws + WS_CAT + (size_t)(tt * 2 + ((w >> 1) ^ 1)) * CAT_KS * 1024;
        auto poll = [&](int first, int n) {
            const unsigned fo = (first + (lane < n ? lane : 0)) * 4;
#pragma unroll 1
            for (int it = 0; it < SPIN_MAX; ++it) {
                unsigned f;
                asm volatile("global_load_dword %0, %1, %2 sc1\n\ts_waitcnt vmcnt(0)" : "=&v"(f) : "v"(fo), "s"(rec) : "memory");
                if (__all(f == FLAG_TAG)) break;
                __builtin_amdgcn_s_sleep(4);
            }
        };
        u32x4 af[14][2];
        f32x16 fc[2][2];
        bf16x8 bq[2][2];
#define SLOT0(s) ((s) < 8 ? br0 + (s) * 2048 : (s) < 12 ? bw0 + ((s) - 8) * 2048 : br0 + 16384 + ((s) - 12) * 2048)
#define SLOT1(s) ((s) < 8 ? br1 + (s) * 2048 : (s) < 12 ? bw1 + ((s) - 8) * 2048 : br1 + 16384 + ((s) - 12) * 2048)
#define BQ(s) do { bq[(s) & 1][0] = trfrag(SLOT0(s), SLOT0(s) + 256); bq[(s) & 1][1] = trfrag(SLOT1(s), SLOT1(s) + 256); } while (0)
#define AWAIT(n, s) asm volatile("s_waitcnt vmcnt(%c2)" : "+v"(af[s][0]), "+v"(af[s][1]) : "i"(n) : "memory")
#define FMM(s, FIRST) do { _Pragma("unroll") for (int i_ = 0; i_ < 2; ++i_) _Pragma("unroll") for (int j_ = 0; j_ < 2; ++j_) { \
            fc[i_][j_] = MFMA32(bq[(s) & 1][j_], __builtin_bit_cast(bf16x8, af[s][i_]), (FIRST) ? splat16(0.f) : fc[i_][j_]); PIN(fc[i_][j_]); } SB(); } while (0)
        poll(32 + w * 7, 7);
        STAMP(8);
#pragma unroll
        for (int s = 0; s < 14; ++s) { GLD16S(af[s][0], l16, a0p + (32 + w * 14 + s) * 1024); GLD16S(af[s][1], l16, a1p + (32 + w * 14 + s) * 1024); }
        BQ(0);
        __builtin_amdgcn_s_setprio(0);
#define HST(s) do { if (s < 13) BQ(s + 1); AWAIT(2 * (13 - s) + (s > 8 ? 16 : 0), s); FMM(s, s == 0); } while (0)
        HST(0); HST(1); HST(2); HST(3); HST(4); HST(5); HST(6); HST(7); HST(8);
        FB(orr, oso, 0, rg, 0); FB(orr, oso, 1, rg, 2048); FB(orr, oso, 2, rg, 4096); FB(orr, oso, 3, rg, 6144);
        FB(orr, oso, 4, rg, 8192); FB(orr, oso, 5, rg, 10240); FB(orr, oso, 6, rg, 12288); FB(orr, oso, 7, rg, 14336);
        HST(9); HST(10); HST(11); HST(12); HST(13);
        STAMP(9);
        __builtin_amdgcn_s_setprio(2);
        poll(w * 8, 8);
        STAMP(10);
#pragma unroll
        for (int s = 0; s < 8; ++s) { GLD16S(af[s][0], l16, a0p + (w * 8 + s) * 1024); GLD16S(af[s][1], l16, a1p + (w * 8 + s) * 1024); }
        BQ(0);
#define OST(s) do { if (s < 7) BQ(s + 1); AWAIT(2 * (7 - s), s); FMM(s, false); } while (0)
        OST(0); OST(1); OST(2); OST(3); OST(4); OST(5); OST(6); OST(7);
        STAMP(11);
#pragma unroll
        for (int j = 1; j < 4; ++j)
#pragma unroll
            for (int q = 0; q < 4; ++q) {
                const f32x16& t = fc[j >> 1][j & 1];
                f32x4 v = {t[4 * q], t[4 * q + 1], t[4 * q + 2], t[4 * q + 3]};
                *(LAS f32x4*)(uintptr_t)(rg + (j * 4 + q) * 1024 + l16) = v;
            }
        asm volatile("s_waitcnt lgkmcnt(0)" ::: "memory");
        lds_inc(lds0 + K2_MISC + SY_FIN);
        lds_wait(lds0 + K2_MISC + SY_FIN, 4 * 64);
#pragma unroll
        for (int j = 1; j < 4; ++j)
#pragma unroll
            for (int q = 0; q < 4; ++q) {
                f32x4 v = *(LAS f32x4*)(uintptr_t)(reg(w ^ j) + (j * 4 + q) * 1024 + l16);
#pragma unroll
                for (int e = 0; e < 4; ++e) fc[0][0][4 * q + e] += v[e];
            }
        asm volatile("" : "+v"(xv[0]), "+v"(xv[1]));
#pragma unroll
        for (int s = 0; s < 2; ++s) {
            auto a0 = __builtin_amdgcn_permlane32_swap(xv[s][0], xv[s][2], false, false);
            auto a1 = __builtin_amdgcn_permlane32_swap(xv[s][1], xv[s][3], false, false);
            const unsigned xc4[4] = {a0[0], a1[0], a0[1], a1[1]};
            unsigned p[4];
#pragma unroll
            for (int e = 0; e < 4; ++e) p[e] = pk2(bflo(xc4[e]) + fc[0][0][8 * s + 2 * e], bfhi(xc4[e]) + fc[0][0][8 * s + 2 * e + 1]);
            auto s0 = __builtin_amdgcn_permlane32_swap(p[0], p[2], false, false);
            auto s1 = __builtin_amdgcn_permlane32_swap(p[1], p[3], false, false);
            u32x4 v = {s0[0], s1[0], s0[1], s1[1]};
            *(u32x4*)(out + oo_ + xo + s * 32) = v;
        }
    }
    STAMP(12);
    STAMP_FLUSH(1, ws);
}

__global__ __launch_bounds__(512, 2) void k2_kernel(const char* __restrict__ wsp, const __hip_bfloat16* __restrict__ bsp, char* __restrict__ ws,
                                                    const char* __restrict__ x, const char* __restrict__ wo, const char* __restrict__ wout, char* __restrict__ out) {
    extern __shared__ __attribute__((aligned(16))) char smem[];
    const unsigned lds0 = (unsigned)(uintptr_t)(LAS char*)smem;
    const int wid = __builtin_amdgcn_readfirstlane(threadIdx.x >> 6);
    const int b = blockIdx.x;
    const int head = (b & 7) * 4 + (b >> 6);
    if (wid < 4) att_role(head, (b >> 3) & 7, wid, ws, lds0);
    else spg_role(b, head, wid - 4, wsp, bsp, ws, lds0, x, wo, wout, out);
}

extern "C" void kernel_launch(void* const* d_in, const int* in_sizes, int n_in, void* d_out, int out_size, void* d_ws, size_t ws_size,
                              hipStream_t stream) {
    const char* x = (const char*)d_in[0];
    const char* wqkv = (const char*)d_in[1];
    const char* wo = (const char*)d_in[2];
    const char* win = (const char*)d_in[4];
    const char* wsp = (const char*)d_in[5];
    const __hip_bfloat16* bsp = (const __hip_bfloat16*)d_in[6];
    const char* wout = (const char*)d_in[7];
    char* ws = (char*)d_ws;
    (void)hipFuncSetAttribute((const void*)k1_kernel, hipFuncAttributeMaxDynamicSharedMemorySize, K1_LDS);
    (void)hipFuncSetAttribute((const void*)k2_kernel, hipFuncAttributeMaxDynamicSharedMemorySize, K2_LDS);
    hipLaunchKernelGGL(k1_kernel, dim3(256), dim3(512), K1_LDS, stream, x, wqkv, win, ws, wsp, (const char*)d_out, wo, wout);
    hipLaunchKernelGGL(k2_kernel, dim3(256), dim3(512), K2_LDS, stream, wsp, bsp, ws, x, wo, wout, (char*)d_out);
}
```

```cpp
#include <hip/hip_runtime.h>
#include <hip/hip_bf16.h>
#include <stdint.h>

typedef __attribute__((ext_vector_type(2))) __bf16 bf16x2;
typedef __attribute__((ext_vector_type(8))) __bf16 bf16x8;
typedef __attribute__((ext_vector_type(16))) __bf16 bf16x16;
typedef __attribute__((ext_vector_type(2))) float f32x2;
typedef __attribute__((ext_vector_type(4))) float f32x4;
typedef __attribute__((ext_vector_type(8))) float f32x8;
typedef __attribute__((ext_vector_type(16))) float f32x16;
typedef __attribute__((ext_vector_type(2))) unsigned u32x2;
typedef __attribute__((ext_vector_type(4))) unsigned u32x4;
typedef __attribute__((ext_vector_type(4))) short s16x4;
#define LAS __attribute__((address_space(3)))
#define DI __device__ __forceinline__
#if defined(__HIP_DEVICE_COMPILE__)
#define NOPK __attribute__((target("no-packed-fp32-ops")))
#else
#define NOPK
#endif
#define SB() __builtin_amdgcn_sched_barrier(0)
#define PIN(x) asm volatile("" : "+v"(x))
#define WAITV(n) asm volatile("s_waitcnt vmcnt(" #n ")" ::: "memory")
#define MFMA16(a, b, c) __builtin_amdgcn_mfma_f32_16x16x32_bf16(a, b, c, 0, 0, 0)
#define MFMA32(a, b, c) __builtin_amdgcn_mfma_f32_32x32x16_bf16(a, b, c, 0, 0, 0)

constexpr int S_ = 2048, D_ = 512, FH = 896;
constexpr size_t WS_Q = 0, WS_K = 2u << 20, WS_V = 4u << 20, WS_Z1 = 6u << 20, WS_Z2 = 10u << 20, WS_Z2N = 14u << 20,
                 WS_CAT = 18u << 20, WS_FLAG = 24u << 20;
constexpr int CNT_STRIDE = 256, CNT_REC = 128;
constexpr int CAT_KS = 88;

#define STAMP_DECL
#define STAMP(i)
#define STAMPC(i)
#define STAMP_FLUSH(kern, wsp_)

#define STFLAG "sc1"
#define GST16P(voff, val, sbase) asm volatile("global_store_dwordx4 %0, %1, %2\n\ts_nop 1" :: "v"(voff), "v"(val), "s"(sbase) : "memory")
#define GST16(voff, val, sbase) asm volatile("global_store_dwordx4 %0, %1, %2 " STFLAG "\n\ts_nop 1" :: "v"(voff), "v"(val), "s"(sbase) : "memory")

#define GLD16(dst, voff, sbase, IMM) asm volatile("global_load_dwordx4 %0, %1, %2 offset:" #IMM : "=v"(dst) : "v"(voff), "s"(sbase) : "memory")

DI void glds16(const void* sbase, unsigned voff, unsigned ldsdst) {
    unsigned keep;
    asm volatile("s_mov_b32 %0, m0\n\ts_mov_b32 m0, %3\n\ts_nop 0\n\tglobal_load_lds_dwordx4 %1, %2\n\ts_mov_b32 m0, %0"
                 : "=&s"(keep) : "v"(voff), "s"(sbase), "s"(ldsdst) : "memory");
}
DI unsigned pk2(float a, float b) { f32x2 v = {a, b}; return __builtin_bit_cast(unsigned, __builtin_convertvector(v, bf16x2)); }
DI float bflo(unsigned u) { return __uint_as_float(u << 16); }
DI float bfhi(unsigned u) { return __uint_as_float(u & 0xffff0000u); }
DI s16x4 trread(unsigned addr) { return __builtin_amdgcn_ds_read_tr16_b64_v4i16((LAS s16x4*)(uintptr_t)addr); }
DI bf16x8 trfrag(unsigned addr0, unsigned addr1) {
    s16x4 lo = trread(addr0), hi = trread(addr1);
    return __builtin_bit_cast(bf16x8, __builtin_shufflevector(lo, hi, 0, 1, 2, 3, 4, 5, 6, 7));
}
DI bf16x8 ldsfrag(unsigned addr) { return *(const LAS bf16x8*)(uintptr_t)addr; }
DI float gelu_t(float u) {
    const float k1 = -2.3022081985f;
    float w = u * __builtin_fmaf(u * u, k1 * 0.044715f, k1);
    float e = __builtin_amdgcn_exp2f(w);
    return u * __builtin_amdgcn_rcpf(1.0f + e);
}

constexpr int K1_MISC = 9 * 16384;
constexpr int K1_SUM = K1_MISC;
constexpr int K1_CS = K1_MISC + 1024;
constexpr int K1_PRIV = K1_MISC + 1024 + 2048;
constexpr int K1_JUNK = K1_PRIV + 8 * 256;
constexpr int K1_LDS = K1_JUNK + 1024;
enum { EPI_Q = 0, EPI_K = 1, EPI_V = 2, EPI_Z1 = 3, EPI_Z2 = 4 };
DI f32x16 splat16(float v) { f32x16 r; for (int i = 0; i < 16; ++i) r[i] = v; return r; }

#define BLDS(rsrc, voff, soff, SIMM, m0b, MIMM) do { unsigned t_; \
    asm volatile("s_add_u32 m0, %4, %c6\n\ts_add_u32 %0, %3, %c5\n\tbuffer_load_dwordx4 %1, %2, %0 offen lds" \
                 : "=&s"(t_) : "v"(voff), "s"(rsrc), "s"(soff), "s"(m0b), "i"(SIMM), "i"(MIMM) : "scc", "memory"); } while (0)

template <bool ORI2, bool GELU>
DI void k1_body(const char* __restrict__ x, const char* __restrict__ W, unsigned col0  , int rp, char* __restrict__ dst  ,
                unsigned dvoff, unsigned jstride, unsigned pstride, float oscale, char* __restrict__ ws, unsigned lds0, const char* __restrict__ wsp) {
    constexpr int LDW = GELU ? 3584 : 3072, NP = GELU ? 3 : 4;
    const int tid = threadIdx.x, lane = tid & 63;
    const int wid = __builtin_amdgcn_readfirstlane(tid >> 6);
    const int wm = wid & 3, wn = wid >> 2;
    const int r = lane & 31, h = lane >> 5;
    STAMP_DECL; STAMP(0); STAMPC(14);
    const __amdgpu_buffer_rsrc_t xr = __builtin_amdgcn_make_buffer_rsrc((void*)x, (short)0, S_ * D_ * 2, 0x00020000);
    const __amdgpu_buffer_rsrc_t wr = __builtin_amdgcn_make_buffer_rsrc((void*)W, (short)0, D_ * LDW, 0x00020000);
    const unsigned xso = rp * 131072 + wid * 16384, wso = col0 + wid * 16 * LDW;
    unsigned xvoff[2];
#pragma unroll
    for (int i = 0; i < 2; ++i) xvoff[i] = (i * 8 + (lane >> 3)) * 1024 + (((lane & 7) ^ ((4 * i + (lane >> 4)) & 7)) << 4);
    const unsigned wvoff = ((lane >> 2) & 7) * LDW + (lane >> 5) * 64 + (lane & 3) * 16;
    const unsigned sdst = lds0 + wid * 2048;
#define ISSX1(c, slot, i) BLDS(xr, xvoff[i], xso, (c) * 128, sdst, (slot) * 16384 + (i) * 1024)
#define ISSW1(pp, st, slot, i) BLDS(wr, wvoff, wso, (pp) * 128 + ((st) * 128 + (i) * 8) * LDW, sdst, (slot) * 16384 + (i) * 1024)
#define ISSX(c, slot) do { ISSX1(c, slot, 0); ISSX1(c, slot, 1); } while (0)
#define ISSW(pp, st, slot) do { ISSW1(pp, st, slot, 0); ISSW1(pp, st, slot, 1); } while (0)
    ISSX(0, 0); ISSX(1, 1); ISSW(0, 0, 2);
    ISSX(2, 3); ISSX(3, 4); ISSW(0, 1, 5);
    STAMP(1);
#define PERM(q) ((q) == 0 ? 0 : (q) == 1 ? 4 : (q) < 5 ? (q) - 1 : (q))
    unsigned xad[4], bad[4];
#pragma unroll
    for (int t = 0; t < 4; ++t) {
        xad[t] = lds0 + (wm * 32 + r) * 128 + (((2 * (t ^ wm) + h) ^ ((r >> 1) & 7)) << 4);
        bad[t] = lds0 + (2 * (t ^ wm) + h) * 1024 + wn * 512 + ((lane & 15) >> 2) * 64 + ((lane >> 4) & 1) * 32 + (lane & 3) * 8;
    }
    const unsigned selw = ((lane & 15) == ((lane >> 4) & 1)) ? 0x3f803f80u : 0u;
    u32x4 selu = {selw, selw, selw, selw};
    const bf16x8 sel = __builtin_bit_cast(bf16x8, selu);
    bf16x8 af[32];
    f32x16 accA, accB;
    f32x16 gram;
    f32x4 csum, rsum = {0, 0, 0, 0};
    const f32x4 z4 = {0, 0, 0, 0};
#define MM(bf, a, c) (ORI2 ? MFMA32(a, bf, c) : MFMA32(bf, a, c))
#define MM16(bf, a) asm volatile("v_mfma_f32_32x32x16_bf16 %0, %1, %2, %3" : "+v"(accB) : "v"(ORI2 ? (a) : (bf)), "v"(ORI2 ? (bf) : (a)), "v"(accA))
#define BFR(q, off) trfrag(bad[PERM(q) & 3] + (off) + (PERM(q) >> 2) * 8192, bad[PERM(q) & 3] + (off) + (PERM(q) >> 2) * 8192 + 256)
#define MAIN(i, bf) do { if ((i) == 0) accA = MM(bf, af[i], splat16(0.f)); else if ((i) < 16) accA = MM(bf, af[i], accA); else if ((i) == 16) MM16(bf, af[i]); else accB = MM(bf, af[i], accB); \
        if ((i) < 16) PIN(accA); else PIN(accB); \
        if (((i) & 7) < 2) { csum = MFMA16(sel, bf, (i) == 0 ? z4 : csum); PIN(csum); } } while (0)
#define READS0(st, s0) _Pragma("unroll") for (int q = 0; q < 8; ++q) { af[st * 8 + q] = ldsfrag(xad[PERM(q) & 3] + (s0 + (PERM(q) >> 2)) * 16384); bf[q] = BFR(q, (s0 + 2) * 16384); } SB()
#define G32(k) "v_mfma_f32_32x32x16_bf16 %0, %" #k ", %" #k ", %0\n\t"
#define G16(k) "v_mfma_f32_16x16x32_bf16 %1, %2, %" #k ", %1\n\t"
#define STATS(st) do { asm volatile("s_cmp_lg_u32 %3, 0\n\ts_cbranch_scc1 1f\n\t" G32(4) G32(5) G32(6) G32(7) G32(8) G32(9) G32(10) G32(11) "s_branch 2f\n1:\n\t" \
            G16(4) G16(5) G16(6) G16(7) G16(8) G16(9) G16(10) G16(11) "2:" : "+v"(gram), "+v"(rsum) : "v"(sel), "s"(wn), "v"(af[st * 8]), "v"(af[st * 8 + 1]), "v"(af[st * 8 + 2]), \
            "v"(af[st * 8 + 3]), "v"(af[st * 8 + 4]), "v"(af[st * 8 + 5]), "v"(af[st * 8 + 6]), "v"(af[st * 8 + 7]) : "scc"); SB(); } while (0)
#define BAR(n) asm volatile("s_waitcnt vmcnt(" #n ") lgkmcnt(0)\n\ts_barrier" ::: "memory")
    gram = splat16(0.f); PIN(gram); PIN(rsum);
    asm volatile("" : "=v"(accB));
    const unsigned csw = lds0 + K1_CS + wn * 512 + (lane & 15) * 16 + wm * 4;
    const unsigned csr = lds0 + K1_CS + wn * 512 + r * 16;
    const unsigned priv = lds0 + K1_PRIV + wid * 256;
#define CSWRITE(par) do { if (lane < 16) { *(LAS float*)(uintptr_t)(csw + (par)) = csum[0]; *(LAS float*)(uintptr_t)(csw + (par) + 256) = csum[1]; } } while (0)
    bf16x8 bq[4];
    {
        bf16x8 bf[8];
        BAR(6); STAMP(2);
        READS0(0, 0);
        ISSX(4, 6);
        const unsigned pso = col0 + ((rp & 7) * 64 + wid * 8) * LDW, junk = lds0 + K1_JUNK;
#pragma unroll
        for (int q = 0; q < 8; ++q) {
            MAIN(q, bf[q]);
            if (q == 0) ISSX1(5, 7, 0); if (q == 1) ISSX1(5, 7, 1); if (q == 2) ISSW1(0, 2, 8, 0); if (q == 3) ISSW1(0, 2, 8, 1);
            if (q == 4) BLDS(wr, wvoff, pso, 128, junk, 0); if (q == 5) BLDS(wr, wvoff, pso, 256, junk, 0); if (q == 6) BLDS(wr, wvoff, pso, (NP - 1) * 128, junk, 0);
            if (q == 7) {
                const __amdgpu_buffer_rsrc_t sr = __builtin_amdgcn_make_buffer_rsrc((void*)wsp, (short)0, S_ * S_ * 2, 0x00020000);
                const unsigned so = ((blockIdx.x & 7) * 32 + (blockIdx.x >> 3)) * 32768 + wm * 1024 + wn * 128, so_v = (lane >> 3) * 4096 + (lane & 7) * 16;
                BLDS(sr, so_v, so, 0, junk, 0);
            }
            SB();
        }
        BAR(10); STAMP(3);
        READS0(1, 3);
        ISSX(6, 0); STATS(0);
#pragma unroll
        for (int q = 0; q < 8; ++q) { MAIN(8 + q, bf[q]); if (q == 0) ISSX1(7, 1, 0); if (q == 2) ISSX1(7, 1, 1); if (q == 4) ISSW1(0, 3, 2, 0); if (q == 6) ISSW1(0, 3, 2, 1); SB(); }
        BAR(10); STAMP(4);
        READS0(2, 6);
        ISSW(1, 0, 3); STATS(1);
#pragma unroll
        for (int q = 0; q < 8; ++q) { MAIN(16 + q, bf[q]); if (q == 0) ISSW1(1, 1, 4, 0); if (q == 2) ISSW1(1, 1, 4, 1); if (q == 4) ISSW1(1, 2, 5, 0); if (q == 6) ISSW1(1, 2, 5, 1); SB(); }
        BAR(6); STAMP(5);
        READS0(3, 0);
        ISSW(1, 3, 6); STATS(2); STATS(3);
        MAIN(24, bf[0]); MAIN(25, bf[1]); MAIN(26, bf[2]); SB();
        asm volatile("s_nop 7\n\ts_nop 7" : "+v"(gram), "+v"(rsum));
        if (wn == 0) {
            auto G = [&](int j) { float v = gram[j]; PIN(v); return v; };
            auto Q4 = [&](int j) { const float a0 = G(j), a1 = G(j + 1), a2 = G(j + 2), a3 = G(j + 3); const float u0 = (r & 1) ? a1 : a0, u1 = (r & 1) ? a3 : a2; return (r & 2) ? u1 : u0; };
            const float t0 = Q4(0), t1 = Q4(4), t2 = Q4(8), t3 = Q4(12);
            const float u0 = (r & 8) ? t1 : t0, u1 = (r & 8) ? t3 : t2;
            const float dg = (r & 16) ? u1 : u0;
            if (((r >> 2) & 1) == h) *(LAS float*)(uintptr_t)(lds0 + K1_SUM + (wm * 32 + r) * 4) = dg;
        } else if (lane < 16) {
            *(LAS float*)(uintptr_t)(lds0 + K1_SUM + 512 + (wm * 32 + lane) * 4) = rsum[0];
            *(LAS float*)(uintptr_t)(lds0 + K1_SUM + 512 + (wm * 32 + 16 + lane) * 4) = rsum[1];
        }
        CSWRITE(0);
        SB();
        MAIN(27, bf[3]); MAIN(28, bf[4]); SB();
        BAR(0); STAMP(6);
#pragma unroll
        for (int t = 0; t < 4; ++t) bad[t] += 3 * 16384;
        bq[0] = BFR(0, 0); MAIN(29, bf[5]); SB();
        bq[1] = BFR(1, 0); MAIN(30, bf[6]); SB();
        bq[2] = BFR(2, 0); MAIN(31, bf[7]); SB();
    }
    float rs, nm;
    {
        const float ss = *(LAS float*)(uintptr_t)(lds0 + K1_SUM + (wm * 32 + r) * 4), sm = *(LAS float*)(uintptr_t)(lds0 + K1_SUM + 512 + (wm * 32 + r) * 4);
        const float mean = sm * (1.0f / 512.0f);
        const float var = fmaxf(ss * (1.0f / 512.0f) - mean * mean, 0.0f);
        rs = __builtin_amdgcn_rsqf(var + 1e-5f) * oscale;
        nm = -mean * rs;
        if (ORI2) *(LAS float*)(uintptr_t)(priv + h * 128 + r * 4) = h ? nm : rs;
    }
    f32x4 ea[2], eb[2], c4; float cs, ylo; unsigned pk[4];
#define EPI_BEGIN0(par) c4 = *(LAS f32x4*)(uintptr_t)(csr + (par))
#define EPI_BEGIN1() do { cs = (c4[0] + c4[1]) + (c4[2] + c4[3]); PIN(cs); if (!ORI2) *(LAS float*)(uintptr_t)(priv + r * 4) = cs; } while (0)
#define EPI_LOAD(q) do { ea[(q) & 1] = *(LAS f32x4*)(uintptr_t)(priv + (q) * 32 + h * 16); if (ORI2) eb[(q) & 1] = *(LAS f32x4*)(uintptr_t)(priv + 128 + (q) * 32 + h * 16); } while (0)
#define EPI_ELEM(V, e) do { float y_ = ORI2 ? __builtin_fmaf(eb[((e) >> 2) & 1][(e) & 3], cs, (V) * ea[((e) >> 2) & 1][(e) & 3]) : __builtin_fmaf(nm, ea[((e) >> 2) & 1][(e) & 3], (V) * rs); \
        if (GELU) y_ = gelu_t(y_); if ((e) & 1) { pk[((e) >> 1) & 3] = pk2(ylo, y_); PIN(pk[((e) >> 1) & 3]); } else { ylo = y_; PIN(ylo); } } while (0)
#define EPI_STORE(j, PLAIN) do { auto s0_ = __builtin_amdgcn_permlane32_swap(pk[0], pk[2], false, false); auto s1_ = __builtin_amdgcn_permlane32_swap(pk[1], pk[3], false, false); \
        u32x4 v_ = {s0_[0], s1_[0], s0_[1], s1_[1]}; if (PLAIN) GST16P(dvoff, v_, dst + (j) * jstride); else GST16(dvoff, v_, dst + (j) * jstride); } while (0)
    EPI_BEGIN0(0);
    unsigned par = 0, rslot = 3, wslot = 7;
    unsigned wnx = wso + 256;
#pragma unroll 1
    for (int p = 1; p < NP; ++p) {
        const bool more = p + 1 < NP;
        if (!more) wnx = 0x40000000u;
        unsigned wd[4];
#pragma unroll
        for (int s = 0; s < 4; ++s) { wd[s] = sdst + wslot * 16384; wslot = wslot == 8 ? 0 : wslot + 1; }
        float hi[8];
        SB();
#pragma unroll
        for (int i = 0; i < 32; ++i) {
            if (i == 29) {
                par ^= 1024;
                if (more) WAITV(2);
                asm volatile("s_waitcnt lgkmcnt(0)\n\ts_barrier" ::: "memory");
                if (p == 1) { STAMP(7); STAMPC(10); } else if (p == 2) { STAMP(8); STAMPC(11); } else STAMP(9);
                EPI_BEGIN0(par);
            }
            if (((i + 3) & 7) == 0) {
                const unsigned d = rslot == 8 ? (unsigned)(-8 * 16384) : 16384u;
                rslot = rslot == 8 ? 0 : rslot + 1;
#pragma unroll
                for (int t = 0; t < 4; ++t) bad[t] += d;
            }
            bq[(i + 3) & 3] = BFR((i + 3) & 7, 0);
            if (i >= 4 && i < 12) asm volatile("v_mov_b32 %0, %1" : "=&v"(hi[i - 4]) : "v"(accB[8 + i - 4]));
            if (i == 0) EPI_BEGIN1();
            if (i == 1 || i == 7 || i == 13 || i == 19) EPI_LOAD((i - 1) / 6);
            if (i >= 4 && i < 28 && (i - 4) % 3 != 2) { const int e = ((i - 4) / 3) * 2 + (i - 4) % 3; EPI_ELEM(e < 8 ? accB[e & 7] : hi[e & 7], e); }
            MAIN(i, bq[i & 3]);
            if (i < 16 && !(i & 1)) BLDS(wr, wvoff, wnx, ((i >> 2) * 128 + ((i >> 1) & 1) * 8) * LDW, wd[i >> 2], ((i >> 1) & 1) * 1024);
            if (i == 15 || i == 27) EPI_STORE(i == 27, NP == 4 && p == 1);
            if (i == 27) CSWRITE(par ^ 1024);
            SB();
        }
        wnx += 128; dst += pstride;
    }
    EPI_BEGIN1();
    EPI_LOAD(0);
#pragma unroll
    for (int q = 0; q < 4; ++q) {
        if (q < 3) EPI_LOAD(q + 1);
#pragma unroll
        for (int e = 0; e < 4; ++e) EPI_ELEM(accB[q * 4 + e], q * 4 + e);
        if (q & 1) EPI_STORE(q >> 1, false);
    }
    STAMP(12);
    STAMP(13);
    STAMP_FLUSH(0, ws);
}

__global__ __launch_bounds__(512, 2) NOPK void k1_kernel(const char* __restrict__ x, const char* __restrict__ wqkv, const char* __restrict__ win, char* __restrict__ ws,
                                                         const char* __restrict__ wsp) {
    extern __shared__ __attribute__((aligned(16))) char smem[];
    const unsigned lds0 = (unsigned)(uintptr_t)(LAS char*)smem;
    const int b = blockIdx.x, xc = b & 7, rr = b >> 3;
    const int rp = (xc & 1) * 8 + (rr & 7), ct = (xc >> 1) * 4 + (rr >> 3);
    const int lane = threadIdx.x & 63, wid = __builtin_amdgcn_readfirstlane(threadIdx.x >> 6);
    const int rt = rp * 4 + (wid & 3), wn = wid >> 2;
    if (threadIdx.x < 2) *(unsigned*)(ws + WS_FLAG + (threadIdx.x ? 256 + (b & 127) : b) * CNT_STRIDE) = 0u;
    if (ct < 4) {
        const int head0 = (ct & 1) * 16 + wn * 2;
        k1_body<false, false>(x, wqkv, ct * 512, rp, ws + (ct < 2 ? WS_Q : WS_K) + ((size_t)head0 * 64 + rt) * 1024, lane * 16, 65536, 4 * 65536,
                              ct < 2 ? 0.25f * 1.4426950408889634f : 1.0f, ws, lds0, wsp);
    } else if (ct < 6) {
        const int head0 = (ct & 1) * 16 + wn * 2;
        k1_body<true, false>(x, wqkv, ct * 512, rp, ws + WS_V + ((size_t)head0 * 64 + rt) * 1024, ((lane >> 4) & 1) * 65536 + (lane >> 5) * 256 + (lane & 15) * 16, 512, 4 * 65536,
                             1.0f, ws, lds0, wsp);
    } else if (ct < 11) {
        const int c0 = min((ct - 6) * 192, 704);
        k1_body<false, true>(x, win, c0 * 2, rp, ws + WS_Z1 + ((size_t)rt * 56 + (c0 >> 4) + wn * 2) * 1024, lane * 16, 1024, 4096, 1.0f, ws, lds0, wsp);
    } else {
        const int c0 = min((ct - 11) * 192, 704);
        k1_body<true, true>(x, win, (FH + c0) * 2, rp, ws + WS_Z2 + ((size_t)rt * 4 * FH + c0 + wn * 32) * 16, (lane >> 5) * (FH * 16) + (lane & 31) * 16, 2 * FH * 16, 1024,
                            1.0f, ws, lds0, wsp);
    }
}

constexpr int K2_K = 0, K2_V = 62 * 1024, K2_W = 126 * 1024, K2_ONES = K2_W + 32768, K2_MISC = K2_ONES + 1024, K2_LDS = K2_MISC + 1024;
enum { SY_KVA = 0, SY_KVB = 4, SY_KVC = 8, SY_P26 = 12, SY_LN = 16, SY_LNST = 20, SY_RED = 24, SY_FLAG = 28, SY_CMB = 36  , SY_P16 = 44, SY_MAIN = 48, SY_FIN = 52 };
constexpr int SPIN_MAX = 1 << 20;
DI void cnt_add(char* ws, int i) { __hip_atomic_fetch_add((unsigned*)(ws + WS_FLAG + (size_t)i * CNT_STRIDE), 1u, __ATOMIC_RELAXED, __HIP_MEMORY_SCOPE_AGENT); }
DI void publish(char* p) {
    unsigned t0, t1;
    asm volatile("v_mov_b32 %0, 0\n\tv_mov_b32 %1, 1\n\ts_waitcnt vmcnt(0)\n\ts_mov_b64 exec, 1\n\tglobal_atomic_add %0, %1, %2\n\ts_mov_b64 exec, -1" : "=&v"(t0), "=&v"(t1) : "s"(p) : "memory");
}
constexpr float REF_OFF = 64.0f;

DI void lds_inc(unsigned addr) {
    unsigned t0, t1;
    asm volatile("v_mov_b32 %0, %2\n\tv_mov_b32 %1, 1\n\ts_mov_b64 exec, 1\n\tds_add_u32 %0, %1\n\ts_mov_b64 exec, -1" : "=&v"(t0), "=&v"(t1) : "s"(addr) : "memory");
}
template <int SLEEP = 0> DI void lds_wait(unsigned addr, unsigned n) {
    unsigned t0, t1, c, v;
    if (SLEEP == 0)
        asm volatile("v_mov_b32 %0, %4\n\ts_mov_b32 %2, %6\n1:\n\tds_read_b32 %1, %0\n\ts_sub_u32 %2, %2, 1\n\ts_waitcnt lgkmcnt(0)\n\tv_readfirstlane_b32 %3, %1\n\ts_nop 0\n\t"
                     "s_cselect_b32 %3, %5, %3\n\ts_cmp_lt_u32 %3, %5\n\ts_cbranch_scc1 1b"
                     : "=&v"(t0), "=&v"(t1), "=&s"(c), "=&s"(v) : "s"(addr), "s"(n), "i"(SPIN_MAX) : "scc", "memory");
    else
        asm volatile("v_mov_b32 %0, %4\n\ts_mov_b32 %2, %6\n\ts_branch 2f\n1:\n\ts_sleep %c7\n2:\n\tds_read_b32 %1, %0\n\ts_sub_u32 %2, %2, 1\n\ts_waitcnt lgkmcnt(0)\n\tv_readfirstlane_b32 %3, %1\n\ts_nop 0\n\t"
                     "s_cselect_b32 %3, %5, %3\n\ts_cmp_lt_u32 %3, %5\n\ts_cbranch_scc1 1b"
                     : "=&v"(t0), "=&v"(t1), "=&s"(c), "=&s"(v) : "s"(addr), "s"(n), "i"(SPIN_MAX), "i"(SLEEP) : "scc", "memory");
}
DI bf16x16 lo8(const f32x16& x) { f32x8 t = __builtin_shufflevector(x, x, 0, 1, 2, 3, 4, 5, 6, 7); return __builtin_bit_cast(bf16x16, t); }
DI bf16x16 hi8(const f32x16& x) { f32x8 t = __builtin_shufflevector(x, x, 8, 9, 10, 11, 12, 13, 14, 15); return __builtin_bit_cast(bf16x16, t); }
#define PRIO_HI() __builtin_amdgcn_s_setprio(2)
#define PRIO_LO() __builtin_amdgcn_s_setprio(0)
#define SMF(a, b, c) __builtin_amdgcn_smfmac_f32_32x32x32_bf16(a, b, c, sidx, 0, 0)
#define GLD16S(dst, voff, sbase) asm volatile("global_load_dwordx4 %0, %1, %2 sc1" : "=v"(dst) : "v"(voff), "s"(sbase) : "memory")

DI float max16(const f32x16& s) {
    float a = fmaxf(fmaxf(s[0], s[1]), s[2]), b = fmaxf(fmaxf(s[3], s[4]), s[5]);
    a = fmaxf(fmaxf(a, s[6]), s[7]); b = fmaxf(fmaxf(b, s[8]), s[9]);
    a = fmaxf(fmaxf(a, s[10]), s[11]); b = fmaxf(fmaxf(b, s[12]), s[13]);
    a = fmaxf(fmaxf(a, s[14]), s[15]);
    return fmaxf(a, b);
}
DI float xhalf_max(float m) {
    auto rr = __builtin_amdgcn_permlane32_swap(__float_as_uint(m), __float_as_uint(m), false, false);
    return fmaxf(__uint_as_float(rr[0]), __uint_as_float(rr[1]));
}

DI void att_exact(const char* kg, const char* vg, bf16x8 q, f32x16& o_out) {
    const int lane = threadIdx.x & 63;
    const int sidx = (int)0xDDDDDDDDu;
    f32x16 z = splat16(0.f);
    float m = -INFINITY;
    for (int t = 0; t < 64; ++t) {
        bf16x8 kf = *(const bf16x8*)(kg + t * 1024 + lane * 16);
        f32x16 s = MFMA32(kf, q, z);
        m = fmaxf(m, max16(s));
    }
    m = xhalf_max(m);
    f32x16 c = splat16(-m), o = z;
    const bool isv = (lane & 16) == 0;
    u32x4 onesu = {0x3f803f80u, 0x3f803f80u, 0x3f803f80u, 0x3f803f80u};
    const unsigned vo = (lane >> 5) * 256 + (lane & 15) * 16;
    for (int t = 0; t < 64; ++t) {
        bf16x8 kf = *(const bf16x8*)(kg + t * 1024 + lane * 16);
        u32x4 v0 = *(const u32x4*)(vg + t * 1024 + vo), v1 = *(const u32x4*)(vg + t * 1024 + 512 + vo);
        if (!isv) { v0 = onesu; v1 = onesu; }
        f32x16 s = MFMA32(kf, q, c);
#pragma unroll
        for (int i = 0; i < 16; ++i) s[i] = __builtin_amdgcn_exp2f(s[i]);
        o = SMF(__builtin_bit_cast(bf16x8, v0), lo8(s), o);
        o = SMF(__builtin_bit_cast(bf16x8, v1), hi8(s), o);
    }
    o_out = o;
}

DI void att_store(const f32x16& o, char* dst, unsigned l16) {
    const float rl = __builtin_amdgcn_rcpf(o[8]);
    unsigned p0 = pk2(o[0] * rl, o[1] * rl), p1 = pk2(o[2] * rl, o[3] * rl), p2 = pk2(o[4] * rl, o[5] * rl), p3 = pk2(o[6] * rl, o[7] * rl);
    auto s0 = __builtin_amdgcn_permlane32_swap(p0, p2, false, false);
    auto s1 = __builtin_amdgcn_permlane32_swap(p1, p3, false, false);
    u32x4 v = {s0[0], s1[0], s0[1], s1[1]};
    GST16(l16, v, dst);
}
DI float att_mag(const f32x16& o) {
    return ((fabsf(o[0]) + fabsf(o[1])) + (fabsf(o[2]) + fabsf(o[3]))) + ((fabsf(o[4]) + fabsf(o[5])) + (fabsf(o[6]) + fabsf(o[7]))) + o[8];
}

DI void att_role(int head, int qb, int w, char* __restrict__ ws, unsigned lds0) {
    const int lane = threadIdx.x & 63;
    const int qh = w & 1, kh = w >> 1;
    const char* kg = ws + WS_K + (size_t)head * 65536;
    const char* vg = ws + WS_V + (size_t)head * 65536;
    const char* qg = ws + WS_Q + (size_t)head * 65536 + (size_t)(qb * 8 + qh * 4) * 1024;
    const unsigned l16 = lane * 16;
    const unsigned vo = (lane >> 5) * 256 + (lane & 15) * 16;
    u32x4 qr[4], k0, ks;
    STAMP_DECL; STAMP(0); STAMPC(14);
    const unsigned qo = l16 + kh * 2048, qo2 = l16 + (kh ^ 1) * 2048;
    GLD16(ks, l16, kg, 0);
    GLD16(qr[0], qo, qg, 0); GLD16(qr[1], qo, qg, 1024); GLD16(qr[2], qo2, qg, 0); GLD16(qr[3], qo2, qg, 1024);
    GLD16(k0, l16, kg + kh * 32768, 0);
    {
        const __amdgpu_buffer_rsrc_t kr = __builtin_amdgcn_make_buffer_rsrc((void*)(ws + WS_K), (short)0, 4u << 20, 0x00020000);
        const unsigned so = head * 65536 + (qh ? (unsigned)(WS_V - WS_K) : 1024u) + kh * 32768, d = lds0 + (qh ? K2_V + kh * 32768 : K2_K + kh * 31744);
        BLDS(kr, l16, so, 0, d, 0); BLDS(kr, l16, so, 1024, d, 1024); BLDS(kr, l16, so, 2048, d, 2048); BLDS(kr, l16, so, 3072, d, 3072); BLDS(kr, l16, so, 4096, d, 4096);
    }
    *(LAS unsigned*)(uintptr_t)(lds0 + K2_ONES + threadIdx.x * 4) = ((threadIdx.x >> 2) & 11) == 0 ? 0x3f803f80u : 0u;
    asm volatile("s_waitcnt lgkmcnt(0)\n\ts_barrier" ::: "memory");
    int sidx = (int)0xDDDDDDDDu; PIN(sidx);
    f32x16 oo[4] = {splat16(0.f), splat16(0.f), splat16(0.f), splat16(0.f)}, sA, sB = splat16(0.f);
    PIN(oo[0]); PIN(oo[1]); PIN(oo[2]); PIN(oo[3]); PIN(sB);
    unsigned kaddr = lds0 + K2_K + kh * 31744 + l16;
    unsigned vaddr = (lane & 16) ? (lds0 + K2_ONES + vo) : (lds0 + K2_V + kh * 32768 + vo);
    unsigned vinc = (lane & 16) ? 0u : 1024u;
    u32x4 e0 = {0x3f80u, 0u, 0u, 0u};
    const unsigned cb = lds0 + (qh ? K2_V : K2_K) + 20 * 1024 + l16, cst = qh ? 32768 : 31744;
    unsigned cwa = cb + kh * cst, cra = cb + (kh ^ 1) * cst;
    char* dst = ws + WS_CAT + ((size_t)(qb * 8 + qh * 4 + kh * 2) * CAT_KS + head) * 1024;
    char* cnt = ws + WS_FLAG + (size_t)(CNT_REC + (qb * 4 + qh * 2 + kh) * 8 + 4 + (head >> 3)) * CNT_STRIDE;
    PIN(kaddr); PIN(vaddr); PIN(vinc); PIN(e0); PIN(cwa); PIN(cra);
    asm volatile("" : "+s"(dst), "+s"(cnt));
    STAMP(1);
    asm volatile("s_waitcnt vmcnt(5)" : "+v"(qr[0]), "+v"(qr[1]), "+v"(qr[2]), "+v"(qr[3]), "+v"(k0), "+v"(ks) :: "memory");
    STAMP(2);
#define QF(i) __builtin_bit_cast(bf16x8, qr[i])
    f32x16 cc[4];
#pragma unroll
    for (int i = 0; i < 4; ++i) { cc[i] = MFMA32(__builtin_bit_cast(bf16x8, ks), QF(i), splat16(0.f)); PIN(cc[i]); SB(); }
#pragma unroll
    for (int i = 0; i < 4; ++i) {
        const float m = __builtin_fmaf(max16(cc[i]), -0.5f, -0.5f * REF_OFF);
        u32x4 mb = {__float_as_uint(m) >> 16, 0u, 0u, 0u};
        cc[i] = MFMA32(__builtin_bit_cast(bf16x8, e0), __builtin_bit_cast(bf16x8, mb), splat16(0.f));
        PIN(cc[i]);
    }
    bf16x8 kf = __builtin_bit_cast(bf16x8, k0);
    WAITV(0); lds_inc(lds0 + K2_MISC + SY_KVA);
    sA = MFMA32(kf, QF(0), cc[0]);
    lds_wait(lds0 + K2_MISC + SY_KVA, 4);
    bf16x8 v0 = ldsfrag(vaddr), v1 = v0;
    STAMP(3);
#define EXPS(X, a, b) do { _Pragma("unroll") for (int i_ = a; i_ < b; ++i_) X[i_] = __builtin_amdgcn_exp2f(X[i_]); PIN(X); SB(); } while (0)
#define HSTEP(X, Y, i, A1, A2, A3) do { \
        EXPS(X, 0, 2); \
        oo[(i + 3) & 3] = SMF(v1, hi8(Y), oo[(i + 3) & 3]); PIN(oo[(i + 3) & 3]); SB(); A1; SB(); \
        EXPS(X, 2, 5); \
        Y = MFMA32(kf, QF((i + 1) & 3), cc[(i + 1) & 3]); PIN(Y); SB(); A2; SB(); \
        EXPS(X, 5, 11); \
        oo[i] = SMF(v0, lo8(X), oo[i]); PIN(oo[i]); SB(); A3; SB(); \
        EXPS(X, 11, 16); \
    } while (0)
#pragma unroll 1
    for (int seg = 0; seg < 5; ++seg) {
        int n = 4;
        if (seg == 1) { n = 8; STAMP(4); lds_wait(lds0 + K2_MISC + SY_KVB, 1); STAMP(5); STAMPC(10); }
        else if (seg == 2) { n = 5; STAMP(6); STAMPC(11); lds_wait(lds0 + K2_MISC + SY_KVB, 3); STAMP(7); STAMPC(12); }
        else if (seg >= 3) {
            n = seg == 3 ? 10 : 5;
            asm volatile("s_waitcnt lgkmcnt(0)" ::: "memory");
            lds_inc(lds0 + K2_MISC + (seg == 3 ? SY_P16 : SY_P26));
        }
#pragma unroll 1
        for (int i = 0; i < n; ++i) {
            HSTEP(sA, sB, 0, { v1 = ldsfrag(vaddr + 512); vaddr += vinc; }, {}, {});
            HSTEP(sB, sA, 1, {}, {}, {});
            HSTEP(sA, sB, 2, {}, { kf = ldsfrag(kaddr); kaddr += 1024; }, {});
            HSTEP(sB, sA, 3, {}, {}, { v0 = ldsfrag(vaddr); });
        }
    }
    oo[3] = SMF(v1, hi8(sB), oo[3]);
    STAMP(8); STAMPC(13);
    __builtin_amdgcn_s_setprio(3);
    lds_wait(lds0 + K2_MISC + SY_P26, 4);
    asm volatile("" :: "v"(sidx));
#pragma unroll
    for (int i = 0; i < 2; ++i) {
        f32x4 a = {oo[2 + i][0], oo[2 + i][1], oo[2 + i][2], oo[2 + i][3]}, b = {oo[2 + i][4], oo[2 + i][5], oo[2 + i][6], oo[2 + i][7]};
        *(LAS f32x4*)(uintptr_t)(cwa + i * 2048) = a;
        *(LAS f32x4*)(uintptr_t)(cwa + i * 2048 + 1024) = b;
    }
    { f32x2 l2 = {oo[2][8], oo[3][8]}; *(LAS f32x2*)(uintptr_t)(cwa + 4096) = l2; }
    asm volatile("s_waitcnt lgkmcnt(0)" ::: "memory");
    lds_inc(lds0 + K2_MISC + SY_CMB + qh * 4);
    lds_wait(lds0 + K2_MISC + SY_CMB + qh * 4, 2);
    {
        const f32x2 l2 = *(LAS f32x2*)(uintptr_t)(cra + 4096);
#pragma unroll
        for (int i = 0; i < 2; ++i) {
            const f32x4 a = *(LAS f32x4*)(uintptr_t)(cra + i * 2048), b = *(LAS f32x4*)(uintptr_t)(cra + i * 2048 + 1024);
#pragma unroll
            for (int e = 0; e < 4; ++e) { oo[i][e] += a[e]; oo[i][4 + e] += b[e]; }
            oo[i][8] += l2[i];
            PIN(oo[i]);
        }
    }
    const bool ok = (att_mag(oo[0]) + att_mag(oo[1]) < 0x1p120f) & (fminf(oo[0][8], oo[1][8]) > 0x1p-80f);
    if (__builtin_expect(__builtin_amdgcn_ballot_w64(!ok) != 0, 0)) { att_exact(kg, vg, QF(0), oo[0]); att_exact(kg, vg, QF(1), oo[1]); }
    att_store(oo[0], dst, l16);
    att_store(oo[1], dst + CAT_KS * 1024, l16);
    publish(cnt);
    STAMP(9);
    STAMP_FLUSH(1, ws);
}

template <int CTRL> DI float dppf(float x) { return __builtin_bit_cast(float, __builtin_amdgcn_mov_dpp(__builtin_bit_cast(int, x), CTRL, 0xf, 0xf, true)); }
DI float fold32(float a, float b) {
    auto r = __builtin_amdgcn_permlane32_swap(__float_as_uint(a), __float_as_uint(b), false, false);
    return __uint_as_float(r[0]) + __uint_as_float(r[1]);
}
DI float fold16(float a, float b) {
    auto r = __builtin_amdgcn_permlane16_swap(__float_as_uint(a), __float_as_uint(b), false, false);
    return __uint_as_float(r[0]) + __uint_as_float(r[1]);
}
DI float rowsum16(float v) { v += dppf<0xB1>(v); v += dppf<0x4E>(v); v += dppf<0x124>(v); v += dppf<0x128>(v); return v; }

DI void spg_role(int b, int head, int w, const char* __restrict__ wsp, const __hip_bfloat16* __restrict__ bsp, char* __restrict__ ws, unsigned lds0,
                 const char* __restrict__ x, const char* __restrict__ wo, const char* __restrict__ wout, char* __restrict__ out) {
    const int lane = threadIdx.x & 63;
    const int xc = b & 7, rr = b >> 3;
    const int tt = xc * 4 + (rr & 3), ft = rr >> 2;
    const bool active = ft < 7;
    STAMP_DECL; STAMP(0); STAMPC(14);
    const unsigned wring = lds0 + K2_W + w * 8192;
    const __amdgpu_buffer_rsrc_t wr = __builtin_amdgcn_make_buffer_rsrc((void*)wsp, (short)0, S_ * S_ * 2, 0x00020000);
    unsigned wso = tt * 64 * 4096 + w * 1024;
    unsigned wvoff[2];
#pragma unroll
    for (int p = 0; p < 2; ++p) wvoff[p] = (lane >> 3) * 4096 + (((lane & 7) ^ ((4 * p + (lane >> 4)) & 7)) << 4);
#define ISSUEW(i) BLDS(wr, wvoff[(i) & 1], wso, (i) * 32768, wring, (i) * 1024)
    f32x16 acc[4][2];
    unsigned zvoff[4], waddr[4];
#define SETUP() do { _Pragma("unroll") for (int j = 0; j < 4; ++j) { \
            acc[j][0] = splat16(0.f); acc[j][1] = splat16(0.f); PIN(acc[j][0]); PIN(acc[j][1]); \
            zvoff[j] = (lane >> 5) * (FH * 16) + (j ^ w) * 512 + (lane & 31) * 16; PIN(zvoff[j]); \
            waddr[j] = wring + (lane & 31) * 128 + (((j * 2 + (lane >> 5)) ^ ((lane >> 1) & 7)) << 4); PIN(waddr[j]); } \
        PIN(wvoff[0]); PIN(wvoff[1]); } while (0)
    *(LAS unsigned*)(uintptr_t)(lds0 + K2_MISC + (threadIdx.x - 256) * 4) = 0u;
    if (w == 3) {
        const __amdgpu_buffer_rsrc_t kr = __builtin_amdgcn_make_buffer_rsrc((void*)(ws + WS_K), (short)0, 4u << 20, 0x00020000);
        const unsigned lo = lane * 16;
        unsigned kso = head * 65536 + 5 * 1024, kd = lds0 + 5 * 1024;
        SETUP();
        asm volatile("s_waitcnt lgkmcnt(0)\n\ts_barrier" ::: "memory");
#define KV4() do { BLDS(kr, lo, kso, 0, kd, K2_K - 1024); BLDS(kr, lo, kso, WS_V - WS_K, kd, K2_V); BLDS(kr, lo, kso, 32768, kd, K2_K + 31744 - 1024); \
                   BLDS(kr, lo, kso, WS_V - WS_K + 32768, kd, K2_V + 32768); kso += 1024; kd += 1024; } while (0)
#pragma unroll 1
        for (int t = 5; t < 13; ++t) KV4();
        unsigned so = tt * 64 * 4096, d = lds0 + K2_W;
#pragma unroll 1
        for (int g = 0; g < 4; ++g) {
            BLDS(wr, wvoff[0], so, 0, d, 0); BLDS(wr, wvoff[1], so, 32768, d, 1024); BLDS(wr, wvoff[0], so, 2 * 32768, d, 2048); BLDS(wr, wvoff[1], so, 3 * 32768, d, 3072);
            BLDS(wr, wvoff[0], so, 4 * 32768, d, 4096); BLDS(wr, wvoff[1], so, 5 * 32768, d, 5120); BLDS(wr, wvoff[0], so, 6 * 32768, d, 6144); BLDS(wr, wvoff[1], so, 7 * 32768, d, 7168);
            so += 1024; d += 8192;
            if (g == 2) WAITV(48);
        }
        WAITV(32);
        PRIO_HI(); lds_inc(lds0 + K2_MISC + SY_KVB); PRIO_LO();
#pragma unroll 1
        for (int t = 13; t < 20; ++t) KV4();
        WAITV(28);
        PRIO_HI(); lds_inc(lds0 + K2_MISC + SY_KVB); PRIO_LO();
#pragma unroll 1
        for (int t = 20; t < 32; ++t) { KV4(); WAITV(52); }
        WAITV(0);
        STAMP(13);
        PRIO_HI();
        lds_inc(lds0 + K2_MISC + SY_KVB);
    } else {
        const char* src = ws + WS_Z2 + (size_t)b * FH * 16;
        u32x4 zc[5], z0;
        const unsigned o0 = w * 1024 + lane * 16, o4 = o0 + (w < 2 ? 12288 : 9216);
        const unsigned zero = 0;
        GLD16(zc[0], o0, src, 0); GLD16(zc[1], o0, src, 3072); GLD16(zc[2], o0, src + 6144, 0); GLD16(zc[3], o0, src + 6144, 3072); GLD16(zc[4], o4, src, 0); GLD16(z0, zero, src, 0);
        asm volatile("s_waitcnt lgkmcnt(0)\n\ts_barrier" ::: "memory");
        SETUP();
        asm volatile("s_waitcnt vmcnt(0)" : "+v"(zc[0]), "+v"(zc[1]), "+v"(zc[2]), "+v"(zc[3]), "+v"(zc[4]), "+v"(z0) :: "memory");
        STAMP(1);
        PRIO_HI();
        char* dst = ws + WS_Z2N + (size_t)b * FH * 16;
        float sh[8], s1[8], s2[8];
#pragma unroll
        for (int e = 0; e < 4; ++e) { sh[2 * e] = bflo(z0[e]); sh[2 * e + 1] = bfhi(z0[e]); }
        const float m4 = w < 2 ? 1.f : 0.f;
#pragma unroll
        for (int e = 0; e < 8; ++e) {
            float v[5];
#pragma unroll
            for (int j = 0; j < 5; ++j) v[j] = ((e & 1) ? bfhi(zc[j][e >> 1]) : bflo(zc[j][e >> 1])) - sh[e];
            v[4] *= m4;
            s1[e] = ((v[0] + v[1]) + (v[2] + v[3])) + v[4];
            s2[e] = __builtin_fmaf(v[4], v[4], (v[0] * v[0] + v[1] * v[1]) + (v[2] * v[2] + v[3] * v[3]));
        }
        f32x4 q;
#pragma unroll
        for (int i = 0; i < 4; ++i) q[i] = rowsum16(fold16(fold32(s1[i], s2[i]), fold32(s1[i + 4], s2[i + 4])));
        LAS float* part = (LAS float*)(uintptr_t)(lds0 + K2_MISC + 256);
        *(LAS f32x4*)(part + w * 16 + (lane >> 4) * 4) = q;
        asm volatile("s_waitcnt lgkmcnt(0)" ::: "memory");
        lds_inc(lds0 + K2_MISC + SY_LN);
        lds_wait(lds0 + K2_MISC + SY_LN, 3);
        STAMP(2);
        float rs[8], nm[8];
        {
            f32x4 t[4];
#pragma unroll
            for (int i = 0; i < 4; ++i) t[i] = (*(LAS f32x4*)(part + i * 4) + *(LAS f32x4*)(part + 16 + i * 4)) + *(LAS f32x4*)(part + 32 + i * 4);
#pragma unroll
            for (int e = 0; e < 8; ++e) {
                float mean = t[e >> 2][e & 3] * (1.0f / FH);
                float var = fmaxf(t[2 + (e >> 2)][e & 3] * (1.0f / FH) - mean * mean, 0.f);
                rs[e] = __builtin_amdgcn_rsqf(var + 1e-5f);
                nm[e] = -(mean + sh[e]) * rs[e];
            }
        }
#pragma unroll
        for (int j = 0; j < 5; ++j) {
            u32x4 v;
#pragma unroll
            for (int e = 0; e < 4; ++e) v[e] = pk2(__builtin_fmaf(bflo(zc[j][e]), rs[2 * e], nm[2 * e]), __builtin_fmaf(bfhi(zc[j][e]), rs[2 * e + 1], nm[2 * e + 1]));
            if (j < 4) GST16(o0, v, dst + j * 3072); else GST16(o4, v, dst);
        }
        STAMP(3);
        WAITV(0);
        STAMP(4);
        unsigned old = 0;
        if (lane == 0) old = __hip_atomic_fetch_add((LAS unsigned*)(uintptr_t)(lds0 + K2_MISC + SY_LNST), 1u, __ATOMIC_RELAXED, __HIP_MEMORY_SCOPE_WORKGROUP);
        old = __builtin_amdgcn_readfirstlane(old);
        if (old == 2) {
            if (lane < 16) cnt_add(ws, lane * 8 + (b >> 5));
            if (!active) { WAITV(0); lds_inc(lds0 + K2_MISC + SY_FLAG); }
        }
    }
    const unsigned l16 = lane * 16;
    auto reg = [&](int ww) { return lds0 + ((ww & 1) ? K2_V + (ww >> 1) * 32768 : K2_K + (ww >> 1) * 31744); };
    auto poll = [&](int first, int n, unsigned cnt) {
        const unsigned fo = (first + lane) * CNT_STRIDE;
        const char* fl = ws + WS_FLAG;
#pragma unroll 1
        for (int it = 0; it < SPIN_MAX; ++it) {
            unsigned f = cnt;
            if (lane < n) { const unsigned z = 0; asm volatile("global_atomic_add %0, %1, %2, %3 sc0\n\ts_waitcnt vmcnt(0)" : "=&v"(f) : "v"(fo), "v"(z), "s"(fl) : "memory"); }
            if (__all(f == cnt)) break;
            __builtin_amdgcn_s_sleep(2);
        }
    };
    if (active) {
        const char* zb = ws + WS_Z2N + ((size_t)w * 64 * FH + ft * 128) * 16;
        u32x4 zf[4][4];
#define ZPTR(c, ks) (zb + (size_t)((c) * 4 + (ks)) * (2 * FH * 16))
        poll(((b >> 3) & 15) * 8 + w * 2, 2, 32);
        STAMP(5);
        PRIO_LO();
#pragma unroll
        for (int ks = 0; ks < 4; ++ks)
#pragma unroll
            for (int j = 0; j < 4; ++j) GLD16S(zf[ks][j], zvoff[j], ZPTR(0, ks));
        PRIO_HI();
        lds_wait(lds0 + K2_MISC + SY_KVB, 2);
        PRIO_LO();
#define ZWAIT(n, ks) asm volatile("s_waitcnt vmcnt(" #n ")" : "+v"(zf[ks][0]), "+v"(zf[ks][1]), "+v"(zf[ks][2]), "+v"(zf[ks][3]) :: "memory")
#define KPAIR(ks, j_, MORE) do { \
        acc[j_][0] = MFMA32(__builtin_bit_cast(bf16x8, zf[ks][j_]), wf[0][ks], acc[j_][0]); PIN(acc[j_][0]); SB(); \
        if (MORE && ks == 0) ISSUEW(2 * j_); \
        acc[j_][1] = MFMA32(__builtin_bit_cast(bf16x8, zf[ks][j_]), wf[1][ks], acc[j_][1]); PIN(acc[j_][1]); SB(); \
        if (MORE && ks == 0) ISSUEW(2 * j_ + 1); \
        if (MORE) GLD16S(zf[ks][j_], zvoff[j_], ZPTR(c + 1, ks)); } while (0)
#define KSTEP(ks, MORE) do { KPAIR(ks, 0, MORE); KPAIR(ks, 1, MORE); KPAIR(ks, 2, MORE); KPAIR(ks, 3, MORE); } while (0)
#define WREAD() _Pragma("unroll") for (int ks = 0; ks < 4; ++ks) { wf[0][ks] = ldsfrag(waddr[ks]); wf[1][ks] = ldsfrag(waddr[ks] + 4096); } \
        asm volatile("s_waitcnt lgkmcnt(0)" : "+v"(wf[0][0]), "+v"(wf[0][1]), "+v"(wf[0][2]), "+v"(wf[0][3]), "+v"(wf[1][0]), "+v"(wf[1][1]), "+v"(wf[1][2]), "+v"(wf[1][3]) :: "memory")
#pragma unroll 1
        for (int c = 0; c < 7; ++c) {
            bf16x8 wf[2][4];
            wso += 128;
            ZWAIT(12, 0);
            WREAD();
            KSTEP(0, true);
            ZWAIT(20, 1); KSTEP(1, true);
            ZWAIT(20, 2); KSTEP(2, true);
            ZWAIT(20, 3); KSTEP(3, true);
        }
        const int fs0 = ft * 8 + w * 2;
        const char* z1p = ws + WS_Z1 + ((size_t)tt * 2 * 56 + fs0) * 1024;
        const char* bp = (const char*)bsp + tt * 128;
        const unsigned bo = (lane & 31) * 2;
        unsigned braw[2];
        {
            const int c = 7; (void)c;
            bf16x8 wf[2][4];
            ZWAIT(12, 0);
            WREAD();
            PRIO_HI(); lds_inc(lds0 + K2_MISC + SY_MAIN); PRIO_LO();
            KSTEP(0, false);
            GLD16(zf[0][0], l16, z1p, 0); GLD16(zf[0][1], l16, z1p, 1024); GLD16(zf[0][2], l16, z1p + 56 * 1024, 0); GLD16(zf[0][3], l16, z1p + 56 * 1024, 1024);
            asm volatile("global_load_ushort %0, %1, %2" : "=v"(braw[0]) : "v"(bo), "s"(bp) : "memory");
            asm volatile("global_load_ushort %0, %1, %2 offset:64" : "=v"(braw[1]) : "v"(bo), "s"(bp) : "memory");
            ZWAIT(14, 1); KSTEP(1, false);
            ZWAIT(10, 2); KSTEP(2, false);
            ZWAIT(6, 3); KSTEP(3, false);
        }
        STAMP(6);
        PRIO_HI();
        lds_wait(lds0 + K2_MISC + SY_P16, 4);
        lds_wait(lds0 + K2_MISC + SY_MAIN, 4);
        auto blk = [&](int dstw, int j) { return (j < 3 ? reg(dstw) + (j - 1) * 8192 : lds0 + K2_W + dstw * 8192) + l16; };
        unsigned wa[3] = {blk(1 ^ w, 1), blk(2 ^ w, 2), blk(3 ^ w, 3)};
        PIN(wa[0]); PIN(wa[1]); PIN(wa[2]); SB();
        PRIO_LO();
#pragma unroll
        for (int j = 1; j < 4; ++j) {
            const unsigned a = wa[j - 1];
#pragma unroll
            for (int t = 0; t < 2; ++t)
#pragma unroll
                for (int q = 0; q < 4; ++q) {
                    f32x4 v = {acc[j][t][4 * q], acc[j][t][4 * q + 1], acc[j][t][4 * q + 2], acc[j][t][4 * q + 3]};
                    *(LAS f32x4*)(uintptr_t)(a + (t * 4 + q) * 1024) = v;
                }
        }
        asm volatile("s_waitcnt lgkmcnt(0)" ::: "memory");
        PRIO_HI();
        lds_inc(lds0 + K2_MISC + SY_RED);
        lds_wait(lds0 + K2_MISC + SY_RED, 4);
#pragma unroll
        for (int j = 1; j < 4; ++j) {
            const unsigned a = blk(w, j);
#pragma unroll
            for (int t = 0; t < 2; ++t)
#pragma unroll
                for (int q = 0; q < 4; ++q) {
                    f32x4 v = *(LAS f32x4*)(uintptr_t)(a + (t * 4 + q) * 1024);
#pragma unroll
                    for (int e = 0; e < 4; ++e) acc[0][t][4 * q + e] += v[e];
                }
        }
        asm volatile("s_waitcnt vmcnt(0)" : "+v"(zf[0][0]), "+v"(zf[0][1]), "+v"(zf[0][2]), "+v"(zf[0][3]), "+v"(braw[0]), "+v"(braw[1]) :: "memory");
        const float bias[2] = {bflo(braw[0]), bflo(braw[1])};
#pragma unroll
        for (int t = 0; t < 2; ++t)
#pragma unroll
            for (int s = 0; s < 2; ++s) {
                auto a0 = __builtin_amdgcn_permlane32_swap(zf[0][2 * t + s][0], zf[0][2 * t + s][2], false, false);
                auto a1 = __builtin_amdgcn_permlane32_swap(zf[0][2 * t + s][1], zf[0][2 * t + s][3], false, false);
                const unsigned zc[4] = {a0[0], a1[0], a0[1], a1[1]};
                unsigned p[4];
#pragma unroll
                for (int e = 0; e < 4; ++e)
                    p[e] = pk2(bflo(zc[e]) * (acc[0][t][8 * s + 2 * e] + bias[t]), bfhi(zc[e]) * (acc[0][t][8 * s + 2 * e + 1] + bias[t]));
                auto s0 = __builtin_amdgcn_permlane32_swap(p[0], p[2], false, false);
                auto s1 = __builtin_amdgcn_permlane32_swap(p[1], p[3], false, false);
                u32x4 v = {s0[0], s1[0], s0[1], s1[1]};
                GST16(l16, v, ws + WS_CAT + ((size_t)(tt * 2 + t) * CAT_KS + 32 + fs0 + s) * 1024);
            }
        publish(ws + WS_FLAG + (size_t)(CNT_REC + tt * 8 + (ft * 4 + w) / 7) * CNT_STRIDE);
        STAMP(7);
    } else {
        const int id = (rr & 3) * 4 + w;
        lds_wait<4>(lds0 + K2_MISC + SY_KVB, 3);
        lds_wait<4>(lds0 + K2_MISC + SY_FLAG, 1);
        PRIO_LO();
#pragma unroll 1
        for (int c = 1; c < 8; ++c) { wso += 128; ISSUEW(0); ISSUEW(1); ISSUEW(2); ISSUEW(3); ISSUEW(4); ISSUEW(5); ISSUEW(6); ISSUEW(7); WAITV(32); }
        lds_wait<32>(lds0 + K2_MISC + SY_P16, 4);
#pragma unroll 1
        for (int i = 0; i < 56; ++i) { glds16(wout + (size_t)(id * 56 + i) * 1024, l16, wring); if ((i & 7) == 7) WAITV(32); }
#pragma unroll 1
        for (int i = 0; i < 32; ++i) { glds16(wo + (size_t)(id * 32 + i) * 1024, l16, wring); if ((i & 7) == 7) WAITV(32); }
#pragma unroll 1
        for (int i = 0; i < 16; ++i) glds16(x + (size_t)(xc * 256 + id * 16 + i) * 1024, l16, wring);
        WAITV(0);
        PRIO_HI();
    }
    {
        const __amdgpu_buffer_rsrc_t hr = __builtin_amdgcn_make_buffer_rsrc((void*)wout, (short)0, FH * 1024, 0x00020000);
        const __amdgpu_buffer_rsrc_t orr = __builtin_amdgcn_make_buffer_rsrc((void*)wo, (short)0, D_ * 1024, 0x00020000);
        unsigned bvoff = ((lane >> 2) & 7) * 1024 + (lane >> 5) * 64 + (lane & 3) * 16;
        const unsigned hso = ft * 128 + w * 14 * 16384, oso = ft * 128 + w * 8 * 16384;
        const unsigned rg = reg(w);
#define FB(rs, so, s, base, off) do { BLDS(rs, bvoff, so, (s) * 16384, base, off); BLDS(rs, bvoff, so, (s) * 16384 + 8192, base, (off) + 1024); } while (0)
        const unsigned bl = (lane >> 5) * 1024 + (w & 1) * 512 + ((lane & 15) >> 2) * 64 + ((lane >> 4) & 1) * 32 + (lane & 3) * 8;
        unsigned br0 = rg + bl, br1 = rg + (bl ^ 512), bw0 = wring + bl, bw1 = wring + (bl ^ 512);
        unsigned xo = (lane & 31) * 1024 + (lane >> 5) * 16;
        PIN(bvoff); PIN(br0); PIN(br1); PIN(bw0); PIN(bw1); PIN(xo); SB();
        PRIO_LO();
        FB(hr, hso, 0, rg, 0); FB(hr, hso, 1, rg, 2048); FB(hr, hso, 2, rg, 4096); FB(hr, hso, 3, rg, 6144);
        FB(hr, hso, 4, rg, 8192); FB(hr, hso, 5, rg, 10240); FB(hr, hso, 6, rg, 12288); FB(hr, hso, 7, rg, 14336);
        FB(hr, hso, 8, wring, 0); FB(hr, hso, 9, wring, 2048); FB(hr, hso, 10, wring, 4096); FB(hr, hso, 11, wring, 6144);
        const size_t oo_ = (size_t)(tt * 64 + (w >> 1) * 32) * 1024 + (ft * 64 + (w & 1) * 32) * 2;
        u32x4 xv[2];
        GLD16(xv[0], xo, x + oo_, 0); GLD16(xv[1], xo, x + oo_, 32);
        PRIO_HI();
        lds_wait<8>(lds0 + K2_MISC + SY_P26, 4);
        FB(hr, hso, 12, rg, 16384); FB(hr, hso, 13, rg, 18432);
        const char* a0p = ws + WS_CAT + (size_t)(tt * 2 + (w >> 1)) * CAT_KS * 1024;
        const char* a1p = ws + WS_CAT + (size_t)(tt * 2 + ((w >> 1) ^ 1)) * CAT_KS * 1024;
        u32x4 af[14][2];
        f32x16 fc[2][2];
        bf16x8 bq[2][2];
#define SLOT0(s) ((s) < 8 ? br0 + (s) * 2048 : (s) < 12 ? bw0 + ((s) - 8) * 2048 : br0 + 16384 + ((s) - 12) * 2048)
#define SLOT1(s) ((s) < 8 ? br1 + (s) * 2048 : (s) < 12 ? bw1 + ((s) - 8) * 2048 : br1 + 16384 + ((s) - 12) * 2048)
#define BQ(s) do { bq[(s) & 1][0] = trfrag(SLOT0(s), SLOT0(s) + 256); bq[(s) & 1][1] = trfrag(SLOT1(s), SLOT1(s) + 256); } while (0)
#define AWAIT(n, s) asm volatile("s_waitcnt vmcnt(%c2)" : "+v"(af[s][0]), "+v"(af[s][1]) : "i"(n) : "memory")
#define FMM(s, FIRST) do { _Pragma("unroll") for (int i_ = 0; i_ < 2; ++i_) _Pragma("unroll") for (int j_ = 0; j_ < 2; ++j_) { \
            fc[i_][j_] = MFMA32(bq[(s) & 1][j_], __builtin_bit_cast(bf16x8, af[s][i_]), (FIRST) ? splat16(0.f) : fc[i_][j_]); PIN(fc[i_][j_]); } SB(); } while (0)
        poll(CNT_REC + tt * 8 + w, 1, 7);
        STAMP(8);
        PRIO_LO();
#pragma unroll
        for (int s = 0; s < 14; ++s) { GLD16S(af[s][0], l16, a0p + (32 + w * 14 + s) * 1024); GLD16S(af[s][1], l16, a1p + (32 + w * 14 + s) * 1024); }
        BQ(0);
#define HST(s) do { if (s < 13) BQ(s + 1); AWAIT(2 * (13 - s) + (s > 8 ? 16 : 0), s); FMM(s, s == 0); } while (0)
        HST(0); HST(1); HST(2); HST(3); HST(4); HST(5); HST(6); HST(7); HST(8);
        FB(orr, oso, 0, rg, 0); FB(orr, oso, 1, rg, 2048); FB(orr, oso, 2, rg, 4096); FB(orr, oso, 3, rg, 6144);
        FB(orr, oso, 4, rg, 8192); FB(orr, oso, 5, rg, 10240); FB(orr, oso, 6, rg, 12288); FB(orr, oso, 7, rg, 14336);
        HST(9); HST(10); HST(11); HST(12); HST(13);
        STAMP(9);
        PRIO_HI();
        poll(CNT_REC + tt * 8 + 4 + w, 1, 8);
        STAMP(10);
#pragma unroll
        for (int s = 0; s < 8; ++s) { GLD16S(af[s][0], l16, a0p + (w * 8 + s) * 1024); GLD16S(af[s][1], l16, a1p + (w * 8 + s) * 1024); }
        BQ(0);
#define OST(s) do { if (s < 7) BQ(s + 1); AWAIT(2 * (7 - s), s); FMM(s, false); } while (0)
        OST(0); OST(1); OST(2); OST(3); OST(4); OST(5); OST(6); OST(7);
        STAMP(11);
#pragma unroll
        for (int j = 1; j < 4; ++j)
#pragma unroll
            for (int q = 0; q < 4; ++q) {
                const f32x16& t = fc[j >> 1][j & 1];
                f32x4 v = {t[4 * q], t[4 * q + 1], t[4 * q + 2], t[4 * q + 3]};
                *(LAS f32x4*)(uintptr_t)(rg + (j * 4 + q) * 1024 + l16) = v;
            }
        asm volatile("s_waitcnt lgkmcnt(0)" ::: "memory");
        lds_inc(lds0 + K2_MISC + SY_FIN);
        lds_wait(lds0 + K2_MISC + SY_FIN, 4);
#pragma unroll
        for (int j = 1; j < 4; ++j)
#pragma unroll
            for (int q = 0; q < 4; ++q) {
                f32x4 v = *(LAS f32x4*)(uintptr_t)(reg(w ^ j) + (j * 4 + q) * 1024 + l16);
#pragma unroll
                for (int e = 0; e < 4; ++e) fc[0][0][4 * q + e] += v[e];
            }
        asm volatile("" : "+v"(xv[0]), "+v"(xv[1]));
#pragma unroll
        for (int s = 0; s < 2; ++s) {
            auto a0 = __builtin_amdgcn_permlane32_swap(xv[s][0], xv[s][2], false, false);
            auto a1 = __builtin_amdgcn_permlane32_swap(xv[s][1], xv[s][3], false, false);
            const unsigned xc4[4] = {a0[0], a1[0], a0[1], a1[1]};
            unsigned p[4];
#pragma unroll
            for (int e = 0; e < 4; ++e) p[e] = pk2(bflo(xc4[e]) + fc[0][0][8 * s + 2 * e], bfhi(xc4[e]) + fc[0][0][8 * s + 2 * e + 1]);
            auto s0 = __builtin_amdgcn_permlane32_swap(p[0], p[2], false, false);
            auto s1 = __builtin_amdgcn_permlane32_swap(p[1], p[3], false, false);
            u32x4 v = {s0[0], s1[0], s0[1], s1[1]};
            *(u32x4*)(out + oo_ + xo + s * 32) = v;
        }
    }
    STAMP(12);
    STAMP_FLUSH(1, ws);
}

__global__ __launch_bounds__(512, 2) NOPK void k2_kernel(const char* __restrict__ wsp, const __hip_bfloat16* __restrict__ bsp, char* __restrict__ ws,
                                                    const char* __restrict__ x, const char* __restrict__ wo, const char* __restrict__ wout, char* __restrict__ out) {
    extern __shared__ __attribute__((aligned(16))) char smem[];
    const unsigned lds0 = (unsigned)(uintptr_t)(LAS char*)smem;
    const int wid = __builtin_amdgcn_readfirstlane(threadIdx.x >> 6);
    const int b = blockIdx.x;
    const int head = (b & 7) * 4 + (b >> 6);
    if (wid < 4) att_role(head, (b >> 3) & 7, wid, ws, lds0);
    else spg_role(b, head, wid - 4, wsp, bsp, ws, lds0, x, wo, wout, out);
}

extern "C" void kernel_launch(void* const* d_in, const int* in_sizes, int n_in, void* d_out, int out_size, void* d_ws, size_t ws_size,
                              hipStream_t stream) {
    const char* x = (const char*)d_in[0];
    const char* wqkv = (const char*)d_in[1];
    const char* wo = (const char*)d_in[2];
    const char* win = (const char*)d_in[4];
    const char* wsp = (const char*)d_in[5];
    const __hip_bfloat16* bsp = (const __hip_bfloat16*)d_in[6];
    const char* wout = (const char*)d_in[7];
    char* ws = (char*)d_ws;
    (void)hipFuncSetAttribute((const void*)k1_kernel, hipFuncAttributeMaxDynamicSharedMemorySize, K1_LDS);
    (void)hipFuncSetAttribute((const void*)k2_kernel, hipFuncAttributeMaxDynamicSharedMemorySize, K2_LDS);
    hipLaunchKernelGGL(k1_kernel, dim3(256), dim3(512), K1_LDS, stream, x, wqkv, win, ws, wsp);
    hipLaunchKernelGGL(k2_kernel, dim3(256), dim3(512), K2_LDS, stream, wsp, bsp, ws, x, wo, wout, (char*)d_out);
}
```

```cpp
#include <hip/hip_runtime.h>
#include <hip/hip_bf16.h>
#include <stdint.h>

typedef __attribute__((ext_vector_type(2))) __bf16 bf16x2;
typedef __attribute__((ext_vector_type(8))) __bf16 bf16x8;
typedef __attribute__((ext_vector_type(16))) __bf16 bf16x16;
typedef __attribute__((ext_vector_type(2))) float f32x2;
typedef __attribute__((ext_vector_type(4))) float f32x4;
typedef __attribute__((ext_vector_type(8))) float f32x8;
typedef __attribute__((ext_vector_type(16))) float f32x16;
typedef __attribute__((ext_vector_type(2))) unsigned u32x2;
typedef __attribute__((ext_vector_type(4))) unsigned u32x4;
typedef __attribute__((ext_vector_type(4))) short s16x4;
#define LAS __attribute__((address_space(3)))
#define DI __device__ __forceinline__
#if defined(__HIP_DEVICE_COMPILE__)
#define NOPK __attribute__((target("no-packed-fp32-ops")))
#else
#define NOPK
#endif
#define SB() __builtin_amdgcn_sched_barrier(0)
#define PIN(x) asm volatile("" : "+v"(x))
#define WAITV_(n) asm volatile("s_waitcnt vmcnt(" #n ")" ::: "memory")
#define WAITV(n) WAITV_(n)
#define MFMA16(a, b, c) __builtin_amdgcn_mfma_f32_16x16x32_bf16(a, b, c, 0, 0, 0)
#define MFMA32(a, b, c) __builtin_amdgcn_mfma_f32_32x32x16_bf16(a, b, c, 0, 0, 0)

constexpr int S_ = 2048, D_ = 512, FH = 896;
constexpr size_t WS_Q = 0, WS_K = 2u << 20, WS_V = 4u << 20, WS_Z1 = 6u << 20, WS_Z2 = 10u << 20, WS_Z2N = 14u << 20,
                 WS_CAT = 18u << 20, WS_FLAG = 24u << 20;
constexpr int CNT_STRIDE = 256, CNT_REC = 128, CNT_QKV = 384, CNT_Z2 = 416, CNT_Z1 = 432, CNT_HB = 448;
constexpr int CAT_KS = 88;

#define STAMP_DECL
#define STAMP(i)
#define STAMPC(i)
#define STAMP_FLUSH(kern, wsp_)

#define STFLAG "sc1"
#define GST16P(voff, val, sbase) asm volatile("global_store_dwordx4 %0, %1, %2\n\ts_nop 1" :: "v"(voff), "v"(val), "s"(sbase) : "memory")
#define GST16(voff, val, sbase) asm volatile("global_store_dwordx4 %0, %1, %2 " STFLAG "\n\ts_nop 1" :: "v"(voff), "v"(val), "s"(sbase) : "memory")

#define GLD16(dst, voff, sbase, IMM) asm volatile("global_load_dwordx4 %0, %1, %2 offset:" #IMM : "=v"(dst) : "v"(voff), "s"(sbase) : "memory")

DI void glds16(const void* sbase, unsigned voff, unsigned ldsdst) {
    unsigned keep;
    asm volatile("s_mov_b32 %0, m0\n\ts_mov_b32 m0, %3\n\ts_nop 0\n\tglobal_load_lds_dwordx4 %1, %2\n\ts_mov_b32 m0, %0"
                 : "=&s"(keep) : "v"(voff), "s"(sbase), "s"(ldsdst) : "memory");
}
DI unsigned pk2(float a, float b) { f32x2 v = {a, b}; return __builtin_bit_cast(unsigned, __builtin_convertvector(v, bf16x2)); }
DI float bflo(unsigned u) { return __uint_as_float(u << 16); }
DI float bfhi(unsigned u) { return __uint_as_float(u & 0xffff0000u); }
DI s16x4 trread(unsigned addr) { return __builtin_amdgcn_ds_read_tr16_b64_v4i16((LAS s16x4*)(uintptr_t)addr); }
DI bf16x8 trfrag(unsigned addr0, unsigned addr1) {
    s16x4 lo = trread(addr0), hi = trread(addr1);
    return __builtin_bit_cast(bf16x8, __builtin_shufflevector(lo, hi, 0, 1, 2, 3, 4, 5, 6, 7));
}
DI bf16x8 ldsfrag(unsigned addr) { return *(const LAS bf16x8*)(uintptr_t)addr; }
DI float gelu_t(float u) {
    const float k1 = -2.3022081985f;
    float w = u * __builtin_fmaf(u * u, k1 * 0.044715f, k1);
    float e = __builtin_amdgcn_exp2f(w);
    return u * __builtin_amdgcn_rcpf(1.0f + e);
}

constexpr int K2_K = 0, K2_V = 62 * 1024, K2_W = 126 * 1024, K2_ONES = K2_W + 32768, K2_MISC = K2_ONES + 1024, K2_LDS = K2_MISC + 1024;
enum { SY_KVA = 0, SY_KVB = 4, SY_KVC = 8, SY_P26 = 12, SY_LN = 16, SY_LNST = 20, SY_RED = 24, SY_FLAG = 28, SY_CMB = 36  , SY_P16 = 44, SY_MAIN = 48, SY_FIN = 52, SY_QKV = 56, SY_Z2 = 60, SY_HB = 128   };
constexpr int SPIN_MAX = 1 << 20;

#define GLD16C(dst, voff, sbase, IMM) asm volatile("global_load_dwordx4 %0, %1, %2 offset:" #IMM " sc1" : "=v"(dst) : "v"(voff), "s"(sbase) : "memory")
DI void hb_sample(const char* p, unsigned ldsdst) {
    unsigned t;
    asm volatile("v_mov_b32 %0, 0\n\ts_mov_b32 m0, %2\n\ts_mov_b64 exec, 1\n\tglobal_load_lds_dword %0, %1 sc1\n\ts_mov_b64 exec, -1" : "=&v"(t) : "s"(p), "s"(ldsdst) : "memory");
}
DI void publish(char* p) {
    unsigned t0, t1;
    asm volatile("v_mov_b32 %0, 0\n\tv_mov_b32 %1, 1\n\ts_waitcnt vmcnt(0)\n\ts_mov_b64 exec, 1\n\tglobal_atomic_add %0, %1, %2\n\ts_mov_b64 exec, -1" : "=&v"(t0), "=&v"(t1) : "s"(p) : "memory");
}
DI void add16(char* p, unsigned stride_shift) {
    unsigned t0, t1;
    asm volatile("v_mbcnt_lo_u32_b32 %0, -1, 0\n\tv_mov_b32 %1, 1\n\tv_lshlrev_b32 %0, %3, %0\n\ts_mov_b64 exec, 0xffff\n\tglobal_atomic_add %0, %1, %2\n\ts_mov_b64 exec, -1"
                 : "=&v"(t0), "=&v"(t1) : "s"(p), "s"(stride_shift) : "memory");
}

constexpr int K1_MISC = 9 * 16384;
constexpr int K1_SUM = K1_MISC;
constexpr int K1_CS = K1_MISC + 1024;
constexpr int K1_PRIV = K1_MISC + 1024 + 2048;
constexpr int K1_JUNK = K1_PRIV + 8 * 256;
constexpr int K1_LDS = K1_JUNK + 1024;
enum { EPI_Q = 0, EPI_K = 1, EPI_V = 2, EPI_Z1 = 3, EPI_Z2 = 4 };
DI f32x16 splat16(float v) { f32x16 r; for (int i = 0; i < 16; ++i) r[i] = v; return r; }

#define BLDS(rsrc, voff, soff, SIMM, m0b, MIMM) do { unsigned t_; \
    asm volatile("s_add_u32 m0, %4, %c6\n\ts_add_u32 %0, %3, %c5\n\tbuffer_load_dwordx4 %1, %2, %0 offen lds" \
                 : "=&s"(t_) : "v"(voff), "s"(rsrc), "s"(soff), "s"(m0b), "i"(SIMM), "i"(MIMM) : "scc", "memory"); } while (0)

#define BLDSC(rsrc, voff, soff, SIMM, m0b, MIMM) do { unsigned t_; \
    asm volatile("s_add_u32 m0, %4, %c6\n\ts_add_u32 %0, %3, %c5\n\tbuffer_load_dwordx4 %1, %2, %0 offen sc1 lds" \
                 : "=&s"(t_) : "v"(voff), "s"(rsrc), "s"(soff), "s"(m0b), "i"(SIMM), "i"(MIMM) : "scc", "memory"); } while (0)
#define ROTK ((blockIdx.x >> 6) + (blockIdx.x >> 3))
#define LWIN 52
template <bool ORI2, bool GELU>
DI void k1_body(const char* __restrict__ x, const char* __restrict__ W, unsigned col0  , int np  , int rp, char* __restrict__ dst  ,
                unsigned dvoff, unsigned jstride, unsigned pstride, float oscale, char* __restrict__ ws, unsigned lds0, const char* __restrict__ wsp) {
    constexpr int LDW = GELU ? 3584 : 3072, NP = GELU ? 3 : 4;
    const int tid = threadIdx.x, lane = tid & 63;
    const int wid = __builtin_amdgcn_readfirstlane(tid >> 6);
    const int wm = wid & 3, wn = wid >> 2;
    const int r = lane & 31, h = lane >> 5;
    const unsigned rot = ROTK;
    const bool leader = blockIdx.x == 0 && wid == 1;
    char* hb = ws + WS_FLAG + CNT_HB * CNT_STRIDE;
    STAMP_DECL; STAMP(0); STAMPC(14);
    const __amdgpu_buffer_rsrc_t xr = __builtin_amdgcn_make_buffer_rsrc((void*)x, (short)0, S_ * D_ * 2, 0x00020000);
    const __amdgpu_buffer_rsrc_t wr = __builtin_amdgcn_make_buffer_rsrc((void*)W, (short)0, D_ * LDW, 0x00020000);
    unsigned xso[4], wso[4];
#pragma unroll
    for (int s = 0; s < 4; ++s) { const unsigned k = (s + rot) & 3; xso[s] = rp * 131072 + wid * 16384 + k * 256; wso[s] = col0 + (wid * 16 + k * 128) * LDW; }
    unsigned xvoff[2];
#pragma unroll
    for (int i = 0; i < 2; ++i) xvoff[i] = (i * 8 + (lane >> 3)) * 1024 + (((lane & 7) ^ ((4 * i + (lane >> 4)) & 7)) << 4);
    const unsigned wvoff = ((lane >> 2) & 7) * LDW + (lane >> 5) * 64 + (lane & 3) * 16;
    const unsigned sdst = lds0 + wid * 2048;
#define ISSX1(c, slot, i) BLDS(xr, xvoff[i], xso[(c) >> 1], ((c) & 1) * 128, sdst, (slot) * 16384 + (i) * 1024)
#define ISSW1(pp, st, slot, i) BLDS(wr, wvoff, wso[st], (pp) * 128 + (i) * 8 * LDW, sdst, (slot) * 16384 + (i) * 1024)
#define ISSX(c, slot) do { ISSX1(c, slot, 0); ISSX1(c, slot, 1); } while (0)
#define ISSW(pp, st, slot) do { ISSW1(pp, st, slot, 0); ISSW1(pp, st, slot, 1); } while (0)
    ISSX(0, 0); ISSX(1, 1); ISSW(0, 0, 2);
    ISSX(2, 3); ISSX(3, 4); ISSW(0, 1, 5);
    STAMP(1);
#define PERM(q) ((q) == 0 ? 0 : (q) == 1 ? 4 : (q) < 5 ? (q) - 1 : (q))
    unsigned xad[4], bad[4];
#pragma unroll
    for (int t = 0; t < 4; ++t) {
        xad[t] = lds0 + (wm * 32 + r) * 128 + (((2 * (t ^ wm) + h) ^ ((r >> 1) & 7)) << 4);
        bad[t] = lds0 + (2 * (t ^ wm) + h) * 1024 + wn * 512 + ((lane & 15) >> 2) * 64 + ((lane >> 4) & 1) * 32 + (lane & 3) * 8;
    }
    const unsigned selw = ((lane & 15) == ((lane >> 4) & 1)) ? 0x3f803f80u : 0u;
    u32x4 selu = {selw, selw, selw, selw};
    const bf16x8 sel = __builtin_bit_cast(bf16x8, selu);
    bf16x8 af[32];
    f32x16 accA, accB;
    f32x16 gram;
    f32x4 csum, rsum = {0, 0, 0, 0};
    const f32x4 z4 = {0, 0, 0, 0};
#define MM(bf, a, c) (ORI2 ? MFMA32(a, bf, c) : MFMA32(bf, a, c))
#define MM16(bf, a) asm volatile("v_mfma_f32_32x32x16_bf16 %0, %1, %2, %3" : "+v"(accB) : "v"(ORI2 ? (a) : (bf)), "v"(ORI2 ? (bf) : (a)), "v"(accA))
#define BFR(q, off) trfrag(bad[PERM(q) & 3] + (off) + (PERM(q) >> 2) * 8192, bad[PERM(q) & 3] + (off) + (PERM(q) >> 2) * 8192 + 256)
#define MAIN(i, bf) do { if ((i) == 0) accA = MM(bf, af[i], splat16(0.f)); else if ((i) < 16) accA = MM(bf, af[i], accA); else if ((i) == 16) MM16(bf, af[i]); else accB = MM(bf, af[i], accB); \
        if ((i) < 16) PIN(accA); else PIN(accB); \
        if (((i) & 7) < 2) { csum = MFMA16(sel, bf, (i) == 0 ? z4 : csum); PIN(csum); } } while (0)
#define READS0(st, s0) _Pragma("unroll") for (int q = 0; q < 8; ++q) { af[st * 8 + q] = ldsfrag(xad[PERM(q) & 3] + (s0 + (PERM(q) >> 2)) * 16384); bf[q] = BFR(q, (s0 + 2) * 16384); } SB()
#define G32(k) "v_mfma_f32_32x32x16_bf16 %0, %" #k ", %" #k ", %0\n\t"
#define G16(k) "v_mfma_f32_16x16x32_bf16 %1, %2, %" #k ", %1\n\t"
#define STATS(st) do { asm volatile("s_cmp_lg_u32 %3, 0\n\ts_cbranch_scc1 1f\n\t" G32(4) G32(5) G32(6) G32(7) G32(8) G32(9) G32(10) G32(11) "s_branch 2f\n1:\n\t" \
            G16(4) G16(5) G16(6) G16(7) G16(8) G16(9) G16(10) G16(11) "2:" : "+v"(gram), "+v"(rsum) : "v"(sel), "s"(wn), "v"(af[st * 8]), "v"(af[st * 8 + 1]), "v"(af[st * 8 + 2]), \
            "v"(af[st * 8 + 3]), "v"(af[st * 8 + 4]), "v"(af[st * 8 + 5]), "v"(af[st * 8 + 6]), "v"(af[st * 8 + 7]) : "scc"); SB(); } while (0)
#define BAR(n) asm volatile("s_waitcnt vmcnt(" #n ") lgkmcnt(0)\n\ts_barrier" ::: "memory")
    gram = splat16(0.f); PIN(gram); PIN(rsum);
    asm volatile("" : "=v"(accB));
    const unsigned csw = lds0 + K1_CS + wn * 512 + (lane & 15) * 16 + wm * 4;
    const unsigned csr = lds0 + K1_CS + wn * 512 + r * 16;
    const unsigned priv = lds0 + K1_PRIV + wid * 256;
#define CSWRITE(par) do { if (lane < 16) { *(LAS float*)(uintptr_t)(csw + (par)) = csum[0]; *(LAS float*)(uintptr_t)(csw + (par) + 256) = csum[1]; } } while (0)
    bf16x8 bq[4];
    {
        bf16x8 bf[8];
        BAR(6); STAMP(2);
        READS0(0, 0);
        ISSX(4, 6);
        const unsigned pso = col0 + ((rp & 7) * 64 + wid * 8) * LDW, junk = lds0 + K1_JUNK;
#pragma unroll
        for (int q = 0; q < 8; ++q) {
            MAIN(q, bf[q]);
            if (q == 0) ISSX1(5, 7, 0); if (q == 1) ISSX1(5, 7, 1); if (q == 2) ISSW1(0, 2, 8, 0); if (q == 3) ISSW1(0, 2, 8, 1);
            if (q == 4) BLDS(wr, wvoff, pso, 128, junk, 0); if (q == 5) BLDS(wr, wvoff, pso, 256, junk, 0); if (q == 6) BLDS(wr, wvoff, pso, (NP - 1) * 128, junk, 0);
            if (q == 7) {
                const __amdgpu_buffer_rsrc_t sr = __builtin_amdgcn_make_buffer_rsrc((void*)wsp, (short)0, S_ * S_ * 2, 0x00020000);
                const unsigned so = ((blockIdx.x & 7) * 32 + (blockIdx.x >> 3)) * 32768 + wm * 1024 + wn * 128, so_v = (lane >> 3) * 4096 + (lane & 7) * 16;
                BLDS(sr, so_v, so, 0, junk, 0);
            }
            SB();
        }
        BAR(10); STAMP(3);
        READS0(1, 3);
        ISSX(6, 0); STATS(0);
#pragma unroll
        for (int q = 0; q < 8; ++q) { MAIN(8 + q, bf[q]); if (q == 0) ISSX1(7, 1, 0); if (q == 2) ISSX1(7, 1, 1); if (q == 4) ISSW1(0, 3, 2, 0); if (q == 6) ISSW1(0, 3, 2, 1); SB(); }
        BAR(10); STAMP(4);
        READS0(2, 6);
        ISSW(1, 0, 3); STATS(1);
#pragma unroll
        for (int q = 0; q < 8; ++q) { MAIN(16 + q, bf[q]); if (q == 0) ISSW1(1, 1, 4, 0); if (q == 2) ISSW1(1, 1, 4, 1); if (q == 4) ISSW1(1, 2, 5, 0); if (q == 6) ISSW1(1, 2, 5, 1); SB(); }
        BAR(6); STAMP(5);
        READS0(3, 0);
        ISSW(1, 3, 6); STATS(2); STATS(3);
        MAIN(24, bf[0]); MAIN(25, bf[1]); MAIN(26, bf[2]); SB();
        asm volatile("s_nop 7\n\ts_nop 7" : "+v"(gram), "+v"(rsum));
        if (wn == 0) {
            auto G = [&](int j) { float v = gram[j]; PIN(v); return v; };
            auto Q4 = [&](int j) { const float a0 = G(j), a1 = G(j + 1), a2 = G(j + 2), a3 = G(j + 3); const float u0 = (r & 1) ? a1 : a0, u1 = (r & 1) ? a3 : a2; return (r & 2) ? u1 : u0; };
            const float t0 = Q4(0), t1 = Q4(4), t2 = Q4(8), t3 = Q4(12);
            const float u0 = (r & 8) ? t1 : t0, u1 = (r & 8) ? t3 : t2;
            const float dg = (r & 16) ? u1 : u0;
            if (((r >> 2) & 1) == h) *(LAS float*)(uintptr_t)(lds0 + K1_SUM + (wm * 32 + r) * 4) = dg;
        } else if (lane < 16) {
            *(LAS float*)(uintptr_t)(lds0 + K1_SUM + 512 + (wm * 32 + lane) * 4) = rsum[0];
            *(LAS float*)(uintptr_t)(lds0 + K1_SUM + 512 + (wm * 32 + 16 + lane) * 4) = rsum[1];
        }
        CSWRITE(0);
        SB();
        MAIN(27, bf[3]); MAIN(28, bf[4]); SB();
        BAR(0); STAMP(6);
        if (leader) add16(hb, 8);
#pragma unroll
        for (int t = 0; t < 4; ++t) bad[t] += 3 * 16384;
        bq[0] = BFR(0, 0); MAIN(29, bf[5]); SB();
        bq[1] = BFR(1, 0); MAIN(30, bf[6]); SB();
        bq[2] = BFR(2, 0); MAIN(31, bf[7]); SB();
    }
    float rs, nm;
    {
        const float ss = *(LAS float*)(uintptr_t)(lds0 + K1_SUM + (wm * 32 + r) * 4), sm = *(LAS float*)(uintptr_t)(lds0 + K1_SUM + 512 + (wm * 32 + r) * 4);
        const float mean = sm * (1.0f / 512.0f);
        const float var = fmaxf(ss * (1.0f / 512.0f) - mean * mean, 0.0f);
        rs = __builtin_amdgcn_rsqf(var + 1e-5f) * oscale;
        nm = -mean * rs;
        if (ORI2) *(LAS float*)(uintptr_t)(priv + h * 128 + r * 4) = h ? nm : rs;
    }
    f32x4 ea[2], eb[2], c4; float cs, ylo; unsigned pk[4];
#define EPI_BEGIN0(par) c4 = *(LAS f32x4*)(uintptr_t)(csr + (par))
#define EPI_BEGIN1() do { cs = (c4[0] + c4[1]) + (c4[2] + c4[3]); PIN(cs); if (!ORI2) *(LAS float*)(uintptr_t)(priv + r * 4) = cs; } while (0)
#define EPI_LOAD(q) do { ea[(q) & 1] = *(LAS f32x4*)(uintptr_t)(priv + (q) * 32 + h * 16); if (ORI2) eb[(q) & 1] = *(LAS f32x4*)(uintptr_t)(priv + 128 + (q) * 32 + h * 16); } while (0)
#define EPI_ELEM(V, e) do { float y_ = ORI2 ? __builtin_fmaf(eb[((e) >> 2) & 1][(e) & 3], cs, (V) * ea[((e) >> 2) & 1][(e) & 3]) : __builtin_fmaf(nm, ea[((e) >> 2) & 1][(e) & 3], (V) * rs); \
        if (GELU) y_ = gelu_t(y_); if ((e) & 1) { pk[((e) >> 1) & 3] = pk2(ylo, y_); PIN(pk[((e) >> 1) & 3]); } else { ylo = y_; PIN(ylo); } } while (0)
#define EPI_STORE(j, PLAIN) do { auto s0_ = __builtin_amdgcn_permlane32_swap(pk[0], pk[2], false, false); auto s1_ = __builtin_amdgcn_permlane32_swap(pk[1], pk[3], false, false); \
        u32x4 v_ = {s0_[0], s1_[0], s0_[1], s1_[1]}; if (PLAIN) GST16P(dvoff, v_, dst + (j) * jstride); else GST16(dvoff, v_, dst + (j) * jstride); } while (0)
    EPI_BEGIN0(0);
    unsigned par = 0, rslot = 3, wslot = 7;
#pragma unroll 1
    for (int p = 1; p < np; ++p) {
        const bool more = p + 1 < np;
#pragma unroll
        for (int s = 0; s < 4; ++s) wso[s] = more ? wso[s] + 128 : 0x40000000u;
        unsigned wd[4];
#pragma unroll
        for (int s = 0; s < 4; ++s) { wd[s] = sdst + wslot * 16384; wslot = wslot == 8 ? 0 : wslot + 1; }
        float hi[8];
        SB();
#pragma unroll
        for (int i = 0; i < 32; ++i) {
            if (i == 29) {
                par ^= 1024;
                if (more) WAITV(2);
                asm volatile("s_waitcnt lgkmcnt(0)\n\ts_barrier" ::: "memory");
                if (p == 1) { STAMP(7); STAMPC(10); } else if (p == 2) { STAMP(8); STAMPC(11); } else STAMP(9);
                EPI_BEGIN0(par);
                if (leader) add16(hb, 8);
                if (wid == 0 && !more) hb_sample(hb + (blockIdx.x & 15) * CNT_STRIDE, lds0 + K2_MISC + SY_HB + 4);
            }
            if (((i + 3) & 7) == 0) {
                const unsigned d = rslot == 8 ? (unsigned)(-8 * 16384) : 16384u;
                rslot = rslot == 8 ? 0 : rslot + 1;
#pragma unroll
                for (int t = 0; t < 4; ++t) bad[t] += d;
            }
            bq[(i + 3) & 3] = BFR((i + 3) & 7, 0);
            if (i >= 4 && i < 12) asm volatile("v_mov_b32 %0, %1" : "=&v"(hi[i - 4]) : "v"(accB[8 + i - 4]));
            if (i == 0) EPI_BEGIN1();
            if (i == 1 || i == 7 || i == 13 || i == 19) EPI_LOAD((i - 1) / 6);
            if (i >= 4 && i < 28 && (i - 4) % 3 != 2) { const int e = ((i - 4) / 3) * 2 + (i - 4) % 3; EPI_ELEM(e < 8 ? accB[e & 7] : hi[e & 7], e); }
            MAIN(i, bq[i & 3]);
            if (i < 16 && !(i & 1)) BLDS(wr, wvoff, wso[i >> 2], 128 + ((i >> 1) & 1) * 8 * LDW, wd[i >> 2], ((i >> 1) & 1) * 1024);
            if (i == 15 || i == 27) EPI_STORE(i == 27, false);
            if (i == 27) CSWRITE(par ^ 1024);
            SB();
        }
        dst += pstride;
    }
    EPI_BEGIN1();
    EPI_LOAD(0);
#pragma unroll
    for (int q = 0; q < 4; ++q) {
        if (q < 3) EPI_LOAD(q + 1);
#pragma unroll
        for (int e = 0; e < 4; ++e) EPI_ELEM(accB[q * 4 + e], q * 4 + e);
        if (q & 1) EPI_STORE(q >> 1, false);
    }
    STAMP(12);
    WAITV(0);
    STAMP(13);
    STAMP_FLUSH(0, ws);
}

DI void phase1(const char* __restrict__ x, const char* __restrict__ wqkv, const char* __restrict__ win, char* __restrict__ ws, const char* __restrict__ wsp, unsigned lds0) {
    const int b = blockIdx.x, xc = b & 7, rr = b >> 3;
    const int rp = (xc & 1) * 8 + (rr & 7), ct = (xc >> 1) * 4 + (rr >> 3);
    const int lane = threadIdx.x & 63, wid = __builtin_amdgcn_readfirstlane(threadIdx.x >> 6);
    const int rt = rp * 4 + (wid & 3), wn = wid >> 2;
    const char* hbw = ws + WS_FLAG + (CNT_HB + (b & 15)) * CNT_STRIDE;
    if (wid == 0) hb_sample(hbw, lds0 + K2_MISC + SY_HB);
    if (b == 0 && wid == 1) {
#pragma unroll
        for (int i = 0; i < 7; ++i) __hip_atomic_store((unsigned*)(ws + WS_FLAG + (size_t)(i * 64 + lane) * CNT_STRIDE), 0u, __ATOMIC_RELAXED, __HIP_MEMORY_SCOPE_AGENT);
    }
    if (threadIdx.x < 256) *(LAS unsigned*)(uintptr_t)(lds0 + K2_ONES + threadIdx.x * 4) = ((threadIdx.x >> 2) & 11) == 0 ? 0x3f803f80u : 0u;
    else if (threadIdx.x < 256 + 32) *(LAS unsigned*)(uintptr_t)(lds0 + K2_MISC + (threadIdx.x - 256) * 4) = 0u;
    if (ct < 4) {
        const int head0 = (ct & 1) * 16 + wn * 2;
        k1_body<false, false>(x, wqkv, ct * 512, 4, rp, ws + (ct < 2 ? WS_Q : WS_K) + ((size_t)head0 * 64 + rt) * 1024, lane * 16, 65536, 4 * 65536,
                              ct < 2 ? 0.25f * 1.4426950408889634f : 1.0f, ws, lds0, wsp);
    } else if (ct < 6) {
        const int head0 = (ct & 1) * 16 + wn * 2;
        k1_body<true, false>(x, wqkv, ct * 512, 4, rp, ws + WS_V + ((size_t)head0 * 64 + rt) * 1024, ((lane >> 4) & 1) * 65536 + (lane >> 5) * 256 + (lane & 15) * 16, 512, 4 * 65536,
                             1.0f, ws, lds0, wsp);
    } else if (ct < 11) {
        const int c0 = (ct - 6) * 192;
        k1_body<false, true>(x, win, c0 * 2, ct == 10 ? 2 : 3, rp, ws + WS_Z1 + ((size_t)rt * 56 + (c0 >> 4) + wn * 2) * 1024, lane * 16, 1024, 4096, 1.0f, ws, lds0, wsp);
    } else {
        const int c0 = (ct - 11) * 192;
        k1_body<true, true>(x, win, (FH + c0) * 2, ct == 15 ? 2 : 3, rp, ws + WS_Z2 + ((size_t)rt * 4 * FH + c0 + wn * 32) * 16, (lane >> 5) * (FH * 16) + (lane & 31) * 16, 2 * FH * 16, 1024,
                            1.0f, ws, lds0, wsp);
    }
    if (wid == 0) {
        const unsigned h0 = __builtin_amdgcn_readfirstlane(*(volatile LAS unsigned*)(uintptr_t)(lds0 + K2_MISC + SY_HB));
#pragma unroll 1
        for (int it = 0; it < SPIN_MAX && __builtin_amdgcn_readfirstlane(*(volatile LAS unsigned*)(uintptr_t)(lds0 + K2_MISC + SY_HB + 4)) == h0; ++it) {
            __builtin_amdgcn_s_sleep(8);
            hb_sample(hbw, lds0 + K2_MISC + SY_HB + 4);
            WAITV(0);
        }
    }
    asm volatile("s_waitcnt lgkmcnt(0)\n\ts_barrier" ::: "memory");
    if (wid == 7) {
        if (ct < 6) add16(ws + WS_FLAG + (size_t)(CNT_QKV + (ct & 1) * 16) * CNT_STRIDE, 8);
        else publish(ws + WS_FLAG + (size_t)((ct < 11 ? CNT_Z1 : CNT_Z2) + rp) * CNT_STRIDE);
    }
}

constexpr float REF_OFF = 64.0f;

DI void lds_inc(unsigned addr) {
    unsigned t0, t1;
    asm volatile("v_mov_b32 %0, %2\n\tv_mov_b32 %1, 1\n\ts_mov_b64 exec, 1\n\tds_add_u32 %0, %1\n\ts_mov_b64 exec, -1" : "=&v"(t0), "=&v"(t1) : "s"(addr) : "memory");
}
template <int SLEEP = 0> DI void lds_wait(unsigned addr, unsigned n) {
    unsigned t0, t1, c, v;
    if (SLEEP == 0)
        asm volatile("v_mov_b32 %0, %4\n\ts_mov_b32 %2, %6\n1:\n\tds_read_b32 %1, %0\n\ts_sub_u32 %2, %2, 1\n\ts_waitcnt lgkmcnt(0)\n\tv_readfirstlane_b32 %3, %1\n\ts_nop 0\n\t"
                     "s_cselect_b32 %3, %5, %3\n\ts_cmp_lt_u32 %3, %5\n\ts_cbranch_scc1 1b"
                     : "=&v"(t0), "=&v"(t1), "=&s"(c), "=&s"(v) : "s"(addr), "s"(n), "i"(SPIN_MAX) : "scc", "memory");
    else
        asm volatile("v_mov_b32 %0, %4\n\ts_mov_b32 %2, %6\n\ts_branch 2f\n1:\n\ts_sleep %c7\n2:\n\tds_read_b32 %1, %0\n\ts_sub_u32 %2, %2, 1\n\ts_waitcnt lgkmcnt(0)\n\tv_readfirstlane_b32 %3, %1\n\ts_nop 0\n\t"
                     "s_cselect_b32 %3, %5, %3\n\ts_cmp_lt_u32 %3, %5\n\ts_cbranch_scc1 1b"
                     : "=&v"(t0), "=&v"(t1), "=&s"(c), "=&s"(v) : "s"(addr), "s"(n), "i"(SPIN_MAX), "i"(SLEEP) : "scc", "memory");
}
DI bf16x16 lo8(const f32x16& x) { f32x8 t = __builtin_shufflevector(x, x, 0, 1, 2, 3, 4, 5, 6, 7); return __builtin_bit_cast(bf16x16, t); }
DI bf16x16 hi8(const f32x16& x) { f32x8 t = __builtin_shufflevector(x, x, 8, 9, 10, 11, 12, 13, 14, 15); return __builtin_bit_cast(bf16x16, t); }
#define PRIO_HI() __builtin_amdgcn_s_setprio(2)
#define PRIO_LO() __builtin_amdgcn_s_setprio(0)
#define SMF(a, b, c) __builtin_amdgcn_smfmac_f32_32x32x32_bf16(a, b, c, sidx, 0, 0)
#define GLD16S(dst, voff, sbase) asm volatile("global_load_dwordx4 %0, %1, %2 sc1" : "=v"(dst) : "v"(voff), "s"(sbase) : "memory")

DI float max16(const f32x16& s) {
    float a = fmaxf(fmaxf(s[0], s[1]), s[2]), b = fmaxf(fmaxf(s[3], s[4]), s[5]);
    a = fmaxf(fmaxf(a, s[6]), s[7]); b = fmaxf(fmaxf(b, s[8]), s[9]);
    a = fmaxf(fmaxf(a, s[10]), s[11]); b = fmaxf(fmaxf(b, s[12]), s[13]);
    a = fmaxf(fmaxf(a, s[14]), s[15]);
    return fmaxf(a, b);
}
DI float xhalf_max(float m) {
    auto rr = __builtin_amdgcn_permlane32_swap(__float_as_uint(m), __float_as_uint(m), false, false);
    return fmaxf(__uint_as_float(rr[0]), __uint_as_float(rr[1]));
}

DI u32x4 ldc(const char* p, unsigned voff) { u32x4 v; asm volatile("global_load_dwordx4 %0, %1, %2 sc1\n\ts_waitcnt vmcnt(0)" : "=&v"(v) : "v"(voff), "s"(p) : "memory"); return v; }
DI void att_exact(const char* kg, const char* vg, bf16x8 q, f32x16& o_out) {
    const int lane = threadIdx.x & 63;
    const int sidx = (int)0xDDDDDDDDu;
    f32x16 z = splat16(0.f);
    float m = -INFINITY;
    for (int t = 0; t < 64; ++t) {
        bf16x8 kf = __builtin_bit_cast(bf16x8, ldc(kg + t * 1024, lane * 16));
        f32x16 s = MFMA32(kf, q, z);
        m = fmaxf(m, max16(s));
    }
    m = xhalf_max(m);
    f32x16 c = splat16(-m), o = z;
    const bool isv = (lane & 16) == 0;
    u32x4 onesu = {0x3f803f80u, 0x3f803f80u, 0x3f803f80u, 0x3f803f80u};
    const unsigned vo = (lane >> 5) * 256 + (lane & 15) * 16;
    for (int t = 0; t < 64; ++t) {
        bf16x8 kf = __builtin_bit_cast(bf16x8, ldc(kg + t * 1024, lane * 16));
        u32x4 v0 = ldc(vg + t * 1024, vo), v1 = ldc(vg + t * 1024 + 512, vo);
        if (!isv) { v0 = onesu; v1 = onesu; }
        f32x16 s = MFMA32(kf, q, c);
#pragma unroll
        for (int i = 0; i < 16; ++i) s[i] = __builtin_amdgcn_exp2f(s[i]);
        o = SMF(__builtin_bit_cast(bf16x8, v0), lo8(s), o);
        o = SMF(__builtin_bit_cast(bf16x8, v1), hi8(s), o);
    }
    o_out = o;
}

DI void att_store(const f32x16& o, char* dst, unsigned l16) {
    const float rl = __builtin_amdgcn_rcpf(o[8]);
    unsigned p0 = pk2(o[0] * rl, o[1] * rl), p1 = pk2(o[2] * rl, o[3] * rl), p2 = pk2(o[4] * rl, o[5] * rl), p3 = pk2(o[6] * rl, o[7] * rl);
    auto s0 = __builtin_amdgcn_permlane32_swap(p0, p2, false, false);
    auto s1 = __builtin_amdgcn_permlane32_swap(p1, p3, false, false);
    u32x4 v = {s0[0], s1[0], s0[1], s1[1]};
    GST16(l16, v, dst);
}
DI float att_mag(const f32x16& o) {
    return ((fabsf(o[0]) + fabsf(o[1])) + (fabsf(o[2]) + fabsf(o[3]))) + ((fabsf(o[4]) + fabsf(o[5])) + (fabsf(o[6]) + fabsf(o[7]))) + o[8];
}

DI void att_role(int head, int qb, int w, char* __restrict__ ws, unsigned lds0) {
    const int lane = threadIdx.x & 63;
    const int qh = w & 1, kh = w >> 1;
    const char* kg = ws + WS_K + (size_t)head * 65536;
    const char* vg = ws + WS_V + (size_t)head * 65536;
    const char* qg = ws + WS_Q + (size_t)head * 65536 + (size_t)(qb * 8 + qh * 4) * 1024;
    const unsigned l16 = lane * 16;
    const unsigned vo = (lane >> 5) * 256 + (lane & 15) * 16;
    u32x4 qr[4], k0, ks;
    STAMP_DECL; STAMP(0); STAMPC(14);
    const unsigned qo = l16 + kh * 2048, qo2 = l16 + (kh ^ 1) * 2048;
    int sidx = (int)0xDDDDDDDDu; PIN(sidx);
    f32x16 oo[4] = {splat16(0.f), splat16(0.f), splat16(0.f), splat16(0.f)}, ss[4];
    ss[3] = splat16(0.f);
    u32x4 v0 = {0, 0, 0, 0}, v1 = {0, 0, 0, 0};
    asm volatile("" : "+{v[0:15]}"(oo[0]), "+{v[16:31]}"(oo[1]), "+{v[32:47]}"(oo[2]), "+{v[48:63]}"(oo[3]), "=&{v[144:159]}"(ss[1]), "=&{v[160:175]}"(ss[2]), "+{v[176:191]}"(ss[3]),
                      "+{v[212:215]}"(v0), "+{v[216:219]}"(v1));
    unsigned kaddr = lds0 + K2_K + kh * 31744 + l16;
    unsigned vaddr = (lane & 16) ? (lds0 + K2_ONES + vo) : (lds0 + K2_V + kh * 32768 + vo);
    unsigned vinc = (lane & 16) ? 0u : 1024u;
    u32x4 e0 = {0x3f80u, 0u, 0u, 0u};
    const unsigned cb = lds0 + (qh ? K2_V : K2_K) + 20 * 1024 + l16, cst = qh ? 32768 : 31744;
    unsigned cwa = cb + kh * cst, cra = cb + (kh ^ 1) * cst;
    char* dst = ws + WS_CAT + ((size_t)(qb * 8 + qh * 4 + kh * 2) * CAT_KS + head) * 1024;
    char* cnt = ws + WS_FLAG + (size_t)(CNT_REC + (qb * 4 + qh * 2 + kh) * 8 + 4 + (head >> 3)) * CNT_STRIDE;
    PIN(kaddr); PIN(vaddr); PIN(vinc); PIN(e0); PIN(cwa); PIN(cra);
    asm volatile("" : "+s"(dst), "+s"(cnt));
    lds_wait<1>(lds0 + K2_MISC + SY_QKV, 1);
    GLD16C(ks, l16, kg, 0);
    GLD16C(qr[0], qo, qg, 0); GLD16C(qr[1], qo, qg, 1024); GLD16C(qr[2], qo2, qg, 0); GLD16C(qr[3], qo2, qg, 1024);
    GLD16C(k0, l16, kg + kh * 32768, 0);
    {
        const __amdgpu_buffer_rsrc_t kr = __builtin_amdgcn_make_buffer_rsrc((void*)(ws + WS_K), (short)0, 4u << 20, 0x00020000);
        const unsigned so = head * 65536 + (qh ? (unsigned)(WS_V - WS_K) : 1024u) + kh * 32768, d = lds0 + (qh ? K2_V + kh * 32768 : K2_K + kh * 31744);
        BLDSC(kr, l16, so, 0, d, 0); BLDSC(kr, l16, so, 1024, d, 1024); BLDSC(kr, l16, so, 2048, d, 2048); BLDSC(kr, l16, so, 3072, d, 3072); BLDSC(kr, l16, so, 4096, d, 4096);
    }
    STAMP(1);
    asm volatile("s_waitcnt vmcnt(5)" : "+v"(qr[0]), "+v"(qr[1]), "+v"(qr[2]), "+v"(qr[3]), "+v"(k0), "+v"(ks) :: "memory");
    STAMP(2);
#define QF(i) __builtin_bit_cast(bf16x8, qr[i])
    f32x16 cc[4];
#pragma unroll
    for (int i = 0; i < 4; ++i) { cc[i] = MFMA32(__builtin_bit_cast(bf16x8, ks), QF(i), splat16(0.f)); PIN(cc[i]); SB(); }
#pragma unroll
    for (int i = 0; i < 4; ++i) {
        const float m = __builtin_fmaf(max16(cc[i]), -0.5f, -0.5f * REF_OFF);
        u32x4 mb = {__float_as_uint(m) >> 16, 0u, 0u, 0u};
        cc[i] = MFMA32(__builtin_bit_cast(bf16x8, e0), __builtin_bit_cast(bf16x8, mb), splat16(0.f));
        PIN(cc[i]);
    }
    u32x4 kf = k0;
    WAITV(0); lds_inc(lds0 + K2_MISC + SY_KVA);
    asm volatile("v_mfma_f32_32x32x16_bf16 v[128:143], v[208:211], v[192:195], v[64:79]" : "=&{v[128:143]}"(ss[0]) : "{v[208:211]}"(kf), "{v[192:195]}"(qr[0]), "{v[64:79]}"(cc[0]));
    lds_wait(lds0 + K2_MISC + SY_KVA, 4);
    STAMP(3);
#pragma unroll 1
    for (int seg = 0; seg < 5; ++seg) {
        int n = 3;
        if (seg == 1) { n = 7; STAMP(4); lds_wait(lds0 + K2_MISC + SY_KVB, 1); STAMP(5); STAMPC(10); }
        else if (seg == 2) { n = 4; STAMP(6); STAMPC(11); lds_wait(lds0 + K2_MISC + SY_KVB, 2); STAMP(7); STAMPC(12); }
        else if (seg >= 3) { n = seg == 3 ? 9 : 4; lds_inc(lds0 + K2_MISC + (seg == 3 ? SY_P16 : SY_P26)); }
        asm volatile("s_nop 7\n\t.p2align 6\n1:\n\t"
            "v_mfma_f32_32x32x16_bf16 v[144:159], v[208:211], v[196:199], v[80:95]\n\tv_exp_f32 v128, v128\n\tv_exp_f32 v129, v129\n\tv_exp_f32 v130, v130\n\t"
            "v_exp_f32 v131, v131\n\tv_smfmac_f32_32x32x32_bf16 v[48:63], v[212:215], v[176:183], v220\n\tds_read_b128 v[212:215], v222\n\tv_exp_f32 v132, v132\n\t"
            "v_exp_f32 v133, v133\n\tv_exp_f32 v134, v134\n\tv_exp_f32 v135, v135\n\tv_smfmac_f32_32x32x32_bf16 v[48:63], v[216:219], v[184:191], v220\n\t"
            "ds_read_b128 v[216:219], v222 offset:512\n\tv_exp_f32 v136, v136\n\tv_exp_f32 v137, v137\n\tv_exp_f32 v138, v138\n\tv_exp_f32 v139, v139\n\t"
            "v_exp_f32 v140, v140\n\tv_exp_f32 v141, v141\n\tv_exp_f32 v142, v142\n\tv_exp_f32 v143, v143\n\t"
            "v_mfma_f32_32x32x16_bf16 v[160:175], v[208:211], v[200:203], v[96:111]\n\tv_exp_f32 v144, v144\n\tv_exp_f32 v145, v145\n\tv_exp_f32 v146, v146\n\t"
            "v_exp_f32 v147, v147\n\ts_waitcnt lgkmcnt(1)\n\tv_smfmac_f32_32x32x32_bf16 v[0:15], v[212:215], v[128:135], v220\n\tv_exp_f32 v148, v148\n\t"
            "v_exp_f32 v149, v149\n\tv_exp_f32 v150, v150\n\tv_exp_f32 v151, v151\n\ts_waitcnt lgkmcnt(0)\n\t"
            "v_smfmac_f32_32x32x32_bf16 v[0:15], v[216:219], v[136:143], v220\n\tv_add_u32 v222, v222, v223\n\tv_exp_f32 v152, v152\n\tv_exp_f32 v153, v153\n\t"
            "v_exp_f32 v154, v154\n\tv_exp_f32 v155, v155\n\tv_exp_f32 v156, v156\n\tv_exp_f32 v157, v157\n\tv_exp_f32 v158, v158\n\tv_exp_f32 v159, v159\n\t"
            "v_mfma_f32_32x32x16_bf16 v[176:191], v[208:211], v[204:207], v[112:127]\n\tds_read_b128 v[208:211], v221\n\tv_exp_f32 v160, v160\n\t"
            "v_exp_f32 v161, v161\n\tv_exp_f32 v162, v162\n\tv_exp_f32 v163, v163\n\tv_smfmac_f32_32x32x32_bf16 v[16:31], v[212:215], v[144:151], v220\n\t"
            "v_exp_f32 v164, v164\n\tv_exp_f32 v165, v165\n\tv_exp_f32 v166, v166\n\tv_exp_f32 v167, v167\n\t"
            "v_smfmac_f32_32x32x32_bf16 v[16:31], v[216:219], v[152:159], v220\n\tv_add_u32 v221, 0x400, v221\n\tv_exp_f32 v168, v168\n\tv_exp_f32 v169, v169\n\t"
            "v_exp_f32 v170, v170\n\tv_exp_f32 v171, v171\n\tv_exp_f32 v172, v172\n\tv_exp_f32 v173, v173\n\tv_exp_f32 v174, v174\n\tv_exp_f32 v175, v175\n\t"
            "s_waitcnt lgkmcnt(0)\n\tv_mfma_f32_32x32x16_bf16 v[128:143], v[208:211], v[192:195], v[64:79]\n\tv_exp_f32 v176, v176\n\tv_exp_f32 v177, v177\n\t"
            "v_exp_f32 v178, v178\n\tv_exp_f32 v179, v179\n\tv_smfmac_f32_32x32x32_bf16 v[32:47], v[212:215], v[160:167], v220\n\tv_exp_f32 v180, v180\n\t"
            "v_exp_f32 v181, v181\n\tv_exp_f32 v182, v182\n\tv_exp_f32 v183, v183\n\tv_smfmac_f32_32x32x32_bf16 v[32:47], v[216:219], v[168:175], v220\n\t"
            "v_exp_f32 v184, v184\n\tv_exp_f32 v185, v185\n\tv_exp_f32 v186, v186\n\tv_exp_f32 v187, v187\n\tv_exp_f32 v188, v188\n\tv_exp_f32 v189, v189\n\t"
            "s_sub_u32 %0, %0, 1\n\tv_exp_f32 v190, v190\n\tv_exp_f32 v191, v191\n\t"
            "s_cbranch_scc0 1b"
            : "+s"(n), "+{v[0:15]}"(oo[0]), "+{v[16:31]}"(oo[1]), "+{v[32:47]}"(oo[2]), "+{v[48:63]}"(oo[3]), "+{v[128:143]}"(ss[0]), "+{v[144:159]}"(ss[1]), "+{v[160:175]}"(ss[2]), "+{v[176:191]}"(ss[3]), "+{v[208:211]}"(kf), "+{v[212:215]}"(v0), "+{v[216:219]}"(v1), "+{v221}"(kaddr), "+{v222}"(vaddr)
            : "{v[64:79]}"(cc[0]), "{v[80:95]}"(cc[1]), "{v[96:111]}"(cc[2]), "{v[112:127]}"(cc[3]), "{v[192:195]}"(qr[0]), "{v[196:199]}"(qr[1]), "{v[200:203]}"(qr[2]), "{v[204:207]}"(qr[3]), "{v220}"(sidx), "{v223}"(vinc)
            : "scc", "memory");
    }
    asm volatile("v_smfmac_f32_32x32x32_bf16 v[48:63], v[212:215], v[176:183], v220\n\ts_nop 0\n\tv_smfmac_f32_32x32x32_bf16 v[48:63], v[216:219], v[184:191], v220\n\ts_nop 7\n\ts_nop 7"
            : "+{v[48:63]}"(oo[3]) : "{v[176:191]}"(ss[3]), "{v[212:215]}"(v0), "{v[216:219]}"(v1), "{v220}"(sidx));
    STAMP(8); STAMPC(13);
    __builtin_amdgcn_s_setprio(3);
    lds_wait(lds0 + K2_MISC + SY_P26, 4);
#pragma unroll
    for (int i = 0; i < 2; ++i) {
        f32x4 a = {oo[2 + i][0], oo[2 + i][1], oo[2 + i][2], oo[2 + i][3]}, b = {oo[2 + i][4], oo[2 + i][5], oo[2 + i][6], oo[2 + i][7]};
        *(LAS f32x4*)(uintptr_t)(cwa + i * 2048) = a;
        *(LAS f32x4*)(uintptr_t)(cwa + i * 2048 + 1024) = b;
    }
    { f32x2 l2 = {oo[2][8], oo[3][8]}; *(LAS f32x2*)(uintptr_t)(cwa + 4096) = l2; }
    asm volatile("s_waitcnt lgkmcnt(0)" ::: "memory");
    lds_inc(lds0 + K2_MISC + SY_CMB + qh * 4);
    lds_wait(lds0 + K2_MISC + SY_CMB + qh * 4, 2);
    {
        const f32x2 l2 = *(LAS f32x2*)(uintptr_t)(cra + 4096);
#pragma unroll
        for (int i = 0; i < 2; ++i) {
            const f32x4 a = *(LAS f32x4*)(uintptr_t)(cra + i * 2048), b = *(LAS f32x4*)(uintptr_t)(cra + i * 2048 + 1024);
#pragma unroll
            for (int e = 0; e < 4; ++e) { oo[i][e] += a[e]; oo[i][4 + e] += b[e]; }
            oo[i][8] += l2[i];
            PIN(oo[i]);
        }
    }
    const bool ok = (att_mag(oo[0]) + att_mag(oo[1]) < 0x1p120f) & (fminf(oo[0][8], oo[1][8]) > 0x1p-80f);
    if (__builtin_expect(__builtin_amdgcn_ballot_w64(!ok) != 0, 0)) { att_exact(kg, vg, QF(0), oo[0]); att_exact(kg, vg, QF(1), oo[1]); }
    att_store(oo[0], dst, l16);
    att_store(oo[1], dst + CAT_KS * 1024, l16);
    publish(cnt);
    STAMP(9);
    STAMP_FLUSH(1, ws);
}

template <int CTRL> DI float dppf(float x) { return __builtin_bit_cast(float, __builtin_amdgcn_mov_dpp(__builtin_bit_cast(int, x), CTRL, 0xf, 0xf, true)); }
DI float fold32(float a, float b) {
    auto r = __builtin_amdgcn_permlane32_swap(__float_as_uint(a), __float_as_uint(b), false, false);
    return __uint_as_float(r[0]) + __uint_as_float(r[1]);
}
DI float fold16(float a, float b) {
    auto r = __builtin_amdgcn_permlane16_swap(__float_as_uint(a), __float_as_uint(b), false, false);
    return __uint_as_float(r[0]) + __uint_as_float(r[1]);
}
DI float rowsum16(float v) { v += dppf<0xB1>(v); v += dppf<0x4E>(v); v += dppf<0x124>(v); v += dppf<0x128>(v); return v; }

DI void spg_role(int b, int head, int w, const char* __restrict__ wsp, const __hip_bfloat16* __restrict__ bsp, char* __restrict__ ws, unsigned lds0,
                 const char* __restrict__ x, const char* __restrict__ wo, const char* __restrict__ wout, char* __restrict__ out) {
    const int lane = threadIdx.x & 63;
    const int xc = b & 7, rr = b >> 3;
    const int tt = xc * 4 + (rr & 3), ft = rr >> 2;
    const bool active = ft < 7;
    STAMP_DECL; STAMP(0); STAMPC(14);
    const unsigned wring = lds0 + K2_W + w * 8192;
    const __amdgpu_buffer_rsrc_t wr = __builtin_amdgcn_make_buffer_rsrc((void*)wsp, (short)0, S_ * S_ * 2, 0x00020000);
    unsigned wso = tt * 64 * 4096 + w * 1024;
    unsigned wvoff[2];
#pragma unroll
    for (int p = 0; p < 2; ++p) wvoff[p] = (lane >> 3) * 4096 + (((lane & 7) ^ ((4 * p + (lane >> 4)) & 7)) << 4);
#define ISSUEW(i) BLDS(wr, wvoff[(i) & 1], wso, (i) * 32768, wring, (i) * 1024)
    auto poll = [&](unsigned idx, unsigned cnt, int n) {
        const unsigned fo = idx * CNT_STRIDE;
        char* fl = ws + WS_FLAG;
#pragma unroll 1
        for (int it = 0; it < SPIN_MAX; ++it) {
            unsigned f = cnt;
            if (lane < n) { const unsigned z = 0; asm volatile("global_atomic_add %0, %1, %2, %3 sc0\n\ts_waitcnt vmcnt(0)" : "=&v"(f) : "v"(fo), "v"(z), "s"(fl) : "memory"); }
            if (__all(f == cnt)) break;
            if ((it & 63) == 63) add16(fl + CNT_HB * CNT_STRIDE, 8);
            __builtin_amdgcn_s_sleep(2);
        }
    };
    f32x16 acc[4][2];
    unsigned zvoff[4], waddr[4];
#define SETUP() do { _Pragma("unroll") for (int j = 0; j < 4; ++j) { \
            acc[j][0] = splat16(0.f); acc[j][1] = splat16(0.f); PIN(acc[j][0]); PIN(acc[j][1]); \
            zvoff[j] = (lane >> 5) * (FH * 16) + (j ^ w) * 512 + (lane & 31) * 16; PIN(zvoff[j]); \
            waddr[j] = wring + (lane & 31) * 128 + (((j * 2 + (lane >> 5)) ^ ((lane >> 1) & 7)) << 4); PIN(waddr[j]); } \
        PIN(wvoff[0]); PIN(wvoff[1]); } while (0)
    ISSUEW(0); ISSUEW(1); ISSUEW(2); ISSUEW(3); ISSUEW(4); ISSUEW(5); ISSUEW(6); ISSUEW(7);
    if (w == 3) {
        const __amdgpu_buffer_rsrc_t kr = __builtin_amdgcn_make_buffer_rsrc((void*)(ws + WS_K), (short)0, 4u << 20, 0x00020000);
        const unsigned lo = lane * 16;
        unsigned kso = head * 65536 + 5 * 1024, kd = lds0 + 5 * 1024;
        poll(CNT_QKV + (head >> 4) * 16 + ((b >> 3) & 15), 48, 1);
        lds_inc(lds0 + K2_MISC + SY_QKV);
        SETUP();
#define KV4() do { BLDSC(kr, lo, kso, 0, kd, K2_K - 1024); BLDSC(kr, lo, kso, WS_V - WS_K, kd, K2_V); BLDSC(kr, lo, kso, 32768, kd, K2_K + 31744 - 1024); \
                   BLDSC(kr, lo, kso, WS_V - WS_K + 32768, kd, K2_V + 32768); kso += 1024; kd += 1024; } while (0)
#pragma unroll 1
        for (int t = 5; t < 20; ++t) KV4();
        WAITV(28);
        PRIO_HI(); lds_inc(lds0 + K2_MISC + SY_KVB); PRIO_LO();
#pragma unroll 1
        for (int t = 20; t < 32; ++t) { KV4(); WAITV(44); }
        STAMP(13);
        PRIO_HI();
    } else {
        const char* src = ws + WS_Z2 + (size_t)b * FH * 16;
        u32x4 zc[5], z0;
        const unsigned o0 = w * 1024 + lane * 16, o4 = o0 + (w < 2 ? 12288 : 9216);
        const unsigned zero = 0;
        if (w == 0) { poll(CNT_Z2 + (b >> 4), 5, 1); lds_inc(lds0 + K2_MISC + SY_Z2); } else lds_wait<1>(lds0 + K2_MISC + SY_Z2, 1);
        GLD16C(zc[0], o0, src, 0); GLD16C(zc[1], o0, src, 3072); GLD16C(zc[2], o0, src + 6144, 0); GLD16C(zc[3], o0, src + 6144, 3072); GLD16C(zc[4], o4, src, 0); GLD16C(z0, zero, src, 0);
        SETUP();
        asm volatile("s_waitcnt vmcnt(0)" : "+v"(zc[0]), "+v"(zc[1]), "+v"(zc[2]), "+v"(zc[3]), "+v"(zc[4]), "+v"(z0) :: "memory");
        STAMP(1);
        PRIO_HI();
        char* dst = ws + WS_Z2N + (size_t)b * FH * 16;
        float sh[8], s1[8], s2[8];
#pragma unroll
        for (int e = 0; e < 4; ++e) { sh[2 * e] = bflo(z0[e]); sh[2 * e + 1] = bfhi(z0[e]); }
        const float m4 = w < 2 ? 1.f : 0.f;
#pragma unroll
        for (int e = 0; e < 8; ++e) {
            float v[5];
#pragma unroll
            for (int j = 0; j < 5; ++j) v[j] = ((e & 1) ? bfhi(zc[j][e >> 1]) : bflo(zc[j][e >> 1])) - sh[e];
            v[4] *= m4;
            s1[e] = ((v[0] + v[1]) + (v[2] + v[3])) + v[4];
            s2[e] = __builtin_fmaf(v[4], v[4], (v[0] * v[0] + v[1] * v[1]) + (v[2] * v[2] + v[3] * v[3]));
        }
        f32x4 q;
#pragma unroll
        for (int i = 0; i < 4; ++i) q[i] = rowsum16(fold16(fold32(s1[i], s2[i]), fold32(s1[i + 4], s2[i + 4])));
        LAS float* part = (LAS float*)(uintptr_t)(lds0 + K2_MISC + 256);
        *(LAS f32x4*)(part + w * 16 + (lane >> 4) * 4) = q;
        asm volatile("s_waitcnt lgkmcnt(0)" ::: "memory");
        lds_inc(lds0 + K2_MISC + SY_LN);
        lds_wait(lds0 + K2_MISC + SY_LN, 3);
        STAMP(2);
        float rs[8], nm[8];
        {
            f32x4 t[4];
#pragma unroll
            for (int i = 0; i < 4; ++i) t[i] = (*(LAS f32x4*)(part + i * 4) + *(LAS f32x4*)(part + 16 + i * 4)) + *(LAS f32x4*)(part + 32 + i * 4);
#pragma unroll
            for (int e = 0; e < 8; ++e) {
                float mean = t[e >> 2][e & 3] * (1.0f / FH);
                float var = fmaxf(t[2 + (e >> 2)][e & 3] * (1.0f / FH) - mean * mean, 0.f);
                rs[e] = __builtin_amdgcn_rsqf(var + 1e-5f);
                nm[e] = -(mean + sh[e]) * rs[e];
            }
        }
#pragma unroll
        for (int j = 0; j < 5; ++j) {
            u32x4 v;
#pragma unroll
            for (int e = 0; e < 4; ++e) v[e] = pk2(__builtin_fmaf(bflo(zc[j][e]), rs[2 * e], nm[2 * e]), __builtin_fmaf(bfhi(zc[j][e]), rs[2 * e + 1], nm[2 * e + 1]));
            if (j < 4) GST16(o0, v, dst + j * 3072); else GST16(o4, v, dst);
        }
        STAMP(3);
        WAITV(0);
        STAMP(4);
        unsigned old = 0;
        if (lane == 0) old = __hip_atomic_fetch_add((LAS unsigned*)(uintptr_t)(lds0 + K2_MISC + SY_LNST), 1u, __ATOMIC_RELAXED, __HIP_MEMORY_SCOPE_WORKGROUP);
        old = __builtin_amdgcn_readfirstlane(old);
        if (old == 2) {
            add16(ws + WS_FLAG + (size_t)(b >> 5) * CNT_STRIDE, 11);
            if (!active) { WAITV(0); lds_inc(lds0 + K2_MISC + SY_FLAG); }
        }
    }
    const unsigned l16 = lane * 16;
    auto reg = [&](int ww) { return lds0 + ((ww & 1) ? K2_V + (ww >> 1) * 32768 : K2_K + (ww >> 1) * 31744); };
    if (active) {
        const char* zb = ws + WS_Z2N + ((size_t)w * 64 * FH + ft * 128) * 16;
        u32x4 zf[4][4];
#define ZPTR(c, ks) (zb + (size_t)((c) * 4 + (ks)) * (2 * FH * 16))
        if (w == 0) { poll(lane < 8 ? ((b >> 3) & 15) * 8 + lane : CNT_Z1 + (tt >> 1), lane < 8 ? 32 : 5, 9); lds_inc(lds0 + K2_MISC + SY_FLAG); }
        else lds_wait<2>(lds0 + K2_MISC + SY_FLAG, 1);
        STAMP(5);
        PRIO_LO();
#pragma unroll
        for (int ks = 0; ks < 4; ++ks)
#pragma unroll
            for (int j = 0; j < 4; ++j) GLD16S(zf[ks][j], zvoff[j], ZPTR(0, ks));
        if (w == 3) { WAITV(16); PRIO_HI(); lds_inc(lds0 + K2_MISC + SY_KVB); PRIO_LO(); }
#define ZWAIT(n, ks) asm volatile("s_waitcnt vmcnt(" #n ")" : "+v"(zf[ks][0]), "+v"(zf[ks][1]), "+v"(zf[ks][2]), "+v"(zf[ks][3]) :: "memory")
#define KPAIR(ks, j_, MORE) do { \
        acc[j_][0] = MFMA32(__builtin_bit_cast(bf16x8, zf[ks][j_]), wf[0][ks], acc[j_][0]); PIN(acc[j_][0]); SB(); \
        if (MORE && ks == 0) ISSUEW(2 * j_); \
        acc[j_][1] = MFMA32(__builtin_bit_cast(bf16x8, zf[ks][j_]), wf[1][ks], acc[j_][1]); PIN(acc[j_][1]); SB(); \
        if (MORE && ks == 0) ISSUEW(2 * j_ + 1); \
        if (MORE) GLD16S(zf[ks][j_], zvoff[j_], ZPTR(c + 1, ks)); } while (0)
#define KSTEP(ks, MORE) do { KPAIR(ks, 0, MORE); KPAIR(ks, 1, MORE); KPAIR(ks, 2, MORE); KPAIR(ks, 3, MORE); } while (0)
#define WREAD() _Pragma("unroll") for (int ks = 0; ks < 4; ++ks) { wf[0][ks] = ldsfrag(waddr[ks]); wf[1][ks] = ldsfrag(waddr[ks] + 4096); } \
        asm volatile("s_waitcnt lgkmcnt(0)" : "+v"(wf[0][0]), "+v"(wf[0][1]), "+v"(wf[0][2]), "+v"(wf[0][3]), "+v"(wf[1][0]), "+v"(wf[1][1]), "+v"(wf[1][2]), "+v"(wf[1][3]) :: "memory")
#pragma unroll 1
        for (int c = 0; c < 7; ++c) {
            bf16x8 wf[2][4];
            wso += 128;
            ZWAIT(12, 0);
            WREAD();
            KSTEP(0, true);
            ZWAIT(20, 1); KSTEP(1, true);
            ZWAIT(20, 2); KSTEP(2, true);
            ZWAIT(20, 3); KSTEP(3, true);
        }
        const int fs0 = ft * 8 + w * 2;
        const char* z1p = ws + WS_Z1 + ((size_t)tt * 2 * 56 + fs0) * 1024;
        const char* bp = (const char*)bsp + tt * 128;
        const unsigned bo = (lane & 31) * 2;
        unsigned braw[2];
        {
            const int c = 7; (void)c;
            bf16x8 wf[2][4];
            ZWAIT(12, 0);
            WREAD();
            PRIO_HI(); lds_inc(lds0 + K2_MISC + SY_MAIN); PRIO_LO();
            KSTEP(0, false);
            GLD16C(zf[0][0], l16, z1p, 0); GLD16C(zf[0][1], l16, z1p, 1024); GLD16C(zf[0][2], l16, z1p + 56 * 1024, 0); GLD16C(zf[0][3], l16, z1p + 56 * 1024, 1024);
            asm volatile("global_load_ushort %0, %1, %2" : "=v"(braw[0]) : "v"(bo), "s"(bp) : "memory");
            asm volatile("global_load_ushort %0, %1, %2 offset:64" : "=v"(braw[1]) : "v"(bo), "s"(bp) : "memory");
            ZWAIT(14, 1); KSTEP(1, false);
            ZWAIT(10, 2); KSTEP(2, false);
            ZWAIT(6, 3); KSTEP(3, false);
        }
        STAMP(6);
        PRIO_HI();
        lds_wait(lds0 + K2_MISC + SY_P16, 4);
        lds_wait(lds0 + K2_MISC + SY_MAIN, 4);
        auto blk = [&](int dstw, int j) { return (j < 3 ? reg(dstw) + (j - 1) * 8192 : lds0 + K2_W + dstw * 8192) + l16; };
        unsigned wa[3] = {blk(1 ^ w, 1), blk(2 ^ w, 2), blk(3 ^ w, 3)};
        PIN(wa[0]); PIN(wa[1]); PIN(wa[2]); SB();
        PRIO_LO();
#pragma unroll
        for (int j = 1; j < 4; ++j) {
            const unsigned a = wa[j - 1];
#pragma unroll
            for (int t = 0; t < 2; ++t)
#pragma unroll
                for (int q = 0; q < 4; ++q) {
                    f32x4 v = {acc[j][t][4 * q], acc[j][t][4 * q + 1], acc[j][t][4 * q + 2], acc[j][t][4 * q + 3]};
                    *(LAS f32x4*)(uintptr_t)(a + (t * 4 + q) * 1024) = v;
                }
        }
        asm volatile("s_waitcnt lgkmcnt(0)" ::: "memory");
        PRIO_HI();
        lds_inc(lds0 + K2_MISC + SY_RED);
        lds_wait(lds0 + K2_MISC + SY_RED, 4);
#pragma unroll
        for (int j = 1; j < 4; ++j) {
            const unsigned a = blk(w, j);
#pragma unroll
            for (int t = 0; t < 2; ++t)
#pragma unroll
                for (int q = 0; q < 4; ++q) {
                    f32x4 v = *(LAS f32x4*)(uintptr_t)(a + (t * 4 + q) * 1024);
#pragma unroll
                    for (int e = 0; e < 4; ++e) acc[0][t][4 * q + e] += v[e];
                }
        }
        asm volatile("s_waitcnt vmcnt(0)" : "+v"(zf[0][0]), "+v"(zf[0][1]), "+v"(zf[0][2]), "+v"(zf[0][3]), "+v"(braw[0]), "+v"(braw[1]) :: "memory");
        const float bias[2] = {bflo(braw[0]), bflo(braw[1])};
#pragma unroll
        for (int t = 0; t < 2; ++t)
#pragma unroll
            for (int s = 0; s < 2; ++s) {
                auto a0 = __builtin_amdgcn_permlane32_swap(zf[0][2 * t + s][0], zf[0][2 * t + s][2], false, false);
                auto a1 = __builtin_amdgcn_permlane32_swap(zf[0][2 * t + s][1], zf[0][2 * t + s][3], false, false);
                const unsigned zc[4] = {a0[0], a1[0], a0[1], a1[1]};
                unsigned p[4];
#pragma unroll
                for (int e = 0; e < 4; ++e)
                    p[e] = pk2(bflo(zc[e]) * (acc[0][t][8 * s + 2 * e] + bias[t]), bfhi(zc[e]) * (acc[0][t][8 * s + 2 * e + 1] + bias[t]));
                auto s0 = __builtin_amdgcn_permlane32_swap(p[0], p[2], false, false);
                auto s1 = __builtin_amdgcn_permlane32_swap(p[1], p[3], false, false);
                u32x4 v = {s0[0], s1[0], s0[1], s1[1]};
                GST16(l16, v, ws + WS_CAT + ((size_t)(tt * 2 + t) * CAT_KS + 32 + fs0 + s) * 1024);
            }
        publish(ws + WS_FLAG + (size_t)(CNT_REC + tt * 8 + (ft * 4 + w) / 7) * CNT_STRIDE);
        STAMP(7);
    } else {
        const int id = (rr & 3) * 4 + w;
        if (w == 3) { WAITV(0); lds_inc(lds0 + K2_MISC + SY_KVB); }
        lds_wait<4>(lds0 + K2_MISC + SY_KVB, 2);
        lds_wait<4>(lds0 + K2_MISC + SY_FLAG, 1);
        PRIO_LO();
#pragma unroll 1
        for (int c = 1; c < 8; ++c) { wso += 128; ISSUEW(0); ISSUEW(1); ISSUEW(2); ISSUEW(3); ISSUEW(4); ISSUEW(5); ISSUEW(6); ISSUEW(7); WAITV(32); }
        lds_wait<32>(lds0 + K2_MISC + SY_P16, 4);
#pragma unroll 1
        for (int i = 0; i < 56; ++i) { glds16(wout + (size_t)(id * 56 + i) * 1024, l16, wring); if ((i & 7) == 7) WAITV(32); }
#pragma unroll 1
        for (int i = 0; i < 32; ++i) { glds16(wo + (size_t)(id * 32 + i) * 1024, l16, wring); if ((i & 7) == 7) WAITV(32); }
#pragma unroll 1
        for (int i = 0; i < 16; ++i) glds16(x + (size_t)(xc * 256 + id * 16 + i) * 1024, l16, wring);
        WAITV(0);
        PRIO_HI();
    }
    {
        const __amdgpu_buffer_rsrc_t hr = __builtin_amdgcn_make_buffer_rsrc((void*)wout, (short)0, FH * 1024, 0x00020000);
        const __amdgpu_buffer_rsrc_t orr = __builtin_amdgcn_make_buffer_rsrc((void*)wo, (short)0, D_ * 1024, 0x00020000);
        unsigned bvoff = ((lane >> 2) & 7) * 1024 + (lane >> 5) * 64 + (lane & 3) * 16;
        const unsigned hso = ft * 128 + w * 14 * 16384, oso = ft * 128 + w * 8 * 16384;
        const unsigned rg = reg(w);
#define FB(rs, so, s, base, off) do { BLDS(rs, bvoff, so, (s) * 16384, base, off); BLDS(rs, bvoff, so, (s) * 16384 + 8192, base, (off) + 1024); } while (0)
        const unsigned bl = (lane >> 5) * 1024 + (w & 1) * 512 + ((lane & 15) >> 2) * 64 + ((lane >> 4) & 1) * 32 + (lane & 3) * 8;
        unsigned br0 = rg + bl, br1 = rg + (bl ^ 512), bw0 = wring + bl, bw1 = wring + (bl ^ 512);
        unsigned xo = (lane & 31) * 1024 + (lane >> 5) * 16;
        PIN(bvoff); PIN(br0); PIN(br1); PIN(bw0); PIN(bw1); PIN(xo); SB();
        PRIO_LO();
        FB(hr, hso, 0, rg, 0); FB(hr, hso, 1, rg, 2048); FB(hr, hso, 2, rg, 4096); FB(hr, hso, 3, rg, 6144);
        FB(hr, hso, 4, rg, 8192); FB(hr, hso, 5, rg, 10240); FB(hr, hso, 6, rg, 12288); FB(hr, hso, 7, rg, 14336);
        FB(hr, hso, 8, wring, 0); FB(hr, hso, 9, wring, 2048); FB(hr, hso, 10, wring, 4096); FB(hr, hso, 11, wring, 6144);
        const size_t oo_ = (size_t)(tt * 64 + (w >> 1) * 32) * 1024 + (ft * 64 + (w & 1) * 32) * 2;
        u32x4 xv[2];
        GLD16(xv[0], xo, x + oo_, 0); GLD16(xv[1], xo, x + oo_, 32);
        PRIO_HI();
        lds_wait<8>(lds0 + K2_MISC + SY_P26, 4);
        FB(hr, hso, 12, rg, 16384); FB(hr, hso, 13, rg, 18432);
        const char* a0p = ws + WS_CAT + (size_t)(tt * 2 + (w >> 1)) * CAT_KS * 1024;
        const char* a1p = ws + WS_CAT + (size_t)(tt * 2 + ((w >> 1) ^ 1)) * CAT_KS * 1024;
        u32x4 af[14][2];
        f32x16 fc[2][2];
        bf16x8 bq[2][2];
#define SLOT0(s) ((s) < 8 ? br0 + (s) * 2048 : (s) < 12 ? bw0 + ((s) - 8) * 2048 : br0 + 16384 + ((s) - 12) * 2048)
#define SLOT1(s) ((s) < 8 ? br1 + (s) * 2048 : (s) < 12 ? bw1 + ((s) - 8) * 2048 : br1 + 16384 + ((s) - 12) * 2048)
#define BQ(s) do { bq[(s) & 1][0] = trfrag(SLOT0(s), SLOT0(s) + 256); bq[(s) & 1][1] = trfrag(SLOT1(s), SLOT1(s) + 256); } while (0)
#define AWAIT(n, s) asm volatile("s_waitcnt vmcnt(%c2)" : "+v"(af[s][0]), "+v"(af[s][1]) : "i"(n) : "memory")
#define FMM(s, FIRST) do { _Pragma("unroll") for (int i_ = 0; i_ < 2; ++i_) _Pragma("unroll") for (int j_ = 0; j_ < 2; ++j_) { \
            fc[i_][j_] = MFMA32(bq[(s) & 1][j_], __builtin_bit_cast(bf16x8, af[s][i_]), (FIRST) ? splat16(0.f) : fc[i_][j_]); PIN(fc[i_][j_]); } SB(); } while (0)
        poll(CNT_REC + tt * 8 + w, 7, 1);
        STAMP(8);
        PRIO_LO();
#pragma unroll
        for (int s = 0; s < 14; ++s) { GLD16S(af[s][0], l16, a0p + (32 + w * 14 + s) * 1024); GLD16S(af[s][1], l16, a1p + (32 + w * 14 + s) * 1024); }
        BQ(0);
#define HST(s) do { if (s < 13) BQ(s + 1); AWAIT(2 * (13 - s) + (s > 8 ? 16 : 0), s); FMM(s, s == 0); } while (0)
        HST(0); HST(1); HST(2); HST(3); HST(4); HST(5); HST(6); HST(7); HST(8);
        FB(orr, oso, 0, rg, 0); FB(orr, oso, 1, rg, 2048); FB(orr, oso, 2, rg, 4096); FB(orr, oso, 3, rg, 6144);
        FB(orr, oso, 4, rg, 8192); FB(orr, oso, 5, rg, 10240); FB(orr, oso, 6, rg, 12288); FB(orr, oso, 7, rg, 14336);
        HST(9); HST(10); HST(11); HST(12); HST(13);
        STAMP(9);
        PRIO_HI();
        poll(CNT_REC + tt * 8 + 4 + w, 8, 1);
        STAMP(10);
#pragma unroll
        for (int s = 0; s < 8; ++s) { GLD16S(af[s][0], l16, a0p + (w * 8 + s) * 1024); GLD16S(af[s][1], l16, a1p + (w * 8 + s) * 1024); }
        BQ(0);
#define OST(s) do { if (s < 7) BQ(s + 1); AWAIT(2 * (7 - s), s); FMM(s, false); } while (0)
        OST(0); OST(1); OST(2); OST(3); OST(4); OST(5); OST(6); OST(7);
        STAMP(11);
#pragma unroll
        for (int j = 1; j < 4; ++j)
#pragma unroll
            for (int q = 0; q < 4; ++q) {
                const f32x16& t = fc[j >> 1][j & 1];
                f32x4 v = {t[4 * q], t[4 * q + 1], t[4 * q + 2], t[4 * q + 3]};
                *(LAS f32x4*)(uintptr_t)(rg + (j * 4 + q) * 1024 + l16) = v;
            }
        asm volatile("s_waitcnt lgkmcnt(0)" ::: "memory");
        lds_inc(lds0 + K2_MISC + SY_FIN);
        lds_wait(lds0 + K2_MISC + SY_FIN, 4);
#pragma unroll
        for (int j = 1; j < 4; ++j)
#pragma unroll
            for (int q = 0; q < 4; ++q) {
                f32x4 v = *(LAS f32x4*)(uintptr_t)(reg(w ^ j) + (j * 4 + q) * 1024 + l16);
#pragma unroll
                for (int e = 0; e < 4; ++e) fc[0][0][4 * q + e] += v[e];
            }
        asm volatile("" : "+v"(xv[0]), "+v"(xv[1]));
#pragma unroll
        for (int s = 0; s < 2; ++s) {
            auto a0 = __builtin_amdgcn_permlane32_swap(xv[s][0], xv[s][2], false, false);
            auto a1 = __builtin_amdgcn_permlane32_swap(xv[s][1], xv[s][3], false, false);
            const unsigned xc4[4] = {a0[0], a1[0], a0[1], a1[1]};
            unsigned p[4];
#pragma unroll
            for (int e = 0; e < 4; ++e) p[e] = pk2(bflo(xc4[e]) + fc[0][0][8 * s + 2 * e], bfhi(xc4[e]) + fc[0][0][8 * s + 2 * e + 1]);
            auto s0 = __builtin_amdgcn_permlane32_swap(p[0], p[2], false, false);
            auto s1 = __builtin_amdgcn_permlane32_swap(p[1], p[3], false, false);
            u32x4 v = {s0[0], s1[0], s0[1], s1[1]};
            *(u32x4*)(out + oo_ + xo + s * 32) = v;
        }
    }
    STAMP(12);
    STAMP_FLUSH(1, ws);
}

__global__ __launch_bounds__(512, 2) NOPK void k_block(const char* __restrict__ x, const char* __restrict__ wqkv, const char* __restrict__ wo, const char* __restrict__ win,
                                                       const char* __restrict__ wsp, const __hip_bfloat16* __restrict__ bsp, const char* __restrict__ wout,
                                                       char* __restrict__ ws, char* __restrict__ out) {
    extern __shared__ __attribute__((aligned(16))) char smem[];
    const unsigned lds0 = (unsigned)(uintptr_t)(LAS char*)smem;
    phase1(x, wqkv, win, ws, wsp, lds0);
    const int wid = __builtin_amdgcn_readfirstlane(threadIdx.x >> 6);
    const int b = blockIdx.x;
    const int head = (b & 7) * 4 + (b >> 6);
    if (wid < 4) att_role(head, (b >> 3) & 7, wid, ws, lds0);
    else spg_role(b, head, wid - 4, wsp, bsp, ws, lds0, x, wo, wout, out);
}

extern "C" void kernel_launch(void* const* d_in, const int* in_sizes, int n_in, void* d_out, int out_size, void* d_ws, size_t ws_size,
                              hipStream_t stream) {
    (void)hipFuncSetAttribute((const void*)k_block, hipFuncAttributeMaxDynamicSharedMemorySize, K2_LDS);
    hipLaunchKernelGGL(k_block, dim3(256), dim3(512), K2_LDS, stream, (const char*)d_in[0], (const char*)d_in[1], (const char*)d_in[2], (const char*)d_in[4],
                       (const char*)d_in[5], (const __hip_bfloat16*)d_in[6], (const char*)d_in[7], (char*)d_ws, (char*)d_out);
}
```

```cpp
#include <hip/hip_runtime.h>
#include <hip/hip_bf16.h>
#include <stdint.h>
#include <utility>

typedef __attribute__((ext_vector_type(2))) __bf16 bf16x2;
typedef __attribute__((ext_vector_type(8))) __bf16 bf16x8;
typedef __attribute__((ext_vector_type(16))) __bf16 bf16x16;
typedef __attribute__((ext_vector_type(2))) float f32x2;
typedef __attribute__((ext_vector_type(4))) float f32x4;
typedef __attribute__((ext_vector_type(8))) float f32x8;
typedef __attribute__((ext_vector_type(16))) float f32x16;
typedef __attribute__((ext_vector_type(4))) unsigned u32x4;
typedef __attribute__((ext_vector_type(4))) short s16x4;
#define LAS __attribute__((address_space(3)))
#define DI __device__ __forceinline__
#if defined(__HIP_DEVICE_COMPILE__)
#define NOPK __attribute__((target("no-packed-fp32-ops")))
#else
#define NOPK
#endif
#define SB() __builtin_amdgcn_sched_barrier(0)
#define PIN(x) asm volatile("" : "+v"(x))
#define WAITV(n) asm volatile("s_waitcnt vmcnt(" #n ")" ::: "memory")
#define MFMA16(a, b, c) __builtin_amdgcn_mfma_f32_16x16x32_bf16(a, b, c, 0, 0, 0)
#define MFMA32(a, b, c) __builtin_amdgcn_mfma_f32_32x32x16_bf16(a, b, c, 0, 0, 0)

constexpr int S_ = 2048, D_ = 512, FH = 896;
constexpr size_t WS_Q = 0, WS_K = 2u << 20, WS_V = 4u << 20, WS_Z1 = 6u << 20, WS_Z2 = 10u << 20, WS_Z2N = 14u << 20,
                 WS_CAT = 18u << 20, WS_FLAG = 24u << 20;
constexpr int CNT_STRIDE = 256, CNT_REC = 128, CNT_QKV = 384, CNT_Z2 = 416, CNT_Z1 = 432, CNT_HB = 448;
constexpr int CAT_KS = 88;

#define GST16(voff, val, sbase) asm volatile("global_store_dwordx4 %0, %1, %2 sc1\n\ts_nop 1" :: "v"(voff), "v"(val), "s"(sbase) : "memory")

#define GLD16(dst, voff, sbase, IMM) asm volatile("global_load_dwordx4 %0, %1, %2 offset:" #IMM : "=v"(dst) : "v"(voff), "s"(sbase) : "memory")

DI void glds16(const void* sbase, unsigned voff, unsigned ldsdst) {
    unsigned keep;
    asm volatile("s_mov_b32 %0, m0\n\ts_mov_b32 m0, %3\n\ts_nop 0\n\tglobal_load_lds_dwordx4 %1, %2\n\ts_mov_b32 m0, %0"
                 : "=&s"(keep) : "v"(voff), "s"(sbase), "s"(ldsdst) : "memory");
}
DI unsigned pk2(float a, float b) { f32x2 v = {a, b}; return __builtin_bit_cast(unsigned, __builtin_convertvector(v, bf16x2)); }
DI float bflo(unsigned u) { return __uint_as_float(u << 16); }
DI float bfhi(unsigned u) { return __uint_as_float(u & 0xffff0000u); }
DI s16x4 trread(unsigned addr) { return __builtin_amdgcn_ds_read_tr16_b64_v4i16((LAS s16x4*)(uintptr_t)addr); }
DI bf16x8 trfrag(unsigned addr0, unsigned addr1) {
    s16x4 lo = trread(addr0), hi = trread(addr1);
    return __builtin_bit_cast(bf16x8, __builtin_shufflevector(lo, hi, 0, 1, 2, 3, 4, 5, 6, 7));
}
DI bf16x8 ldsfrag(unsigned addr) { return *(const LAS bf16x8*)(uintptr_t)addr; }
DI float gelu_t(float u) {
    const float k1 = -2.3022081985f;
    float w = u * __builtin_fmaf(u * u, k1 * 0.044715f, k1);
    float e = __builtin_amdgcn_exp2f(w);
    return u * __builtin_amdgcn_rcpf(1.0f + e);
}

constexpr int K2_K = 0, K2_V = 62 * 1024, K2_W = 126 * 1024, K2_ONES = K2_W + 32768, K2_MISC = K2_ONES + 1024, K2_LDS = K2_MISC + 1024;
enum { SY_KVA = 0, SY_KVB = 4, SY_P26 = 12, SY_LN = 16, SY_LNST = 20, SY_RED = 24, SY_FLAG = 28, SY_CMB = 36  , SY_P16 = 44, SY_MAIN = 48, SY_FIN = 52, SY_QKV = 56, SY_Z2 = 60, SY_HB = 128   };
constexpr int SPIN_MAX = 1 << 21, LSPIN_MAX = 1 << 24;

#define GLD16C(dst, voff, sbase, IMM) asm volatile("global_load_dwordx4 %0, %1, %2 offset:" #IMM " sc1" : "=v"(dst) : "v"(voff), "s"(sbase) : "memory")
DI void hb_sample(const char* p, unsigned ldsdst) {
    unsigned t;
    asm volatile("v_mov_b32 %0, 0\n\ts_mov_b32 m0, %2\n\ts_mov_b64 exec, 1\n\tglobal_load_lds_dword %0, %1 sc1\n\ts_mov_b64 exec, -1" : "=&v"(t) : "s"(p), "s"(ldsdst) : "memory");
}
DI void publish(char* p) {
    unsigned t0, t1;
    asm volatile("v_mov_b32 %0, 0\n\tv_mov_b32 %1, 1\n\ts_waitcnt vmcnt(0)\n\ts_mov_b64 exec, 1\n\tglobal_atomic_add %0, %1, %2\n\ts_mov_b64 exec, -1" : "=&v"(t0), "=&v"(t1) : "s"(p) : "memory");
}
DI void add16(char* p, unsigned stride_shift) {
    unsigned t0, t1;
    asm volatile("v_mbcnt_lo_u32_b32 %0, -1, 0\n\tv_mov_b32 %1, 1\n\tv_lshlrev_b32 %0, %3, %0\n\ts_mov_b64 exec, 0xffff\n\tglobal_atomic_add %0, %1, %2\n\ts_mov_b64 exec, -1"
                 : "=&v"(t0), "=&v"(t1) : "s"(p), "s"(stride_shift) : "memory");
}

constexpr int K1_MISC = 9 * 16384;
constexpr int K1_SUM = K1_MISC;
constexpr int K1_CS = K1_MISC + 1024;
constexpr int K1_PRIV = K1_MISC + 1024 + 2048;
constexpr int K1_JUNK = K1_PRIV + 8 * 256;
constexpr int K1_LDS = K1_JUNK + 1024;
static_assert(K1_LDS <= K2_ONES, "phase 2 words are initialised while phase 1 runs");
template <class F, int... I> DI void sfor_(F&& f, std::integer_sequence<int, I...>) { (f(std::integral_constant<int, I>{}), ...); }
template <int N, class F> DI void sfor(F&& f) { sfor_(f, std::make_integer_sequence<int, N>{}); }
#define LAMBDA(a) [&](auto a) __attribute__((always_inline))
DI unsigned word(bf16x8 v, int e) { const u32x4 u = __builtin_bit_cast(u32x4, v); return u[e]; }
DI f32x16 splat16(float v) { f32x16 r; for (int i = 0; i < 16; ++i) r[i] = v; return r; }

#define BLDS(rsrc, voff, soff, SIMM, m0b, MIMM) do { unsigned t_; \
    asm volatile("s_add_u32 m0, %4, %c6\n\ts_add_u32 %0, %3, %c5\n\tbuffer_load_dwordx4 %1, %2, %0 offen lds" \
                 : "=&s"(t_) : "v"(voff), "s"(rsrc), "s"(soff), "s"(m0b), "i"(SIMM), "i"(MIMM) : "scc", "memory"); } while (0)

#define BLDSC(rsrc, voff, soff, SIMM, m0b, MIMM) do { unsigned t_; \
    asm volatile("s_add_u32 m0, %4, %c6\n\ts_add_u32 %0, %3, %c5\n\tbuffer_load_dwordx4 %1, %2, %0 offen sc1 lds" \
                 : "=&s"(t_) : "v"(voff), "s"(rsrc), "s"(soff), "s"(m0b), "i"(SIMM), "i"(MIMM) : "scc", "memory"); } while (0)

template <bool ORI2, bool GELU>
DI void k1_body(const char* __restrict__ x, const char* __restrict__ W, unsigned col0  , int np  , int rp, char* __restrict__ dst  ,
                unsigned dvoff, unsigned jstride, unsigned pstride, float oscale, char* __restrict__ ws, unsigned lds0, const char* __restrict__ wsp) {
    constexpr int LDW = GELU ? 3584 : 3072, NP = GELU ? 3 : 4;
    const int tid = threadIdx.x, lane = tid & 63;
    const int wid = __builtin_amdgcn_readfirstlane(tid >> 6);
    const int wm = wid & 3, wn = wid >> 2;
    const int r = lane & 31, h = lane >> 5;
    const bool leader = blockIdx.x == 0 && wid == 1;
    char* hb = ws + WS_FLAG + CNT_HB * CNT_STRIDE;
    const __amdgpu_buffer_rsrc_t xr = __builtin_amdgcn_make_buffer_rsrc((void*)x, (short)0, S_ * D_ * 2, 0x00020000);
    const __amdgpu_buffer_rsrc_t wr = __builtin_amdgcn_make_buffer_rsrc((void*)W, (short)0, D_ * LDW, 0x00020000);
    unsigned xso[4], wso[4];
#pragma unroll
    for (int s = 0; s < 4; ++s) { xso[s] = rp * 131072 + wid * 16384 + s * 256; wso[s] = col0 + (wid * 16 + s * 128) * LDW; }
    unsigned xvoff[2];
#pragma unroll
    for (int i = 0; i < 2; ++i) xvoff[i] = (i * 8 + (lane >> 3)) * 1024 + (((lane & 7) ^ ((4 * i + (lane >> 4)) & 7)) << 4);
    const unsigned wvoff = ((lane >> 2) & 7) * LDW + (lane >> 5) * 64 + (lane & 3) * 16;
    const unsigned sdst = lds0 + wid * 2048;
#define ISSX1(c, slot, i) BLDS(xr, xvoff[i], xso[(c) >> 1], ((c) & 1) * 128, sdst, (slot) * 16384 + (i) * 1024)
#define ISSW1(pp, st, slot, i) BLDS(wr, wvoff, wso[st], (pp) * 128 + (i) * 8 * LDW, sdst, (slot) * 16384 + (i) * 1024)
#define ISSX(c, slot) do { ISSX1(c, slot, 0); ISSX1(c, slot, 1); } while (0)
#define ISSW(pp, st, slot) do { ISSW1(pp, st, slot, 0); ISSW1(pp, st, slot, 1); } while (0)
    ISSX(0, 0); ISSX(1, 1); ISSW(0, 0, 2);
    ISSX(2, 3); ISSX(3, 4); ISSW(0, 1, 5);
#define PERM(q) ((q) == 0 ? 0 : (q) == 1 ? 4 : (q) < 5 ? (q) - 1 : (q))
    unsigned xad[4], bad[4];
#pragma unroll
    for (int t = 0; t < 4; ++t) {
        xad[t] = lds0 + (wm * 32 + r) * 128 + (((2 * (t ^ wm) + h) ^ ((r >> 1) & 7)) << 4);
        bad[t] = lds0 + (2 * (t ^ wm) + h) * 1024 + wn * 512 + ((lane & 15) >> 2) * 64 + ((lane >> 4) & 1) * 32 + (lane & 3) * 8;
    }
    const unsigned selw = ((lane & 15) == ((lane >> 4) & 1)) ? 0x3f803f80u : 0u;
    u32x4 selu = {selw, selw, selw, selw};
    const bf16x8 sel = __builtin_bit_cast(bf16x8, selu);
    bf16x8 af[32];
    f32x16 accA, accB;
    float st0 = 0.f, st1 = 0.f;
    const unsigned one2 = 0x3f803f80u;
    f32x4 csum;
    const f32x4 z4 = {0, 0, 0, 0};
#define MM(bf, a, c) (ORI2 ? MFMA32(a, bf, c) : MFMA32(bf, a, c))
#define MM16(bf, a) asm volatile("v_mfma_f32_32x32x16_bf16 %0, %1, %2, %3" : "+v"(accB) : "v"(ORI2 ? (a) : (bf)), "v"(ORI2 ? (bf) : (a)), "v"(accA))
#define BFR(q, off) trfrag(bad[PERM(q) & 3] + (off) + (PERM(q) >> 2) * 8192, bad[PERM(q) & 3] + (off) + (PERM(q) >> 2) * 8192 + 256)
#define MAIN(i, bf) do { if ((i) == 0) accA = MM(bf, af[i], splat16(0.f)); else if ((i) < 16) accA = MM(bf, af[i], accA); else if ((i) == 16) MM16(bf, af[i]); else accB = MM(bf, af[i], accB); \
        if ((i) < 16) PIN(accA); else PIN(accB); \
        if ((i) == 18) asm volatile("" :: "v"(accA));        \
        if (((i) & 7) < 2) { csum = MFMA16(sel, bf, (i) == 0 ? z4 : csum); PIN(csum); } } while (0)
#define DA(k, l) "v_dot2c_f32_bf16 %0, %" #k ", %" #k "\n\tv_dot2c_f32_bf16 %1, %" #l ", %" #l "\n\t"
#define DB(k, l) "v_dot2c_f32_bf16 %0, %" #k ", %2\n\tv_dot2c_f32_bf16 %1, %" #l ", %2\n\t"
#define AW(i, e) "v"(word(af[i], e))
#define STATS4(i) asm volatile("s_cmp_lg_u32 %3, 0\n\ts_cbranch_scc1 1f\n\t" DA(4, 5) DA(6, 7) DA(8, 9) DA(10, 11) DA(12, 13) DA(14, 15) DA(16, 17) DA(18, 19) "s_branch 2f\n1:\n\t" \
            DB(4, 5) DB(6, 7) DB(8, 9) DB(10, 11) DB(12, 13) DB(14, 15) DB(16, 17) DB(18, 19) "2:" : "+v"(st0), "+v"(st1) : "v"(one2), "s"(wn), \
            AW(i, 0), AW(i, 1), AW(i, 2), AW(i, 3), AW(i + 1, 0), AW(i + 1, 1), AW(i + 1, 2), AW(i + 1, 3), AW(i + 2, 0), AW(i + 2, 1), AW(i + 2, 2), AW(i + 2, 3), \
            AW(i + 3, 0), AW(i + 3, 1), AW(i + 3, 2), AW(i + 3, 3) : "scc")
#define BAR(n) asm volatile("s_waitcnt vmcnt(" #n ") lgkmcnt(0)\n\ts_barrier" ::: "memory")
    asm volatile("" : "=v"(accB));
    const unsigned csw = lds0 + K1_CS + wn * 512 + (lane & 15) * 16 + wm * 4;
    const unsigned csr = lds0 + K1_CS + wn * 512 + r * 16;
    const unsigned priv = lds0 + K1_PRIV + wid * 256;
#define CSWRITE(par) do { if (lane < 16) { *(LAS float*)(uintptr_t)(csw + (par)) = csum[0]; *(LAS float*)(uintptr_t)(csw + (par) + 256) = csum[1]; } } while (0)
    bf16x8 bq[4];
    {
        const unsigned pso = col0 + ((rp & 7) * 64 + wid * 8) * LDW, junk = lds0 + K1_JUNK;
#pragma unroll
        for (int t = 0; t < 4; ++t) bad[t] += 2 * 16384;
        sfor<35>(LAMBDA(U) {
            constexpr int u = U, i = u - 3, st = u >> 3, q = u & 7, ip = i < 0 ? 0 : i, s4 = st & 3;
            if (q == 0) {
                if (st == 4) {
                    const float t = st0 + st1;
                    auto rr = __builtin_amdgcn_permlane32_swap(__float_as_uint(t), __float_as_uint(t), false, false);
                    *(LAS float*)(uintptr_t)(lds0 + K1_SUM + wn * 512 + (wm * 32 + r) * 4) = __uint_as_float(rr[0]) + __uint_as_float(rr[1]);
                    CSWRITE(0);
                }
                if (st == 0) BAR(6); else if (st < 3) BAR(7); else if (st == 3) BAR(6); else BAR(3);
                if (st > 0) {
#pragma unroll
                    for (int t = 0; t < 4; ++t) { if (st < 4) xad[t] += st == 3 ? -6 * 16384 : 3 * 16384; bad[t] += st == 3 ? -6 * 16384 : st == 4 ? 16384 : 3 * 16384; }
                }
                if (st == 4 && leader) { WAITV(0); add16(hb, 8); }
            }
            if constexpr (st < 4 && q < 4) { af[st * 8 + 2 * q] = ldsfrag(xad[PERM(2 * q) & 3] + (PERM(2 * q) >> 2) * 16384); af[st * 8 + 2 * q + 1] = ldsfrag(xad[PERM(2 * q + 1) & 3] + (PERM(2 * q + 1) >> 2) * 16384); }
            bq[u & 3] = BFR(q, 0);
            if (i >= 0) MAIN(ip, bq[ip & 3]);
            if (st == 0) { if (q < 2) ISSX1(4, 6, q & 1); else if (q < 4) ISSX1(5, 7, q & 1); else if (q < 6) ISSW1(0, 2, 8, q & 1); else if (q == 6) BLDS(wr, wvoff, pso, 128, junk, 0); }
            if (st == 1) { if (q < 2) ISSX1(6, 0, q & 1); else if (q < 4) ISSX1(7, 1, q & 1); else if (q < 6) ISSW1(0, 3, 2, q & 1); }
            if (st == 2 && q < 6) ISSW1(1, q >> 1, 3 + (q >> 1), q & 1);
            if (st == 3) {
                if (q < 2) ISSW1(1, 3, 6, q & 1);
                if (q == 2) BLDS(wr, wvoff, pso, 256, junk, 0);
                if (q == 3) BLDS(wr, wvoff, pso, (NP - 1) * 128, junk, 0);
                if (q == 4) {
                    const __amdgpu_buffer_rsrc_t sr = __builtin_amdgcn_make_buffer_rsrc((void*)wsp, (short)0, S_ * S_ * 2, 0x00020000);
                    const unsigned so = ((blockIdx.x & 7) * 32 + (blockIdx.x >> 3)) * 32768 + wm * 1024 + wn * 128, so_v = (lane >> 3) * 4096 + (lane & 7) * 16;
                    BLDS(sr, so_v, so, 0, junk, 0);
                }
            }
            if constexpr (st < 4 && (q == 4 || q == 6)) STATS4(s4 * 8 + (q - 4) * 2);
            SB();
        });
    }
    float rs, nm;
    {
        const float ss = *(LAS float*)(uintptr_t)(lds0 + K1_SUM + (wm * 32 + r) * 4), sm = *(LAS float*)(uintptr_t)(lds0 + K1_SUM + 512 + (wm * 32 + r) * 4);
        const float mean = sm * (1.0f / 512.0f);
        const float var = fmaxf(ss * (1.0f / 512.0f) - mean * mean, 0.0f);
        rs = __builtin_amdgcn_rsqf(var + 1e-5f) * oscale;
        nm = -mean * rs;
        if (ORI2) *(LAS float*)(uintptr_t)(priv + h * 128 + r * 4) = h ? nm : rs;
    }
    f32x4 ea[2], eb[2], c4; float cs; unsigned pk[4];
#define EPI_BEGIN0(par) c4 = *(LAS f32x4*)(uintptr_t)(csr + (par))
#define EPI_BEGIN1() do { cs = (c4[0] + c4[1]) + (c4[2] + c4[3]); PIN(cs); if (!ORI2) *(LAS float*)(uintptr_t)(priv + r * 4) = cs; } while (0)
#define EPI_LOAD(q) do { ea[(q) & 1] = *(LAS f32x4*)(uintptr_t)(priv + (q) * 32 + h * 16); if (ORI2) eb[(q) & 1] = *(LAS f32x4*)(uintptr_t)(priv + 128 + (q) * 32 + h * 16); } while (0)
#define EPI_Y(V, e) (ORI2 ? __builtin_fmaf(eb[((e) >> 2) & 1][(e) & 3], cs, (V) * ea[((e) >> 2) & 1][(e) & 3]) : __builtin_fmaf(nm, ea[((e) >> 2) & 1][(e) & 3], (V) * rs))
#define EPI_PAIR(V0, V1, e) do { float y0_ = EPI_Y(V0, e), y1_ = EPI_Y(V1, (e) + 1); if (GELU) { y0_ = gelu_t(y0_); y1_ = gelu_t(y1_); } \
        pk[((e) >> 1) & 3] = pk2(y0_, y1_); PIN(pk[((e) >> 1) & 3]); } while (0)
#define EPI_STORE(j) do { auto s0_ = __builtin_amdgcn_permlane32_swap(pk[0], pk[2], false, false); auto s1_ = __builtin_amdgcn_permlane32_swap(pk[1], pk[3], false, false); \
        u32x4 v_ = {s0_[0], s1_[0], s0_[1], s1_[1]}; GST16(dvoff, v_, dst + (j) * jstride); } while (0)
    EPI_BEGIN0(0);
    unsigned par = 0, rslot = 3, wslot = 7;
#pragma unroll 1
    for (int p = 1; p < np; ++p) {
        const bool more = p + 1 < np;
#pragma unroll
        for (int s = 0; s < 4; ++s) wso[s] = more ? wso[s] + 128 : 0x40000000u;
        unsigned wd[4];
#pragma unroll
        for (int s = 0; s < 4; ++s) { wd[s] = sdst + wslot * 16384; wslot = wslot == 8 ? 0 : wslot + 1; }
        float hi[8];
        SB();
        sfor<32>(LAMBDA(I) {
            constexpr int i = I;
            if (i == 29) {
                par ^= 1024;
                if (more) WAITV(2);
                asm volatile("s_waitcnt lgkmcnt(0)\n\ts_barrier" ::: "memory");
                EPI_BEGIN0(par);
                if (leader) add16(hb, 8);
                if (wid == 0 && !more) hb_sample(hb + (blockIdx.x & 15) * CNT_STRIDE, lds0 + K2_MISC + SY_HB + 4);
            }
            if (((i + 3) & 7) == 0) {
                const unsigned d = rslot == 8 ? (unsigned)(-8 * 16384) : 16384u;
                rslot = rslot == 8 ? 0 : rslot + 1;
#pragma unroll
                for (int t = 0; t < 4; ++t) bad[t] += d;
            }
            bq[(i + 3) & 3] = BFR((i + 3) & 7, 0);
            if constexpr (i >= 4 && i < 12) asm volatile("v_mov_b32 %0, %1" : "=&v"(hi[i - 4]) : "v"(accB[8 + i - 4]));
            if (i == 0) EPI_BEGIN1();
            if (i == 1 || i == 7 || i == 13 || i == 19) EPI_LOAD((i - 1) / 6);
            if constexpr (i >= 5 && i < 28 && (i - 5) % 3 == 0) { constexpr int e = ((i - 5) / 3) * 2; EPI_PAIR(e < 8 ? accB[e & 7] : hi[e & 7], e < 8 ? accB[(e + 1) & 7] : hi[(e + 1) & 7], e); }
            MAIN(i, bq[i & 3]);
            if constexpr (i < 16 && !(i & 1)) BLDS(wr, wvoff, wso[i >> 2], 128 + ((i >> 1) & 1) * 8 * LDW, wd[i >> 2], ((i >> 1) & 1) * 1024);
            if (i == 15 || i == 27) EPI_STORE(i == 27);
            if (i == 27) CSWRITE(par ^ 1024);
            SB();
        });
        dst += pstride;
    }
    EPI_BEGIN1();
    EPI_LOAD(0);
#pragma unroll
    for (int q = 0; q < 4; ++q) {
        if (q < 3) EPI_LOAD(q + 1);
#pragma unroll
        for (int e = 0; e < 4; e += 2) EPI_PAIR(accB[q * 4 + e], accB[q * 4 + e + 1], q * 4 + e);
        if (q & 1) EPI_STORE(q >> 1);
    }
    WAITV(0);
}

DI void phase1(const char* __restrict__ x, const char* __restrict__ wqkv, const char* __restrict__ win, char* __restrict__ ws, const char* __restrict__ wsp, unsigned lds0) {
    const int b = blockIdx.x, xc = b & 7, rr = b >> 3;
    const int rp = (xc & 1) * 8 + (rr & 7), ct = (xc >> 1) * 4 + (rr >> 3);
    const int lane = threadIdx.x & 63, wid = __builtin_amdgcn_readfirstlane(threadIdx.x >> 6);
    const int rt = rp * 4 + (wid & 3), wn = wid >> 2;
    const char* hbw = ws + WS_FLAG + (CNT_HB + (b & 15)) * CNT_STRIDE;
    if (wid == 0) hb_sample(hbw, lds0 + K2_MISC + SY_HB);
    if (b == 0 && wid == 1) {
#pragma unroll
        for (int i = 0; i < 7; ++i) __hip_atomic_store((unsigned*)(ws + WS_FLAG + (size_t)(i * 64 + lane) * CNT_STRIDE), 0u, __ATOMIC_RELAXED, __HIP_MEMORY_SCOPE_AGENT);
    }
    if (threadIdx.x < 256) *(LAS unsigned*)(uintptr_t)(lds0 + K2_ONES + threadIdx.x * 4) = ((threadIdx.x >> 2) & 11) == 0 ? 0x3f803f80u : 0u;
    else if (threadIdx.x < 256 + 32) *(LAS unsigned*)(uintptr_t)(lds0 + K2_MISC + (threadIdx.x - 256) * 4) = 0u;
    if (ct < 4) {
        const int head0 = (ct & 1) * 16 + wn * 2;
        k1_body<false, false>(x, wqkv, ct * 512, 4, rp, ws + (ct < 2 ? WS_Q : WS_K) + ((size_t)head0 * 64 + rt) * 1024, lane * 16, 65536, 4 * 65536,
                              ct < 2 ? 0.25f * 1.4426950408889634f : 1.0f, ws, lds0, wsp);
    } else if (ct < 6) {
        const int head0 = (ct & 1) * 16 + wn * 2;
        k1_body<true, false>(x, wqkv, ct * 512, 4, rp, ws + WS_V + ((size_t)head0 * 64 + rt) * 1024, ((lane >> 4) & 1) * 65536 + (lane >> 5) * 256 + (lane & 15) * 16, 512, 4 * 65536,
                             1.0f, ws, lds0, wsp);
    } else if (ct < 11) {
        const int c0 = (ct - 6) * 192;
        k1_body<false, true>(x, win, c0 * 2, ct == 10 ? 2 : 3, rp, ws + WS_Z1 + ((size_t)rt * 56 + (c0 >> 4) + wn * 2) * 1024, lane * 16, 1024, 4096, 1.0f, ws, lds0, wsp);
    } else {
        const int c0 = (ct - 11) * 192;
        k1_body<true, true>(x, win, (FH + c0) * 2, ct == 15 ? 2 : 3, rp, ws + WS_Z2 + ((size_t)rt * 4 * FH + c0 + wn * 32) * 16, (lane >> 5) * (FH * 16) + (lane & 31) * 16, 2 * FH * 16, 1024,
                            1.0f, ws, lds0, wsp);
    }
    if (wid == 0) {
        const unsigned h0 = __builtin_amdgcn_readfirstlane(*(volatile LAS unsigned*)(uintptr_t)(lds0 + K2_MISC + SY_HB));
#pragma unroll 1
        for (int it = 0; it < SPIN_MAX && __builtin_amdgcn_readfirstlane(*(volatile LAS unsigned*)(uintptr_t)(lds0 + K2_MISC + SY_HB + 4)) == h0; ++it) {
            __builtin_amdgcn_s_sleep(8);
            hb_sample(hbw, lds0 + K2_MISC + SY_HB + 4);
            WAITV(0);
        }
    }
    asm volatile("s_waitcnt lgkmcnt(0)\n\ts_barrier" ::: "memory");
    if (wid == 7) {
        if (ct < 6) add16(ws + WS_FLAG + (size_t)(CNT_QKV + (ct & 1) * 16) * CNT_STRIDE, 8);
        else publish(ws + WS_FLAG + (size_t)((ct < 11 ? CNT_Z1 : CNT_Z2) + rp) * CNT_STRIDE);
    }
}

constexpr float REF_OFF = 96.0f;

DI void lds_inc(unsigned addr) {
    unsigned t0, t1;
    asm volatile("v_mov_b32 %0, %2\n\tv_mov_b32 %1, 1\n\ts_mov_b64 exec, 1\n\tds_add_u32 %0, %1\n\ts_mov_b64 exec, -1" : "=&v"(t0), "=&v"(t1) : "s"(addr) : "memory");
}
template <int SLEEP = 0> DI void lds_wait(unsigned addr, unsigned n) {
    unsigned t0, t1, c, v;
    if (SLEEP == 0)
        asm volatile("v_mov_b32 %0, %4\n\ts_mov_b32 %2, %6\n1:\n\tds_read_b32 %1, %0\n\ts_sub_u32 %2, %2, 1\n\ts_waitcnt lgkmcnt(0)\n\tv_readfirstlane_b32 %3, %1\n\ts_nop 0\n\t"
                     "s_cselect_b32 %3, %5, %3\n\ts_cmp_lt_u32 %3, %5\n\ts_cbranch_scc1 1b"
                     : "=&v"(t0), "=&v"(t1), "=&s"(c), "=&s"(v) : "s"(addr), "s"(n), "i"(LSPIN_MAX) : "scc", "memory");
    else
        asm volatile("v_mov_b32 %0, %4\n\ts_mov_b32 %2, %6\n\ts_branch 2f\n1:\n\ts_sleep %c7\n2:\n\tds_read_b32 %1, %0\n\ts_sub_u32 %2, %2, 1\n\ts_waitcnt lgkmcnt(0)\n\tv_readfirstlane_b32 %3, %1\n\ts_nop 0\n\t"
                     "s_cselect_b32 %3, %5, %3\n\ts_cmp_lt_u32 %3, %5\n\ts_cbranch_scc1 1b"
                     : "=&v"(t0), "=&v"(t1), "=&s"(c), "=&s"(v) : "s"(addr), "s"(n), "i"(LSPIN_MAX), "i"(SLEEP) : "scc", "memory");
}
DI bf16x16 lo8(const f32x16& x) { f32x8 t = __builtin_shufflevector(x, x, 0, 1, 2, 3, 4, 5, 6, 7); return __builtin_bit_cast(bf16x16, t); }
DI bf16x16 hi8(const f32x16& x) { f32x8 t = __builtin_shufflevector(x, x, 8, 9, 10, 11, 12, 13, 14, 15); return __builtin_bit_cast(bf16x16, t); }
#define PRIO_HI() __builtin_amdgcn_s_setprio(2)
#define PRIO_LO() __builtin_amdgcn_s_setprio(0)
#define SMF(a, b, c) __builtin_amdgcn_smfmac_f32_32x32x32_bf16(a, b, c, sidx, 0, 0)
#define GLD16S(dst, voff, sbase) GLD16C(dst, voff, sbase, 0)

DI float max16(const f32x16& s) {
    float a = fmaxf(fmaxf(s[0], s[1]), s[2]), b = fmaxf(fmaxf(s[3], s[4]), s[5]);
    a = fmaxf(fmaxf(a, s[6]), s[7]); b = fmaxf(fmaxf(b, s[8]), s[9]);
    a = fmaxf(fmaxf(a, s[10]), s[11]); b = fmaxf(fmaxf(b, s[12]), s[13]);
    a = fmaxf(fmaxf(a, s[14]), s[15]);
    return fmaxf(a, b);
}
DI float xhalf_max(float m) {
    auto rr = __builtin_amdgcn_permlane32_swap(__float_as_uint(m), __float_as_uint(m), false, false);
    return fmaxf(__uint_as_float(rr[0]), __uint_as_float(rr[1]));
}

#define LD4C(d, p, voff, o0, o1, o2, o3) asm volatile("global_load_dwordx4 %0, %4, %5 offset:" #o0 " sc1\n\tglobal_load_dwordx4 %1, %4, %5 offset:" #o1 " sc1\n\t" \
        "global_load_dwordx4 %2, %4, %5 offset:" #o2 " sc1\n\tglobal_load_dwordx4 %3, %4, %5 offset:" #o3 " sc1\n\ts_waitcnt vmcnt(0)" \
        : "=&v"(d[0]), "=&v"(d[1]), "=&v"(d[2]), "=&v"(d[3]) : "v"(voff), "s"(p) : "memory")
DI void att_exact(const char* kg, const char* vg, bf16x8 q0, bf16x8 q1, f32x16& o0, f32x16& o1) {
    const unsigned lane = threadIdx.x & 63, l16 = lane * 16, vo = (lane >> 5) * 256 + (lane & 15) * 16;
    const int sidx = (int)0xDDDDDDDDu;
    const f32x16 z = splat16(0.f);
    float m0 = -INFINITY, m1 = -INFINITY;
#pragma unroll 1
    for (int t = 0; t < 64; t += 4) {
        bf16x8 kf[4];
        LD4C(kf, kg + t * 1024, l16, 0, 1024, 2048, 3072);
#pragma unroll
        for (int i = 0; i < 4; ++i) { m0 = fmaxf(m0, max16(MFMA32(kf[i], q0, z))); m1 = fmaxf(m1, max16(MFMA32(kf[i], q1, z))); }
    }
    const f32x16 c0 = splat16(-xhalf_max(m0)), c1 = splat16(-xhalf_max(m1));
    o0 = z; o1 = z;
    const u32x4 onesu = {0x3f803f80u, 0x3f803f80u, 0x3f803f80u, 0x3f803f80u};
#pragma unroll 1
    for (int t = 0; t < 64; t += 2) {
        bf16x8 kf[4]; u32x4 vf[4];
        LD4C(vf, vg + t * 1024, vo, 0, 512, 1024, 1536);
        LD4C(kf, kg + t * 1024, l16, 0, 1024, 0, 1024);
#pragma unroll
        for (int i = 0; i < 2; ++i) {
            const bf16x8 va = __builtin_bit_cast(bf16x8, (lane & 16) ? onesu : vf[2 * i]), vb = __builtin_bit_cast(bf16x8, (lane & 16) ? onesu : vf[2 * i + 1]);
            f32x16 s0 = MFMA32(kf[i], q0, c0), s1 = MFMA32(kf[i], q1, c1);
#pragma unroll
            for (int e = 0; e < 16; ++e) { s0[e] = __builtin_amdgcn_exp2f(s0[e]); s1[e] = __builtin_amdgcn_exp2f(s1[e]); }
            o0 = SMF(va, lo8(s0), o0); o0 = SMF(vb, hi8(s0), o0);
            o1 = SMF(va, lo8(s1), o1); o1 = SMF(vb, hi8(s1), o1);
        }
    }
}

DI void att_store(const f32x16& o, char* dst, unsigned l16) {
    const float rl = __builtin_amdgcn_rcpf(o[8]);
    unsigned p0 = pk2(o[0] * rl, o[1] * rl), p1 = pk2(o[2] * rl, o[3] * rl), p2 = pk2(o[4] * rl, o[5] * rl), p3 = pk2(o[6] * rl, o[7] * rl);
    auto s0 = __builtin_amdgcn_permlane32_swap(p0, p2, false, false);
    auto s1 = __builtin_amdgcn_permlane32_swap(p1, p3, false, false);
    u32x4 v = {s0[0], s1[0], s0[1], s1[1]};
    GST16(l16, v, dst);
}
DI float att_mag(const f32x16& o) {
    return ((fabsf(o[0]) + fabsf(o[1])) + (fabsf(o[2]) + fabsf(o[3]))) + ((fabsf(o[4]) + fabsf(o[5])) + (fabsf(o[6]) + fabsf(o[7]))) + o[8];
}

DI void att_role(int head, int qb, int w, char* __restrict__ ws, unsigned lds0) {
    const int lane = threadIdx.x & 63;
    const int qh = w & 1, kh = w >> 1;
    const char* kg = ws + WS_K + (size_t)head * 65536;
    const char* vg = ws + WS_V + (size_t)head * 65536;
    const char* qg = ws + WS_Q + (size_t)head * 65536 + (size_t)(qb * 8 + qh * 4) * 1024;
    const unsigned l16 = lane * 16;
    const unsigned vo = (lane >> 5) * 256 + (lane & 15) * 16;
    u32x4 qr[4], k0, ks;
    const unsigned qo = l16 + kh * 2048, qo2 = l16 + (kh ^ 1) * 2048;
    int sidx = (int)0xDDDDDDDDu; PIN(sidx);
    f32x16 oo[4] = {splat16(0.f), splat16(0.f), splat16(0.f), splat16(0.f)}, ss[4];
    ss[3] = splat16(0.f);
    u32x4 v0 = {0, 0, 0, 0}, v1 = {0, 0, 0, 0};
    asm volatile("" : "+{v[0:15]}"(oo[0]), "+{v[16:31]}"(oo[1]), "+{v[32:47]}"(oo[2]), "+{v[48:63]}"(oo[3]), "=&{v[144:159]}"(ss[1]), "=&{v[160:175]}"(ss[2]), "+{v[176:191]}"(ss[3]),
                      "+{v[212:215]}"(v0), "+{v[216:219]}"(v1));
    unsigned kaddr = lds0 + K2_K + kh * 31744 + l16;
    unsigned vaddr = (lane & 16) ? (lds0 + K2_ONES + vo) : (lds0 + K2_V + kh * 32768 + vo);
    unsigned vinc = (lane & 16) ? 0u : 1024u;
    u32x4 e0 = {0x3f80u, 0u, 0u, 0u};
    const unsigned cb = lds0 + (qh ? K2_V : K2_K) + 20 * 1024 + l16, cst = qh ? 32768 : 31744;
    unsigned cwa = cb + kh * cst, cra = cb + (kh ^ 1) * cst;
    char* dst = ws + WS_CAT + ((size_t)(qb * 8 + qh * 4 + kh * 2) * CAT_KS + head) * 1024;
    char* cnt = ws + WS_FLAG + (size_t)(CNT_REC + (qb * 4 + qh * 2 + kh) * 8 + 4 + (head >> 3)) * CNT_STRIDE;
    PIN(kaddr); PIN(vaddr); PIN(vinc); PIN(e0); PIN(cwa); PIN(cra);
    asm volatile("" : "+s"(dst), "+s"(cnt));
    lds_wait<1>(lds0 + K2_MISC + SY_QKV, 1);
    GLD16C(ks, l16, kg, 0);
    GLD16C(qr[0], qo, qg, 0); GLD16C(qr[1], qo, qg, 1024); GLD16C(qr[2], qo2, qg, 0); GLD16C(qr[3], qo2, qg, 1024);
    GLD16C(k0, l16, kg + kh * 32768, 0);
    {
        const __amdgpu_buffer_rsrc_t kr = __builtin_amdgcn_make_buffer_rsrc((void*)(ws + WS_K), (short)0, 4u << 20, 0x00020000);
        const unsigned so = head * 65536 + (qh ? (unsigned)(WS_V - WS_K) : 1024u) + kh * 32768, d = lds0 + (qh ? K2_V + kh * 32768 : K2_K + kh * 31744);
        BLDSC(kr, l16, so, 0, d, 0); BLDSC(kr, l16, so, 1024, d, 1024); BLDSC(kr, l16, so, 2048, d, 2048); BLDSC(kr, l16, so, 3072, d, 3072); BLDSC(kr, l16, so, 4096, d, 4096);
    }
    asm volatile("s_waitcnt vmcnt(5)" : "+v"(qr[0]), "+v"(qr[1]), "+v"(qr[2]), "+v"(qr[3]), "+v"(k0), "+v"(ks) :: "memory");
#define QF(i) __builtin_bit_cast(bf16x8, qr[i])
    f32x16 cc[4];
#pragma unroll
    for (int i = 0; i < 4; ++i) { cc[i] = MFMA32(__builtin_bit_cast(bf16x8, ks), QF(i), splat16(0.f)); PIN(cc[i]); SB(); }
#pragma unroll
    for (int i = 0; i < 4; ++i) {
        const float m = __builtin_fmaf(max16(cc[i]), -0.5f, -0.5f * REF_OFF);
        u32x4 mb = {__float_as_uint(m) >> 16, 0u, 0u, 0u};
        cc[i] = MFMA32(__builtin_bit_cast(bf16x8, e0), __builtin_bit_cast(bf16x8, mb), splat16(0.f));
        PIN(cc[i]);
    }
    u32x4 kf = k0;
    WAITV(0); lds_inc(lds0 + K2_MISC + SY_KVA);
    asm volatile("v_mfma_f32_32x32x16_bf16 v[128:143], v[208:211], v[192:195], v[64:79]" : "=&{v[128:143]}"(ss[0]) : "{v[208:211]}"(kf), "{v[192:195]}"(qr[0]), "{v[64:79]}"(cc[0]));
    lds_wait(lds0 + K2_MISC + SY_KVA, 4);
#pragma unroll 1
    for (int seg = 0; seg < 5; ++seg) {
        int n = 3;
        if (seg == 1) { n = 7; lds_wait(lds0 + K2_MISC + SY_KVB, 1); }
        else if (seg == 2) { n = 4; lds_wait(lds0 + K2_MISC + SY_KVB, 2); }
        else if (seg >= 3) { n = seg == 3 ? 9 : 4; lds_inc(lds0 + K2_MISC + (seg == 3 ? SY_P16 : SY_P26)); }
        asm volatile("s_nop 7\n\t.p2align 6\n1:\n\t"
            "v_mfma_f32_32x32x16_bf16 v[144:159], v[208:211], v[196:199], v[80:95]\n\tv_exp_f32 v128, v128\n\tv_exp_f32 v129, v129\n\tv_exp_f32 v130, v130\n\t"
            "v_exp_f32 v131, v131\n\tv_smfmac_f32_32x32x32_bf16 v[48:63], v[212:215], v[176:183], v220\n\tds_read_b128 v[212:215], v222\n\tv_exp_f32 v132, v132\n\t"
            "v_exp_f32 v133, v133\n\tv_exp_f32 v134, v134\n\tv_exp_f32 v135, v135\n\tv_smfmac_f32_32x32x32_bf16 v[48:63], v[216:219], v[184:191], v220\n\t"
            "ds_read_b128 v[216:219], v222 offset:512\n\tv_exp_f32 v136, v136\n\tv_exp_f32 v137, v137\n\tv_exp_f32 v138, v138\n\tv_exp_f32 v139, v139\n\t"
            "v_exp_f32 v140, v140\n\tv_exp_f32 v141, v141\n\tv_exp_f32 v142, v142\n\tv_exp_f32 v143, v143\n\t"
            "v_mfma_f32_32x32x16_bf16 v[160:175], v[208:211], v[200:203], v[96:111]\n\tv_exp_f32 v144, v144\n\tv_exp_f32 v145, v145\n\tv_exp_f32 v146, v146\n\t"
            "v_exp_f32 v147, v147\n\ts_waitcnt lgkmcnt(1)\n\tv_smfmac_f32_32x32x32_bf16 v[0:15], v[212:215], v[128:135], v220\n\tv_exp_f32 v148, v148\n\t"
            "v_exp_f32 v149, v149\n\tv_exp_f32 v150, v150\n\tv_exp_f32 v151, v151\n\ts_waitcnt lgkmcnt(0)\n\t"
            "v_smfmac_f32_32x32x32_bf16 v[0:15], v[216:219], v[136:143], v220\n\tv_add_u32 v222, v222, v223\n\tv_exp_f32 v152, v152\n\tv_exp_f32 v153, v153\n\t"
            "v_exp_f32 v154, v154\n\tv_exp_f32 v155, v155\n\tv_exp_f32 v156, v156\n\tv_exp_f32 v157, v157\n\tv_exp_f32 v158, v158\n\tv_exp_f32 v159, v159\n\t"
            "v_mfma_f32_32x32x16_bf16 v[176:191], v[208:211], v[204:207], v[112:127]\n\tds_read_b128 v[208:211], v221\n\tv_exp_f32 v160, v160\n\t"
            "v_exp_f32 v161, v161\n\tv_exp_f32 v162, v162\n\tv_exp_f32 v163, v163\n\tv_smfmac_f32_32x32x32_bf16 v[16:31], v[212:215], v[144:151], v220\n\t"
            "v_exp_f32 v164, v164\n\tv_exp_f32 v165, v165\n\tv_exp_f32 v166, v166\n\tv_exp_f32 v167, v167\n\t"
            "v_smfmac_f32_32x32x32_bf16 v[16:31], v[216:219], v[152:159], v220\n\tv_add_u32 v221, 0x400, v221\n\tv_exp_f32 v168, v168\n\tv_exp_f32 v169, v169\n\t"
            "v_exp_f32 v170, v170\n\tv_exp_f32 v171, v171\n\tv_exp_f32 v172, v172\n\tv_exp_f32 v173, v173\n\tv_exp_f32 v174, v174\n\tv_exp_f32 v175, v175\n\t"
            "s_waitcnt lgkmcnt(0)\n\tv_mfma_f32_32x32x16_bf16 v[128:143], v[208:211], v[192:195], v[64:79]\n\tv_exp_f32 v176, v176\n\tv_exp_f32 v177, v177\n\t"
            "v_exp_f32 v178, v178\n\tv_exp_f32 v179, v179\n\tv_smfmac_f32_32x32x32_bf16 v[32:47], v[212:215], v[160:167], v220\n\tv_exp_f32 v180, v180\n\t"
            "v_exp_f32 v181, v181\n\tv_exp_f32 v182, v182\n\tv_exp_f32 v183, v183\n\tv_smfmac_f32_32x32x32_bf16 v[32:47], v[216:219], v[168:175], v220\n\t"
            "v_exp_f32 v184, v184\n\tv_exp_f32 v185, v185\n\tv_exp_f32 v186, v186\n\tv_exp_f32 v187, v187\n\tv_exp_f32 v188, v188\n\tv_exp_f32 v189, v189\n\t"
            "s_sub_u32 %0, %0, 1\n\tv_exp_f32 v190, v190\n\tv_exp_f32 v191, v191\n\t"
            "s_cbranch_scc0 1b"
            : "+s"(n), "+{v[0:15]}"(oo[0]), "+{v[16:31]}"(oo[1]), "+{v[32:47]}"(oo[2]), "+{v[48:63]}"(oo[3]), "+{v[128:143]}"(ss[0]), "+{v[144:159]}"(ss[1]), "+{v[160:175]}"(ss[2]), "+{v[176:191]}"(ss[3]), "+{v[208:211]}"(kf), "+{v[212:215]}"(v0), "+{v[216:219]}"(v1), "+{v221}"(kaddr), "+{v222}"(vaddr)
            : "{v[64:79]}"(cc[0]), "{v[80:95]}"(cc[1]), "{v[96:111]}"(cc[2]), "{v[112:127]}"(cc[3]), "{v[192:195]}"(qr[0]), "{v[196:199]}"(qr[1]), "{v[200:203]}"(qr[2]), "{v[204:207]}"(qr[3]), "{v220}"(sidx), "{v223}"(vinc)
            : "scc", "memory");
    }
    asm volatile("v_smfmac_f32_32x32x32_bf16 v[48:63], v[212:215], v[176:183], v220\n\ts_nop 0\n\tv_smfmac_f32_32x32x32_bf16 v[48:63], v[216:219], v[184:191], v220\n\ts_nop 7\n\ts_nop 7"
            : "+{v[48:63]}"(oo[3]) : "{v[176:191]}"(ss[3]), "{v[212:215]}"(v0), "{v[216:219]}"(v1), "{v220}"(sidx));
    __builtin_amdgcn_s_setprio(3);
    lds_wait(lds0 + K2_MISC + SY_P26, 4);
#pragma unroll
    for (int i = 0; i < 2; ++i) {
        f32x4 a = {oo[2 + i][0], oo[2 + i][1], oo[2 + i][2], oo[2 + i][3]}, b = {oo[2 + i][4], oo[2 + i][5], oo[2 + i][6], oo[2 + i][7]};
        *(LAS f32x4*)(uintptr_t)(cwa + i * 2048) = a;
        *(LAS f32x4*)(uintptr_t)(cwa + i * 2048 + 1024) = b;
    }
    { f32x2 l2 = {oo[2][8], oo[3][8]}; *(LAS f32x2*)(uintptr_t)(cwa + 4096) = l2; }
    asm volatile("s_waitcnt lgkmcnt(0)" ::: "memory");
    lds_inc(lds0 + K2_MISC + SY_CMB + qh * 4);
    lds_wait(lds0 + K2_MISC + SY_CMB + qh * 4, 2);
    {
        const f32x2 l2 = *(LAS f32x2*)(uintptr_t)(cra + 4096);
#pragma unroll
        for (int i = 0; i < 2; ++i) {
            const f32x4 a = *(LAS f32x4*)(uintptr_t)(cra + i * 2048), b = *(LAS f32x4*)(uintptr_t)(cra + i * 2048 + 1024);
#pragma unroll
            for (int e = 0; e < 4; ++e) { oo[i][e] += a[e]; oo[i][4 + e] += b[e]; }
            oo[i][8] += l2[i];
            PIN(oo[i]);
        }
    }
    const bool ok = (att_mag(oo[0]) + att_mag(oo[1]) < 0x1p120f) & (fminf(oo[0][8], oo[1][8]) > 0x1p-100f);
    if (__builtin_expect(__builtin_amdgcn_ballot_w64(!ok) != 0, 0)) att_exact(kg, vg, QF(0), QF(1), oo[0], oo[1]);
    att_store(oo[0], dst, l16);
    att_store(oo[1], dst + CAT_KS * 1024, l16);
    publish(cnt);
}

template <int CTRL> DI float dppf(float x) { return __builtin_bit_cast(float, __builtin_amdgcn_mov_dpp(__builtin_bit_cast(int, x), CTRL, 0xf, 0xf, true)); }
DI float fold32(float a, float b) {
    auto r = __builtin_amdgcn_permlane32_swap(__float_as_uint(a), __float_as_uint(b), false, false);
    return __uint_as_float(r[0]) + __uint_as_float(r[1]);
}
DI float fold16(float a, float b) {
    auto r = __builtin_amdgcn_permlane16_swap(__float_as_uint(a), __float_as_uint(b), false, false);
    return __uint_as_float(r[0]) + __uint_as_float(r[1]);
}
DI float rowsum16(float v) { v += dppf<0xB1>(v); v += dppf<0x4E>(v); v += dppf<0x124>(v); v += dppf<0x128>(v); return v; }

DI void spg_role(int b, int head, int w, const char* __restrict__ wsp, const __hip_bfloat16* __restrict__ bsp, char* __restrict__ ws, unsigned lds0,
                 const char* __restrict__ x, const char* __restrict__ wo, const char* __restrict__ wout, char* __restrict__ out) {
    const int lane = threadIdx.x & 63;
    const int xc = b & 7, rr = b >> 3;
    const int tt = xc * 4 + (rr & 3), ft = rr >> 2;
    const bool active = ft < 7;
    const unsigned wring = lds0 + K2_W + w * 8192;
    const __amdgpu_buffer_rsrc_t wr = __builtin_amdgcn_make_buffer_rsrc((void*)wsp, (short)0, S_ * S_ * 2, 0x00020000);
    unsigned wso = tt * 64 * 4096 + w * 1024;
    unsigned wvoff[2];
#pragma unroll
    for (int p = 0; p < 2; ++p) wvoff[p] = (lane >> 3) * 4096 + (((lane & 7) ^ ((4 * p + (lane >> 4)) & 7)) << 4);
#define ISSUEW(i) BLDS(wr, wvoff[(i) & 1], wso, (i) * 32768, wring, (i) * 1024)
    auto poll = [&](unsigned idx, unsigned cnt, int n) {
        const unsigned fo = idx * CNT_STRIDE;
        char* fl = ws + WS_FLAG;
#pragma unroll 1
        for (int it = 0; it < SPIN_MAX; ++it) {
            unsigned f = cnt;
            if (lane < n) { const unsigned z = 0; asm volatile("global_atomic_add %0, %1, %2, %3 sc0\n\ts_waitcnt vmcnt(0)" : "=&v"(f) : "v"(fo), "v"(z), "s"(fl) : "memory"); }
            if (__all(f == cnt)) break;
            if ((it & 63) == 63) add16(fl + CNT_HB * CNT_STRIDE, 8);
            __builtin_amdgcn_s_sleep(2);
        }
    };
    f32x16 acc[4][2];
    unsigned zvoff[4], waddr[4];
#define SETUP() do { _Pragma("unroll") for (int j = 0; j < 4; ++j) { \
            acc[j][0] = splat16(0.f); acc[j][1] = splat16(0.f); PIN(acc[j][0]); PIN(acc[j][1]); \
            zvoff[j] = (lane >> 5) * (FH * 16) + (j ^ w) * 512 + (lane & 31) * 16; PIN(zvoff[j]); \
            waddr[j] = wring + (lane & 31) * 128 + (((j * 2 + (lane >> 5)) ^ ((lane >> 1) & 7)) << 4); PIN(waddr[j]); } \
        PIN(wvoff[0]); PIN(wvoff[1]); } while (0)
    ISSUEW(0); ISSUEW(1); ISSUEW(2); ISSUEW(3); ISSUEW(4); ISSUEW(5); ISSUEW(6); ISSUEW(7);
    if (w == 3) {
        const __amdgpu_buffer_rsrc_t kr = __builtin_amdgcn_make_buffer_rsrc((void*)(ws + WS_K), (short)0, 4u << 20, 0x00020000);
        const unsigned lo = lane * 16;
        unsigned kso = head * 65536 + 5 * 1024, kd = lds0 + 5 * 1024;
        poll(CNT_QKV + (head >> 4) * 16 + ((b >> 3) & 15), 48, 1);
        lds_inc(lds0 + K2_MISC + SY_QKV);
        SETUP();
#define KV4() do { BLDSC(kr, lo, kso, 0, kd, K2_K - 1024); BLDSC(kr, lo, kso, WS_V - WS_K, kd, K2_V); BLDSC(kr, lo, kso, 32768, kd, K2_K + 31744 - 1024); \
                   BLDSC(kr, lo, kso, WS_V - WS_K + 32768, kd, K2_V + 32768); kso += 1024; kd += 1024; } while (0)
#pragma unroll 1
        for (int t = 5; t < 20; ++t) KV4();
        WAITV(28);
        PRIO_HI(); lds_inc(lds0 + K2_MISC + SY_KVB); PRIO_LO();
#pragma unroll 1
        for (int t = 20; t < 32; ++t) { KV4(); WAITV(44); }
        PRIO_HI();
    } else {
        const char* src = ws + WS_Z2 + (size_t)b * FH * 16;
        u32x4 zc[5], z0;
        const unsigned o0 = w * 1024 + lane * 16, o4 = o0 + (w < 2 ? 12288 : 9216);
        const unsigned zero = 0;
        if (w == 0) { poll(CNT_Z2 + (b >> 4), 5, 1); lds_inc(lds0 + K2_MISC + SY_Z2); } else lds_wait<1>(lds0 + K2_MISC + SY_Z2, 1);
        GLD16C(zc[0], o0, src, 0); GLD16C(zc[1], o0, src, 3072); GLD16C(zc[2], o0, src + 6144, 0); GLD16C(zc[3], o0, src + 6144, 3072); GLD16C(zc[4], o4, src, 0); GLD16C(z0, zero, src, 0);
        SETUP();
        asm volatile("s_waitcnt vmcnt(0)" : "+v"(zc[0]), "+v"(zc[1]), "+v"(zc[2]), "+v"(zc[3]), "+v"(zc[4]), "+v"(z0) :: "memory");
        PRIO_HI();
        char* dst = ws + WS_Z2N + (size_t)b * FH * 16;
        float sh[8], s1[8], s2[8];
#pragma unroll
        for (int e = 0; e < 4; ++e) { sh[2 * e] = bflo(z0[e]); sh[2 * e + 1] = bfhi(z0[e]); }
        const float m4 = w < 2 ? 1.f : 0.f;
#pragma unroll
        for (int e = 0; e < 8; ++e) {
            float v[5];
#pragma unroll
            for (int j = 0; j < 5; ++j) v[j] = ((e & 1) ? bfhi(zc[j][e >> 1]) : bflo(zc[j][e >> 1])) - sh[e];
            v[4] *= m4;
            s1[e] = ((v[0] + v[1]) + (v[2] + v[3])) + v[4];
            s2[e] = __builtin_fmaf(v[4], v[4], (v[0] * v[0] + v[1] * v[1]) + (v[2] * v[2] + v[3] * v[3]));
        }
        f32x4 q;
#pragma unroll
        for (int i = 0; i < 4; ++i) q[i] = rowsum16(fold16(fold32(s1[i], s2[i]), fold32(s1[i + 4], s2[i + 4])));
        LAS float* part = (LAS float*)(uintptr_t)(lds0 + K2_MISC + 256);
        *(LAS f32x4*)(part + w * 16 + (lane >> 4) * 4) = q;
        asm volatile("s_waitcnt lgkmcnt(0)" ::: "memory");
        lds_inc(lds0 + K2_MISC + SY_LN);
        lds_wait(lds0 + K2_MISC + SY_LN, 3);
        float rs[8], nm[8];
        {
            f32x4 t[4];
#pragma unroll
            for (int i = 0; i < 4; ++i) t[i] = (*(LAS f32x4*)(part + i * 4) + *(LAS f32x4*)(part + 16 + i * 4)) + *(LAS f32x4*)(part + 32 + i * 4);
#pragma unroll
            for (int e = 0; e < 8; ++e) {
                float mean = t[e >> 2][e & 3] * (1.0f / FH);
                float var = fmaxf(t[2 + (e >> 2)][e & 3] * (1.0f / FH) - mean * mean, 0.f);
                rs[e] = __builtin_amdgcn_rsqf(var + 1e-5f);
                nm[e] = -(mean + sh[e]) * rs[e];
            }
        }
#pragma unroll
        for (int j = 0; j < 5; ++j) {
            u32x4 v;
#pragma unroll
            for (int e = 0; e < 4; ++e) v[e] = pk2(__builtin_fmaf(bflo(zc[j][e]), rs[2 * e], nm[2 * e]), __builtin_fmaf(bfhi(zc[j][e]), rs[2 * e + 1], nm[2 * e + 1]));
            if (j < 4) GST16(o0, v, dst + j * 3072); else GST16(o4, v, dst);
        }
        WAITV(0);
        unsigned old = 0;
        if (lane == 0) old = __hip_atomic_fetch_add((LAS unsigned*)(uintptr_t)(lds0 + K2_MISC + SY_LNST), 1u, __ATOMIC_RELAXED, __HIP_MEMORY_SCOPE_WORKGROUP);
        old = __builtin_amdgcn_readfirstlane(old);
        if (old == 2) {
            add16(ws + WS_FLAG + (size_t)(b >> 5) * CNT_STRIDE, 11);
            if (!active) { WAITV(0); lds_inc(lds0 + K2_MISC + SY_FLAG); }
        }
    }
    const unsigned l16 = lane * 16;
    auto reg = [&](int ww) { return lds0 + ((ww & 1) ? K2_V + (ww >> 1) * 32768 : K2_K + (ww >> 1) * 31744); };
    if (active) {
        const char* zb = ws + WS_Z2N + ((size_t)w * 64 * FH + ft * 128) * 16;
        u32x4 zf[4][4];
#define ZPTR(c, ks) (zb + (size_t)((c) * 4 + (ks)) * (2 * FH * 16))
        if (w == 0) { poll(lane < 8 ? ((b >> 3) & 15) * 8 + lane : CNT_Z1 + (tt >> 1), lane < 8 ? 32 : 5, 9); lds_inc(lds0 + K2_MISC + SY_FLAG); }
        else lds_wait<2>(lds0 + K2_MISC + SY_FLAG, 1);
        PRIO_LO();
#pragma unroll
        for (int ks = 0; ks < 4; ++ks)
#pragma unroll
            for (int j = 0; j < 4; ++j) GLD16S(zf[ks][j], zvoff[j], ZPTR(0, ks));
        if (w == 3) { WAITV(16); PRIO_HI(); lds_inc(lds0 + K2_MISC + SY_KVB); PRIO_LO(); }
#define ZWAIT(n, ks) asm volatile("s_waitcnt vmcnt(" #n ")" : "+v"(zf[ks][0]), "+v"(zf[ks][1]), "+v"(zf[ks][2]), "+v"(zf[ks][3]) :: "memory")
#define KPAIR(ks, j_, MORE) do { \
        acc[j_][0] = MFMA32(__builtin_bit_cast(bf16x8, zf[ks][j_]), wf[0][ks], acc[j_][0]); PIN(acc[j_][0]); SB(); \
        if (MORE && ks == 0) ISSUEW(2 * j_); \
        acc[j_][1] = MFMA32(__builtin_bit_cast(bf16x8, zf[ks][j_]), wf[1][ks], acc[j_][1]); PIN(acc[j_][1]); SB(); \
        if (MORE && ks == 0) ISSUEW(2 * j_ + 1); \
        if (MORE) GLD16S(zf[ks][j_], zvoff[j_], ZPTR(c + 1, ks)); } while (0)
#define KSTEP(ks, MORE) do { KPAIR(ks, 0, MORE); KPAIR(ks, 1, MORE); KPAIR(ks, 2, MORE); KPAIR(ks, 3, MORE); } while (0)
#define WREAD() _Pragma("unroll") for (int ks = 0; ks < 4; ++ks) { wf[0][ks] = ldsfrag(waddr[ks]); wf[1][ks] = ldsfrag(waddr[ks] + 4096); } \
        asm volatile("s_waitcnt lgkmcnt(0)" : "+v"(wf[0][0]), "+v"(wf[0][1]), "+v"(wf[0][2]), "+v"(wf[0][3]), "+v"(wf[1][0]), "+v"(wf[1][1]), "+v"(wf[1][2]), "+v"(wf[1][3]) :: "memory")
#pragma unroll 1
        for (int c = 0; c < 7; ++c) {
            bf16x8 wf[2][4];
            wso += 128;
            ZWAIT(12, 0);
            WREAD();
            KSTEP(0, true);
            ZWAIT(20, 1); KSTEP(1, true);
            ZWAIT(20, 2); KSTEP(2, true);
            ZWAIT(20, 3); KSTEP(3, true);
        }
        const int fs0 = ft * 8 + w * 2;
        const char* z1p = ws + WS_Z1 + ((size_t)tt * 2 * 56 + fs0) * 1024;
        const char* bp = (const char*)bsp + tt * 128;
        const unsigned bo = (lane & 31) * 2;
        unsigned braw[2];
        {
            const int c = 7; (void)c;
            bf16x8 wf[2][4];
            ZWAIT(12, 0);
            WREAD();
            PRIO_HI(); lds_inc(lds0 + K2_MISC + SY_MAIN); PRIO_LO();
            KSTEP(0, false);
            GLD16C(zf[0][0], l16, z1p, 0); GLD16C(zf[0][1], l16, z1p, 1024); GLD16C(zf[0][2], l16, z1p + 56 * 1024, 0); GLD16C(zf[0][3], l16, z1p + 56 * 1024, 1024);
            asm volatile("global_load_ushort %0, %1, %2" : "=v"(braw[0]) : "v"(bo), "s"(bp) : "memory");
            asm volatile("global_load_ushort %0, %1, %2 offset:64" : "=v"(braw[1]) : "v"(bo), "s"(bp) : "memory");
            ZWAIT(14, 1); KSTEP(1, false);
            ZWAIT(10, 2); KSTEP(2, false);
            ZWAIT(6, 3); KSTEP(3, false);
        }
        PRIO_HI();
        lds_wait(lds0 + K2_MISC + SY_P16, 4);
        lds_wait(lds0 + K2_MISC + SY_MAIN, 4);
        auto blk = [&](int dstw, int j) { return (j < 3 ? reg(dstw) + (j - 1) * 8192 : lds0 + K2_W + dstw * 8192) + l16; };
        unsigned wa[3] = {blk(1 ^ w, 1), blk(2 ^ w, 2), blk(3 ^ w, 3)};
        PIN(wa[0]); PIN(wa[1]); PIN(wa[2]); SB();
        PRIO_LO();
#pragma unroll
        for (int j = 1; j < 4; ++j) {
            const unsigned a = wa[j - 1];
#pragma unroll
            for (int t = 0; t < 2; ++t)
#pragma unroll
                for (int q = 0; q < 4; ++q) {
                    f32x4 v = {acc[j][t][4 * q], acc[j][t][4 * q + 1], acc[j][t][4 * q + 2], acc[j][t][4 * q + 3]};
                    *(LAS f32x4*)(uintptr_t)(a + (t * 4 + q) * 1024) = v;
                }
        }
        asm volatile("s_waitcnt lgkmcnt(0)" ::: "memory");
        PRIO_HI();
        lds_inc(lds0 + K2_MISC + SY_RED);
        lds_wait(lds0 + K2_MISC + SY_RED, 4);
#pragma unroll
        for (int j = 1; j < 4; ++j) {
            const unsigned a = blk(w, j);
#pragma unroll
            for (int t = 0; t < 2; ++t)
#pragma unroll
                for (int q = 0; q < 4; ++q) {
                    f32x4 v = *(LAS f32x4*)(uintptr_t)(a + (t * 4 + q) * 1024);
#pragma unroll
                    for (int e = 0; e < 4; ++e) acc[0][t][4 * q + e] += v[e];
                }
        }
        asm volatile("s_waitcnt vmcnt(0)" : "+v"(zf[0][0]), "+v"(zf[0][1]), "+v"(zf[0][2]), "+v"(zf[0][3]), "+v"(braw[0]), "+v"(braw[1]) :: "memory");
        const float bias[2] = {bflo(braw[0]), bflo(braw[1])};
#pragma unroll
        for (int t = 0; t < 2; ++t)
#pragma unroll
            for (int s = 0; s < 2; ++s) {
                auto a0 = __builtin_amdgcn_permlane32_swap(zf[0][2 * t + s][0], zf[0][2 * t + s][2], false, false);
                auto a1 = __builtin_amdgcn_permlane32_swap(zf[0][2 * t + s][1], zf[0][2 * t + s][3], false, false);
                const unsigned zc[4] = {a0[0], a1[0], a0[1], a1[1]};
                unsigned p[4];
#pragma unroll
                for (int e = 0; e < 4; ++e)
                    p[e] = pk2(bflo(zc[e]) * (acc[0][t][8 * s + 2 * e] + bias[t]), bfhi(zc[e]) * (acc[0][t][8 * s + 2 * e + 1] + bias[t]));
                auto s0 = __builtin_amdgcn_permlane32_swap(p[0], p[2], false, false);
                auto s1 = __builtin_amdgcn_permlane32_swap(p[1], p[3], false, false);
                u32x4 v = {s0[0], s1[0], s0[1], s1[1]};
                GST16(l16, v, ws + WS_CAT + ((size_t)(tt * 2 + t) * CAT_KS + 32 + fs0 + s) * 1024);
            }
        publish(ws + WS_FLAG + (size_t)(CNT_REC + tt * 8 + (ft * 4 + w) / 7) * CNT_STRIDE);
    } else {
        const int id = (rr & 3) * 4 + w;
        if (w == 3) { WAITV(0); lds_inc(lds0 + K2_MISC + SY_KVB); }
        lds_wait<4>(lds0 + K2_MISC + SY_KVB, 2);
        lds_wait<4>(lds0 + K2_MISC + SY_FLAG, 1);
        PRIO_LO();
#pragma unroll 1
        for (int c = 1; c < 8; ++c) { wso += 128; ISSUEW(0); ISSUEW(1); ISSUEW(2); ISSUEW(3); ISSUEW(4); ISSUEW(5); ISSUEW(6); ISSUEW(7); WAITV(32); }
        lds_wait<32>(lds0 + K2_MISC + SY_P16, 4);
#pragma unroll 1
        for (int i = 0; i < 56; ++i) { glds16(wout + (size_t)(id * 56 + i) * 1024, l16, wring); if ((i & 7) == 7) WAITV(32); }
#pragma unroll 1
        for (int i = 0; i < 32; ++i) { glds16(wo + (size_t)(id * 32 + i) * 1024, l16, wring); if ((i & 7) == 7) WAITV(32); }
#pragma unroll 1
        for (int i = 0; i < 16; ++i) glds16(x + (size_t)(xc * 256 + id * 16 + i) * 1024, l16, wring);
        WAITV(0);
        PRIO_HI();
    }
    {
        const __amdgpu_buffer_rsrc_t hr = __builtin_amdgcn_make_buffer_rsrc((void*)wout, (short)0, FH * 1024, 0x00020000);
        const __amdgpu_buffer_rsrc_t orr = __builtin_amdgcn_make_buffer_rsrc((void*)wo, (short)0, D_ * 1024, 0x00020000);
        unsigned bvoff = ((lane >> 2) & 7) * 1024 + (lane >> 5) * 64 + (lane & 3) * 16;
        const unsigned hso = ft * 128 + w * 14 * 16384, oso = ft * 128 + w * 8 * 16384;
        const unsigned rg = reg(w);
#define FB(rs, so, s, base, off) do { BLDS(rs, bvoff, so, (s) * 16384, base, off); BLDS(rs, bvoff, so, (s) * 16384 + 8192, base, (off) + 1024); } while (0)
        const unsigned bl = (lane >> 5) * 1024 + (w & 1) * 512 + ((lane & 15) >> 2) * 64 + ((lane >> 4) & 1) * 32 + (lane & 3) * 8;
        unsigned br0 = rg + bl, br1 = rg + (bl ^ 512), bw0 = wring + bl, bw1 = wring + (bl ^ 512);
        unsigned xo = (lane & 31) * 1024 + (lane >> 5) * 16;
        PIN(bvoff); PIN(br0); PIN(br1); PIN(bw0); PIN(bw1); PIN(xo); SB();
        PRIO_LO();
        FB(hr, hso, 0, rg, 0); FB(hr, hso, 1, rg, 2048); FB(hr, hso, 2, rg, 4096); FB(hr, hso, 3, rg, 6144);
        FB(hr, hso, 4, rg, 8192); FB(hr, hso, 5, rg, 10240); FB(hr, hso, 6, rg, 12288); FB(hr, hso, 7, rg, 14336);
        FB(hr, hso, 8, wring, 0); FB(hr, hso, 9, wring, 2048); FB(hr, hso, 10, wring, 4096); FB(hr, hso, 11, wring, 6144);
        const size_t oo_ = (size_t)(tt * 64 + (w >> 1) * 32) * 1024 + (ft * 64 + (w & 1) * 32) * 2;
        u32x4 xv[2];
        GLD16(xv[0], xo, x + oo_, 0); GLD16(xv[1], xo, x + oo_, 32);
        PRIO_HI();
        lds_wait<8>(lds0 + K2_MISC + SY_P26, 4);
        FB(hr, hso, 12, rg, 16384); FB(hr, hso, 13, rg, 18432);
        const char* a0p = ws + WS_CAT + (size_t)(tt * 2 + (w >> 1)) * CAT_KS * 1024;
        const char* a1p = ws + WS_CAT + (size_t)(tt * 2 + ((w >> 1) ^ 1)) * CAT_KS * 1024;
        u32x4 af[14][2];
        f32x16 fc[2][2];
        bf16x8 bq[2][2];
#define SLOT0(s) ((s) < 8 ? br0 + (s) * 2048 : (s) < 12 ? bw0 + ((s) - 8) * 2048 : br0 + 16384 + ((s) - 12) * 2048)
#define SLOT1(s) ((s) < 8 ? br1 + (s) * 2048 : (s) < 12 ? bw1 + ((s) - 8) * 2048 : br1 + 16384 + ((s) - 12) * 2048)
#define BQ(s) do { bq[(s) & 1][0] = trfrag(SLOT0(s), SLOT0(s) + 256); bq[(s) & 1][1] = trfrag(SLOT1(s), SLOT1(s) + 256); } while (0)
#define AWAIT(n, s) asm volatile("s_waitcnt vmcnt(%c2)" : "+v"(af[s][0]), "+v"(af[s][1]) : "i"(n) : "memory")
#define FMM(s, FIRST) do { _Pragma("unroll") for (int i_ = 0; i_ < 2; ++i_) _Pragma("unroll") for (int j_ = 0; j_ < 2; ++j_) { \
            fc[i_][j_] = MFMA32(bq[(s) & 1][j_], __builtin_bit_cast(bf16x8, af[s][i_]), (FIRST) ? splat16(0.f) : fc[i_][j_]); PIN(fc[i_][j_]); } SB(); } while (0)
        poll(CNT_REC + tt * 8 + w, 7, 1);
        PRIO_LO();
#pragma unroll
        for (int s = 0; s < 14; ++s) { GLD16S(af[s][0], l16, a0p + (32 + w * 14 + s) * 1024); GLD16S(af[s][1], l16, a1p + (32 + w * 14 + s) * 1024); }
        BQ(0);
#define HST(s) do { if (s < 13) BQ(s + 1); AWAIT(2 * (13 - s) + (s > 8 ? 16 : 0), s); FMM(s, s == 0); } while (0)
        HST(0); HST(1); HST(2); HST(3); HST(4); HST(5); HST(6); HST(7); HST(8);
        FB(orr, oso, 0, rg, 0); FB(orr, oso, 1, rg, 2048); FB(orr, oso, 2, rg, 4096); FB(orr, oso, 3, rg, 6144);
        FB(orr, oso, 4, rg, 8192); FB(orr, oso, 5, rg, 10240); FB(orr, oso, 6, rg, 12288); FB(orr, oso, 7, rg, 14336);
        HST(9); HST(10); HST(11); HST(12); HST(13);
        PRIO_HI();
        poll(CNT_REC + tt * 8 + 4 + w, 8, 1);
#pragma unroll
        for (int s = 0; s < 8; ++s) { GLD16S(af[s][0], l16, a0p + (w * 8 + s) * 1024); GLD16S(af[s][1], l16, a1p + (w * 8 + s) * 1024); }
        BQ(0);
#define OST(s) do { if (s < 7) BQ(s + 1); AWAIT(2 * (7 - s), s); FMM(s, false); } while (0)
        OST(0); OST(1); OST(2); OST(3); OST(4); OST(5); OST(6); OST(7);
#pragma unroll
        for (int j = 1; j < 4; ++j)
#pragma unroll
            for (int q = 0; q < 4; ++q) {
                const f32x16& t = fc[j >> 1][j & 1];
                f32x4 v = {t[4 * q], t[4 * q + 1], t[4 * q + 2], t[4 * q + 3]};
                *(LAS f32x4*)(uintptr_t)(rg + (j * 4 + q) * 1024 + l16) = v;
            }
        asm volatile("s_waitcnt lgkmcnt(0)" ::: "memory");
        lds_inc(lds0 + K2_MISC + SY_FIN);
        lds_wait(lds0 + K2_MISC + SY_FIN, 4);
#pragma unroll
        for (int j = 1; j < 4; ++j)
#pragma unroll
            for (int q = 0; q < 4; ++q) {
                f32x4 v = *(LAS f32x4*)(uintptr_t)(reg(w ^ j) + (j * 4 + q) * 1024 + l16);
#pragma unroll
                for (int e = 0; e < 4; ++e) fc[0][0][4 * q + e] += v[e];
            }
        asm volatile("" : "+v"(xv[0]), "+v"(xv[1]));
#pragma unroll
        for (int s = 0; s < 2; ++s) {
            auto a0 = __builtin_amdgcn_permlane32_swap(xv[s][0], xv[s][2], false, false);
            auto a1 = __builtin_amdgcn_permlane32_swap(xv[s][1], xv[s][3], false, false);
            const unsigned xc4[4] = {a0[0], a1[0], a0[1], a1[1]};
            unsigned p[4];
#pragma unroll
            for (int e = 0; e < 4; ++e) p[e] = pk2(bflo(xc4[e]) + fc[0][0][8 * s + 2 * e], bfhi(xc4[e]) + fc[0][0][8 * s + 2 * e + 1]);
            auto s0 = __builtin_amdgcn_permlane32_swap(p[0], p[2], false, false);
            auto s1 = __builtin_amdgcn_permlane32_swap(p[1], p[3], false, false);
            u32x4 v = {s0[0], s1[0], s0[1], s1[1]};
            *(u32x4*)(out + oo_ + xo + s * 32) = v;
        }
    }
}

__global__ __launch_bounds__(512, 2) NOPK void k_block(const char* __restrict__ x, const char* __restrict__ wqkv, const char* __restrict__ wo, const char* __restrict__ win,
                                                       const char* __restrict__ wsp, const __hip_bfloat16* __restrict__ bsp, const char* __restrict__ wout,
                                                       char* __restrict__ ws, char* __restrict__ out) {
    extern __shared__ __attribute__((aligned(16))) char smem[];
    const unsigned lds0 = (unsigned)(uintptr_t)(LAS char*)smem;
    phase1(x, wqkv, win, ws, wsp, lds0);
    const int wid = __builtin_amdgcn_readfirstlane(threadIdx.x >> 6);
    const int b = blockIdx.x;
    const int head = (b & 7) * 4 + (b >> 6);
    if (wid < 4) att_role(head, (b >> 3) & 7, wid, ws, lds0);
    else spg_role(b, head, wid - 4, wsp, bsp, ws, lds0, x, wo, wout, out);
}

extern "C" void kernel_launch(void* const* d_in, const int* in_sizes, int n_in, void* d_out, int out_size, void* d_ws, size_t ws_size,
                              hipStream_t stream) {
    (void)hipFuncSetAttribute((const void*)k_block, hipFuncAttributeMaxDynamicSharedMemorySize, K2_LDS);
    hipLaunchKernelGGL(k_block, dim3(256), dim3(512), K2_LDS, stream, (const char*)d_in[0], (const char*)d_in[1], (const char*)d_in[2], (const char*)d_in[4],
                       (const char*)d_in[5], (const __hip_bfloat16*)d_in[6], (const char*)d_in[7], (char*)d_ws, (char*)d_out);
}
```

```cpp
#include <hip/hip_runtime.h>
#include <hip/hip_bf16.h>
#include <stdint.h>
#include <utility>

typedef __attribute__((ext_vector_type(2))) __bf16 bf16x2;
typedef __attribute__((ext_vector_type(8))) __bf16 bf16x8;
typedef __attribute__((ext_vector_type(16))) __bf16 bf16x16;
typedef __attribute__((ext_vector_type(2))) float f32x2;
typedef __attribute__((ext_vector_type(4))) float f32x4;
typedef __attribute__((ext_vector_type(8))) float f32x8;
typedef __attribute__((ext_vector_type(16))) float f32x16;
typedef __attribute__((ext_vector_type(4))) unsigned u32x4;
typedef __attribute__((ext_vector_type(4))) short s16x4;
#define LAS __attribute__((address_space(3)))
#define DI __device__ __forceinline__
#if defined(__HIP_DEVICE_COMPILE__)
#define NOPK __attribute__((target("no-packed-fp32-ops")))
#else
#define NOPK
#endif
#define SB() __builtin_amdgcn_sched_barrier(0)
#define PIN(x) asm volatile("" : "+v"(x))
#define WAITV(n) asm volatile("s_waitcnt vmcnt(" #n ")" ::: "memory")
#define MFMA16(a, b, c) __builtin_amdgcn_mfma_f32_16x16x32_bf16(a, b, c, 0, 0, 0)
#define MFMA32(a, b, c) __builtin_amdgcn_mfma_f32_32x32x16_bf16(a, b, c, 0, 0, 0)

constexpr int S_ = 2048, D_ = 512, FH = 896;
constexpr size_t WS_Q = 0, WS_K = 2u << 20, WS_V = 4u << 20, WS_Z1 = 6u << 20, WS_Z2 = 10u << 20, WS_Z2N = 14u << 20,
                 WS_CAT = 18u << 20, WS_FLAG = 24u << 20;
constexpr int CNT_STRIDE = 256, CNT_REC = 128, CNT_QKV = 384, CNT_Z2 = 416, CNT_Z1 = 432, CNT_HB = 448;
constexpr int CAT_KS = 88;

#define GST16(voff, val, sbase) asm volatile("global_store_dwordx4 %0, %1, %2 sc1\n\ts_nop 1" :: "v"(voff), "v"(val), "s"(sbase) : "memory")

#define GLD16(dst, voff, sbase, IMM) asm volatile("global_load_dwordx4 %0, %1, %2 offset:" #IMM : "=v"(dst) : "v"(voff), "s"(sbase) : "memory")

DI void glds16(const void* sbase, unsigned voff, unsigned ldsdst) {
    unsigned keep;
    asm volatile("s_mov_b32 %0, m0\n\ts_mov_b32 m0, %3\n\ts_nop 0\n\tglobal_load_lds_dwordx4 %1, %2\n\ts_mov_b32 m0, %0"
                 : "=&s"(keep) : "v"(voff), "s"(sbase), "s"(ldsdst) : "memory");
}
DI unsigned pk2(float a, float b) { f32x2 v = {a, b}; return __builtin_bit_cast(unsigned, __builtin_convertvector(v, bf16x2)); }
DI float bflo(unsigned u) { return __uint_as_float(u << 16); }
DI float bfhi(unsigned u) { return __uint_as_float(u & 0xffff0000u); }
DI s16x4 trread(unsigned addr) { return __builtin_amdgcn_ds_read_tr16_b64_v4i16((LAS s16x4*)(uintptr_t)addr); }
DI bf16x8 trfrag(unsigned addr0, unsigned addr1) {
    s16x4 lo = trread(addr0), hi = trread(addr1);
    return __builtin_bit_cast(bf16x8, __builtin_shufflevector(lo, hi, 0, 1, 2, 3, 4, 5, 6, 7));
}
DI bf16x8 ldsfrag(unsigned addr) { return *(const LAS bf16x8*)(uintptr_t)addr; }
DI float gelu_t(float u) {
    const float k1 = -2.3022081985f;
    float w = u * __builtin_fmaf(u * u, k1 * 0.044715f, k1);
    float e = __builtin_amdgcn_exp2f(w);
    return u * __builtin_amdgcn_rcpf(1.0f + e);
}

constexpr int K2_K = 0, K2_V = 62 * 1024, K2_W = 126 * 1024, K2_ONES = K2_W + 32768, K2_MISC = K2_ONES + 1024, K2_LDS = K2_MISC + 1024;
enum { SY_KVA = 0, SY_KVB = 4, SY_P26 = 12, SY_LN = 16, SY_LNST = 20, SY_RED = 24, SY_FLAG = 28, SY_CMB = 36  , SY_P16 = 44, SY_MAIN = 48, SY_FIN = 52, SY_QKV = 56, SY_Z2 = 60, SY_HB = 128   };
constexpr int SPIN_MAX = 1 << 21, LSPIN_MAX = 1 << 24;

#define GLD16C(dst, voff, sbase, IMM) asm volatile("global_load_dwordx4 %0, %1, %2 offset:" #IMM " sc1" : "=v"(dst) : "v"(voff), "s"(sbase) : "memory")
DI void hb_sample(const char* p, unsigned ldsdst) {
    unsigned t;
    asm volatile("v_mov_b32 %0, 0\n\ts_mov_b32 m0, %2\n\ts_mov_b64 exec, 1\n\tglobal_load_lds_dword %0, %1 sc1\n\ts_mov_b64 exec, -1" : "=&v"(t) : "s"(p), "s"(ldsdst) : "memory");
}
DI void publish(char* p) {
    unsigned t0, t1;
    asm volatile("v_mov_b32 %0, 0\n\tv_mov_b32 %1, 1\n\ts_waitcnt vmcnt(0)\n\ts_mov_b64 exec, 1\n\tglobal_atomic_add %0, %1, %2\n\ts_mov_b64 exec, -1" : "=&v"(t0), "=&v"(t1) : "s"(p) : "memory");
}
DI void add16(char* p, unsigned stride_shift) {
    unsigned t0, t1;
    asm volatile("v_mbcnt_lo_u32_b32 %0, -1, 0\n\tv_mov_b32 %1, 1\n\tv_lshlrev_b32 %0, %3, %0\n\ts_mov_b64 exec, 0xffff\n\tglobal_atomic_add %0, %1, %2\n\ts_mov_b64 exec, -1"
                 : "=&v"(t0), "=&v"(t1) : "s"(p), "s"(stride_shift) : "memory");
}

constexpr int K1_MISC = 9 * 16384;
constexpr int K1_SUM = K1_MISC;
constexpr int K1_CS = K1_MISC + 1024;
constexpr int K1_PRIV = K1_MISC + 1024 + 2048;
constexpr int K1_JUNK = K1_PRIV + 8 * 256;
constexpr int K1_LDS = K1_JUNK + 1024;
static_assert(K1_LDS <= K2_ONES, "phase 2 words are initialised while phase 1 runs");
template <class F, int... I> DI void sfor_(F&& f, std::integer_sequence<int, I...>) { (f(std::integral_constant<int, I>{}), ...); }
template <int N, class F> DI void sfor(F&& f) { sfor_(f, std::make_integer_sequence<int, N>{}); }
#define LAMBDA(a) [&](auto a) __attribute__((always_inline))
DI unsigned word(bf16x8 v, int e) { const u32x4 u = __builtin_bit_cast(u32x4, v); return u[e]; }
DI f32x16 splat16(float v) { f32x16 r; for (int i = 0; i < 16; ++i) r[i] = v; return r; }

#define BLDS(rsrc, voff, soff, SIMM, m0b, MIMM) do { unsigned t_; \
    asm volatile("s_add_u32 m0, %4, %c6\n\ts_add_u32 %0, %3, %c5\n\tbuffer_load_dwordx4 %1, %2, %0 offen lds" \
                 : "=&s"(t_) : "v"(voff), "s"(rsrc), "s"(soff), "s"(m0b), "i"(SIMM), "i"(MIMM) : "scc", "memory"); } while (0)

#define BLDSC(rsrc, voff, soff, SIMM, m0b, MIMM) do { unsigned t_; \
    asm volatile("s_add_u32 m0, %4, %c6\n\ts_add_u32 %0, %3, %c5\n\tbuffer_load_dwordx4 %1, %2, %0 offen sc1 lds" \
                 : "=&s"(t_) : "v"(voff), "s"(rsrc), "s"(soff), "s"(m0b), "i"(SIMM), "i"(MIMM) : "scc", "memory"); } while (0)

template <bool ORI2, bool GELU>
DI void k1_body(const char* __restrict__ x, const char* __restrict__ W, unsigned col0  , int np  , int rp, char* __restrict__ dst  ,
                unsigned dvoff, unsigned jstride, unsigned pstride, float oscale, char* __restrict__ ws, unsigned lds0, const char* __restrict__ wsp) {
    constexpr int LDW = GELU ? 3584 : 3072, NP = GELU ? 3 : 4;
    const int tid = threadIdx.x, lane = tid & 63;
    const int wid = __builtin_amdgcn_readfirstlane(tid >> 6);
    const int wm = wid & 3, wn = wid >> 2;
    const int r = lane & 31, h = lane >> 5;
    const bool leader = blockIdx.x == 0 && wid == 1;
    char* hb = ws + WS_FLAG + CNT_HB * CNT_STRIDE;
    const __amdgpu_buffer_rsrc_t xr = __builtin_amdgcn_make_buffer_rsrc((void*)x, (short)0, S_ * D_ * 2, 0x00020000);
    const __amdgpu_buffer_rsrc_t wr = __builtin_amdgcn_make_buffer_rsrc((void*)W, (short)0, D_ * LDW, 0x00020000);
    unsigned xso[4], wso[4];
#pragma unroll
    for (int s = 0; s < 4; ++s) { xso[s] = rp * 131072 + wid * 16384 + s * 256; wso[s] = col0 + (wid * 16 + s * 128) * LDW; }
    unsigned xvoff[2];
#pragma unroll
    for (int i = 0; i < 2; ++i) xvoff[i] = (i * 8 + (lane >> 3)) * 1024 + (((lane & 7) ^ ((4 * i + (lane >> 4)) & 7)) << 4);
    const unsigned wvoff = ((lane >> 2) & 7) * LDW + (lane >> 5) * 64 + (lane & 3) * 16;
    const unsigned sdst = lds0 + wid * 2048;
#define ISSX1(c, slot, i) BLDS(xr, xvoff[i], xso[(c) >> 1], ((c) & 1) * 128, sdst, (slot) * 16384 + (i) * 1024)
#define ISSW1(pp, st, slot, i) BLDS(wr, wvoff, wso[st], (pp) * 128 + (i) * 8 * LDW, sdst, (slot) * 16384 + (i) * 1024)
#define ISSX(c, slot) do { ISSX1(c, slot, 0); ISSX1(c, slot, 1); } while (0)
#define ISSW(pp, st, slot) do { ISSW1(pp, st, slot, 0); ISSW1(pp, st, slot, 1); } while (0)
    ISSX(0, 0); ISSX(1, 1); ISSW(0, 0, 2);
    ISSX(2, 3); ISSX(3, 4); ISSW(0, 1, 5);
#define PERM(q) ((q) == 0 ? 0 : (q) == 1 ? 4 : (q) < 5 ? (q) - 1 : (q))
    unsigned xad[4], bad[4];
#pragma unroll
    for (int t = 0; t < 4; ++t) {
        xad[t] = lds0 + (wm * 32 + r) * 128 + (((2 * (t ^ wm) + h) ^ ((r >> 1) & 7)) << 4);
        bad[t] = lds0 + (2 * (t ^ wm) + h) * 1024 + wn * 512 + ((lane & 15) >> 2) * 64 + ((lane >> 4) & 1) * 32 + (lane & 3) * 8;
    }
    const unsigned selw = ((lane & 15) == ((lane >> 4) & 1)) ? 0x3f803f80u : 0u;
    u32x4 selu = {selw, selw, selw, selw};
    const bf16x8 sel = __builtin_bit_cast(bf16x8, selu);
    bf16x8 af[32];
    f32x16 accA, accB;
    float st0 = 0.f, st1 = 0.f;
    const unsigned one2 = 0x3f803f80u;
    f32x4 csum;
    const f32x4 z4 = {0, 0, 0, 0};
#define MM(bf, a, c) (ORI2 ? MFMA32(a, bf, c) : MFMA32(bf, a, c))
#define MM16(bf, a) asm volatile("v_mfma_f32_32x32x16_bf16 %0, %1, %2, %3" : "+v"(accB) : "v"(ORI2 ? (a) : (bf)), "v"(ORI2 ? (bf) : (a)), "v"(accA))
#define BFR(q, off) trfrag(bad[PERM(q) & 3] + (off) + (PERM(q) >> 2) * 8192, bad[PERM(q) & 3] + (off) + (PERM(q) >> 2) * 8192 + 256)
#define MAIN(i, bf) do { if ((i) == 0) accA = MM(bf, af[i], splat16(0.f)); else if ((i) < 16) accA = MM(bf, af[i], accA); else if ((i) == 16) MM16(bf, af[i]); else accB = MM(bf, af[i], accB); \
        if ((i) < 16) PIN(accA); else PIN(accB); \
        if ((i) == 18) asm volatile("" :: "v"(accA));        \
        if (((i) & 7) < 2) { csum = MFMA16(sel, bf, (i) == 0 ? z4 : csum); PIN(csum); } } while (0)
#define DA(k, l) "v_dot2c_f32_bf16 %0, %" #k ", %" #k "\n\tv_dot2c_f32_bf16 %1, %" #l ", %" #l "\n\t"
#define DB(k, l) "v_dot2c_f32_bf16 %0, %" #k ", %2\n\tv_dot2c_f32_bf16 %1, %" #l ", %2\n\t"
#define AW(i, e) "v"(word(af[i], e))
#define STATS4(i) asm volatile("s_cmp_lg_u32 %3, 0\n\ts_cbranch_scc1 1f\n\t" DA(4, 5) DA(6, 7) DA(8, 9) DA(10, 11) DA(12, 13) DA(14, 15) DA(16, 17) DA(18, 19) "s_branch 2f\n1:\n\t" \
            DB(4, 5) DB(6, 7) DB(8, 9) DB(10, 11) DB(12, 13) DB(14, 15) DB(16, 17) DB(18, 19) "2:" : "+v"(st0), "+v"(st1) : "v"(one2), "s"(wn), \
            AW(i, 0), AW(i, 1), AW(i, 2), AW(i, 3), AW(i + 1, 0), AW(i + 1, 1), AW(i + 1, 2), AW(i + 1, 3), AW(i + 2, 0), AW(i + 2, 1), AW(i + 2, 2), AW(i + 2, 3), \
            AW(i + 3, 0), AW(i + 3, 1), AW(i + 3, 2), AW(i + 3, 3) : "scc")
#define BAR(n) asm volatile("s_waitcnt vmcnt(" #n ") lgkmcnt(0)\n\ts_barrier" ::: "memory")
    asm volatile("" : "=v"(accB));
    const unsigned csw = lds0 + K1_CS + wn * 512 + (lane & 15) * 16 + wm * 4;
    const unsigned csr = lds0 + K1_CS + wn * 512 + r * 16;
    const unsigned priv = lds0 + K1_PRIV + wid * 256;
#define CSWRITE(par) do { if (lane < 16) { *(LAS float*)(uintptr_t)(csw + (par)) = csum[0]; *(LAS float*)(uintptr_t)(csw + (par) + 256) = csum[1]; } } while (0)
    bf16x8 bq[4];
    {
        const unsigned pso = col0 + ((rp & 7) * 64 + wid * 8) * LDW, junk = lds0 + K1_JUNK;
#pragma unroll
        for (int t = 0; t < 4; ++t) bad[t] += 2 * 16384;
        sfor<35>(LAMBDA(U) {
            constexpr int u = U, i = u - 3, st = u >> 3, q = u & 7, ip = i < 0 ? 0 : i, s4 = st & 3;
            if (q == 0) {
                if (st == 4) {
                    const float t = st0 + st1;
                    auto rr = __builtin_amdgcn_permlane32_swap(__float_as_uint(t), __float_as_uint(t), false, false);
                    *(LAS float*)(uintptr_t)(lds0 + K1_SUM + wn * 512 + (wm * 32 + r) * 4) = __uint_as_float(rr[0]) + __uint_as_float(rr[1]);
                    CSWRITE(0);
                }
                if (st == 0) BAR(6); else if (st < 3) BAR(7); else if (st == 3) BAR(6); else BAR(3);
                if (st > 0) {
#pragma unroll
                    for (int t = 0; t < 4; ++t) { if (st < 4) xad[t] += st == 3 ? -6 * 16384 : 3 * 16384; bad[t] += st == 3 ? -6 * 16384 : st == 4 ? 16384 : 3 * 16384; }
                }
                if (st == 4 && leader) { WAITV(0); add16(hb, 8); }
            }
            if constexpr (st < 4 && q < 4) { af[st * 8 + 2 * q] = ldsfrag(xad[PERM(2 * q) & 3] + (PERM(2 * q) >> 2) * 16384); af[st * 8 + 2 * q + 1] = ldsfrag(xad[PERM(2 * q + 1) & 3] + (PERM(2 * q + 1) >> 2) * 16384); }
            bq[u & 3] = BFR(q, 0);
            if (i >= 0) MAIN(ip, bq[ip & 3]);
            if (st == 0) { if (q < 2) ISSX1(4, 6, q & 1); else if (q < 4) ISSX1(5, 7, q & 1); else if (q < 6) ISSW1(0, 2, 8, q & 1); else if (q == 6) BLDS(wr, wvoff, pso, 128, junk, 0); }
            if (st == 1) { if (q < 2) ISSX1(6, 0, q & 1); else if (q < 4) ISSX1(7, 1, q & 1); else if (q < 6) ISSW1(0, 3, 2, q & 1); }
            if (st == 2 && q < 6) ISSW1(1, q >> 1, 3 + (q >> 1), q & 1);
            if (st == 3) {
                if (q < 2) ISSW1(1, 3, 6, q & 1);
                if (q == 2) BLDS(wr, wvoff, pso, 256, junk, 0);
                if (q == 3) BLDS(wr, wvoff, pso, (NP - 1) * 128, junk, 0);
                if (q == 4) {
                    const __amdgpu_buffer_rsrc_t sr = __builtin_amdgcn_make_buffer_rsrc((void*)wsp, (short)0, S_ * S_ * 2, 0x00020000);
                    const unsigned so = ((blockIdx.x & 7) * 32 + (blockIdx.x >> 3)) * 32768 + wm * 1024 + wn * 128, so_v = (lane >> 3) * 4096 + (lane & 7) * 16;
                    BLDS(sr, so_v, so, 0, junk, 0);
                }
            }
            if constexpr (st < 4 && (q == 4 || q == 6)) STATS4(s4 * 8 + (q - 4) * 2);
            SB();
        });
    }
    float rs, nm;
    {
        const float ss = *(LAS float*)(uintptr_t)(lds0 + K1_SUM + (wm * 32 + r) * 4), sm = *(LAS float*)(uintptr_t)(lds0 + K1_SUM + 512 + (wm * 32 + r) * 4);
        const float mean = sm * (1.0f / 512.0f);
        const float var = fmaxf(ss * (1.0f / 512.0f) - mean * mean, 0.0f);
        rs = __builtin_amdgcn_rsqf(var + 1e-5f) * oscale;
        nm = -mean * rs;
        if (ORI2) *(LAS float*)(uintptr_t)(priv + h * 128 + r * 4) = h ? nm : rs;
    }
    f32x4 ea[2], eb[2], c4; float cs; unsigned pk[4];
#define EPI_BEGIN0(par) c4 = *(LAS f32x4*)(uintptr_t)(csr + (par))
#define EPI_BEGIN1() do { cs = (c4[0] + c4[1]) + (c4[2] + c4[3]); PIN(cs); if (!ORI2) *(LAS float*)(uintptr_t)(priv + r * 4) = cs; } while (0)
#define EPI_LOAD(q) do { ea[(q) & 1] = *(LAS f32x4*)(uintptr_t)(priv + (q) * 32 + h * 16); if (ORI2) eb[(q) & 1] = *(LAS f32x4*)(uintptr_t)(priv + 128 + (q) * 32 + h * 16); } while (0)
#define EPI_Y(V, e) (ORI2 ? __builtin_fmaf(eb[((e) >> 2) & 1][(e) & 3], cs, (V) * ea[((e) >> 2) & 1][(e) & 3]) : __builtin_fmaf(nm, ea[((e) >> 2) & 1][(e) & 3], (V) * rs))
#define EPI_PAIR(V0, V1, e) do { float y0_ = EPI_Y(V0, e), y1_ = EPI_Y(V1, (e) + 1); if (GELU) { y0_ = gelu_t(y0_); y1_ = gelu_t(y1_); } \
        pk[((e) >> 1) & 3] = pk2(y0_, y1_); PIN(pk[((e) >> 1) & 3]); } while (0)
#define EPI_STORE(j) do { auto s0_ = __builtin_amdgcn_permlane32_swap(pk[0], pk[2], false, false); auto s1_ = __builtin_amdgcn_permlane32_swap(pk[1], pk[3], false, false); \
        u32x4 v_ = {s0_[0], s1_[0], s0_[1], s1_[1]}; GST16(dvoff, v_, dst + (j) * jstride); } while (0)
    EPI_BEGIN0(0);
    unsigned par = 0, rslot = 3, wslot = 7;
#pragma unroll 1
    for (int p = 1; p < np; ++p) {
        const bool more = p + 1 < np;
#pragma unroll
        for (int s = 0; s < 4; ++s) wso[s] = more ? wso[s] + 128 : 0x40000000u;
        unsigned wd[4];
#pragma unroll
        for (int s = 0; s < 4; ++s) { wd[s] = sdst + wslot * 16384; wslot = wslot == 8 ? 0 : wslot + 1; }
        float hi[8];
        SB();
        sfor<32>(LAMBDA(I) {
            constexpr int i = I;
            if (i == 29) {
                par ^= 1024;
                if (more) WAITV(2);
                asm volatile("s_waitcnt lgkmcnt(0)\n\ts_barrier" ::: "memory");
                EPI_BEGIN0(par);
                if (leader) add16(hb, 8);
                if (wid == 0 && !more) hb_sample(hb + (blockIdx.x & 15) * CNT_STRIDE, lds0 + K2_MISC + SY_HB + 4);
            }
            if (((i + 3) & 7) == 0) {
                const unsigned d = rslot == 8 ? (unsigned)(-8 * 16384) : 16384u;
                rslot = rslot == 8 ? 0 : rslot + 1;
#pragma unroll
                for (int t = 0; t < 4; ++t) bad[t] += d;
            }
            bq[(i + 3) & 3] = BFR((i + 3) & 7, 0);
            if constexpr (i >= 4 && i < 12) asm volatile("v_mov_b32 %0, %1" : "=&v"(hi[i - 4]) : "v"(accB[8 + i - 4]));
            if (i == 0) EPI_BEGIN1();
            if (i == 1 || i == 7 || i == 13 || i == 19) EPI_LOAD((i - 1) / 6);
            if constexpr (i >= 5 && i < 28 && (i - 5) % 3 == 0) { constexpr int e = ((i - 5) / 3) * 2; EPI_PAIR(e < 8 ? accB[e & 7] : hi[e & 7], e < 8 ? accB[(e + 1) & 7] : hi[(e + 1) & 7], e); }
            MAIN(i, bq[i & 3]);
            if constexpr (i < 16 && !(i & 1)) BLDS(wr, wvoff, wso[i >> 2], 128 + ((i >> 1) & 1) * 8 * LDW, wd[i >> 2], ((i >> 1) & 1) * 1024);
            if (i == 15 || i == 27) EPI_STORE(i == 27);
            if (i == 27) CSWRITE(par ^ 1024);
            SB();
        });
        dst += pstride;
    }
    EPI_BEGIN1();
    EPI_LOAD(0);
#pragma unroll
    for (int q = 0; q < 4; ++q) {
        if (q < 3) EPI_LOAD(q + 1);
#pragma unroll
        for (int e = 0; e < 4; e += 2) EPI_PAIR(accB[q * 4 + e], accB[q * 4 + e + 1], q * 4 + e);
        if (q & 1) EPI_STORE(q >> 1);
    }
    WAITV(0);
}

DI void phase1(const char* __restrict__ x, const char* __restrict__ wqkv, const char* __restrict__ win, char* __restrict__ ws, const char* __restrict__ wsp, unsigned lds0) {
    const int b = blockIdx.x, xc = b & 7, rr = b >> 3;
    const int rp = (xc & 1) * 8 + (rr & 7), ct = (xc >> 1) * 4 + (rr >> 3);
    const int lane = threadIdx.x & 63, wid = __builtin_amdgcn_readfirstlane(threadIdx.x >> 6);
    const int rt = rp * 4 + (wid & 3), wn = wid >> 2;
    const char* hbw = ws + WS_FLAG + (CNT_HB + (b & 15)) * CNT_STRIDE;
    if (wid == 0) hb_sample(hbw, lds0 + K2_MISC + SY_HB);
    if (b == 0 && wid == 1) {
#pragma unroll
        for (int i = 0; i < 7; ++i) __hip_atomic_store((unsigned*)(ws + WS_FLAG + (size_t)(i * 64 + lane) * CNT_STRIDE), 0u, __ATOMIC_RELAXED, __HIP_MEMORY_SCOPE_AGENT);
    }
    if (threadIdx.x < 256) *(LAS unsigned*)(uintptr_t)(lds0 + K2_ONES + threadIdx.x * 4) = ((threadIdx.x >> 2) & 11) == 0 ? 0x3f803f80u : 0u;
    else if (threadIdx.x < 256 + 32) *(LAS unsigned*)(uintptr_t)(lds0 + K2_MISC + (threadIdx.x - 256) * 4) = 0u;
    if (ct < 4) {
        const int head0 = (ct & 1) * 16 + wn * 2;
        k1_body<false, false>(x, wqkv, ct * 512, 4, rp, ws + (ct < 2 ? WS_Q : WS_K) + ((size_t)head0 * 64 + rt) * 1024, lane * 16, 65536, 4 * 65536,
                              ct < 2 ? 0.25f * 1.4426950408889634f : 1.0f, ws, lds0, wsp);
    } else if (ct < 6) {
        const int head0 = (ct & 1) * 16 + wn * 2;
        k1_body<true, false>(x, wqkv, ct * 512, 4, rp, ws + WS_V + ((size_t)head0 * 64 + rt) * 1024, ((lane >> 4) & 1) * 65536 + (lane >> 5) * 256 + (lane & 15) * 16, 512, 4 * 65536,
                             1.0f, ws, lds0, wsp);
    } else if (ct < 11) {
        const int c0 = (ct - 6) * 192;
        k1_body<false, true>(x, win, c0 * 2, ct == 10 ? 2 : 3, rp, ws + WS_Z1 + ((size_t)rt * 56 + (c0 >> 4) + wn * 2) * 1024, lane * 16, 1024, 4096, 1.0f, ws, lds0, wsp);
    } else {
        const int c0 = (ct - 11) * 192;
        k1_body<true, true>(x, win, (FH + c0) * 2, ct == 15 ? 2 : 3, rp, ws + WS_Z2 + ((size_t)rt * 4 * FH + c0 + wn * 32) * 16, (lane >> 5) * (FH * 16) + (lane & 31) * 16, 2 * FH * 16, 1024,
                            1.0f, ws, lds0, wsp);
    }
    if (wid == 0) {
        const unsigned h0 = __builtin_amdgcn_readfirstlane(*(volatile LAS unsigned*)(uintptr_t)(lds0 + K2_MISC + SY_HB));
#pragma unroll 1
        for (int it = 0; it < SPIN_MAX && __builtin_amdgcn_readfirstlane(*(volatile LAS unsigned*)(uintptr_t)(lds0 + K2_MISC + SY_HB + 4)) == h0; ++it) {
            __builtin_amdgcn_s_sleep(8);
            hb_sample(hbw, lds0 + K2_MISC + SY_HB + 4);
            WAITV(0);
        }
    }
    asm volatile("s_waitcnt lgkmcnt(0)\n\ts_barrier" ::: "memory");
    if (wid == 7) {
        if (ct < 6) add16(ws + WS_FLAG + (size_t)(CNT_QKV + (ct & 1) * 16) * CNT_STRIDE, 8);
        else publish(ws + WS_FLAG + (size_t)((ct < 11 ? CNT_Z1 : CNT_Z2) + rp) * CNT_STRIDE);
    }
}

constexpr float REF_OFF = 96.0f;

DI void lds_inc(unsigned addr) {
    unsigned t0, t1;
    asm volatile("v_mov_b32 %0, %2\n\tv_mov_b32 %1, 1\n\ts_mov_b64 exec, 1\n\tds_add_u32 %0, %1\n\ts_mov_b64 exec, -1" : "=&v"(t0), "=&v"(t1) : "s"(addr) : "memory");
}
template <int SLEEP = 0> DI void lds_wait(unsigned addr, unsigned n) {
    unsigned t0, t1, c, v;
    if (SLEEP == 0)
        asm volatile("v_mov_b32 %0, %4\n\ts_mov_b32 %2, %6\n1:\n\tds_read_b32 %1, %0\n\ts_sub_u32 %2, %2, 1\n\ts_waitcnt lgkmcnt(0)\n\tv_readfirstlane_b32 %3, %1\n\ts_nop 0\n\t"
                     "s_cselect_b32 %3, %5, %3\n\ts_cmp_lt_u32 %3, %5\n\ts_cbranch_scc1 1b"
                     : "=&v"(t0), "=&v"(t1), "=&s"(c), "=&s"(v) : "s"(addr), "s"(n), "i"(LSPIN_MAX) : "scc", "memory");
    else
        asm volatile("v_mov_b32 %0, %4\n\ts_mov_b32 %2, %6\n\ts_branch 2f\n1:\n\ts_sleep %c7\n2:\n\tds_read_b32 %1, %0\n\ts_sub_u32 %2, %2, 1\n\ts_waitcnt lgkmcnt(0)\n\tv_readfirstlane_b32 %3, %1\n\ts_nop 0\n\t"
                     "s_cselect_b32 %3, %5, %3\n\ts_cmp_lt_u32 %3, %5\n\ts_cbranch_scc1 1b"
                     : "=&v"(t0), "=&v"(t1), "=&s"(c), "=&s"(v) : "s"(addr), "s"(n), "i"(LSPIN_MAX), "i"(SLEEP) : "scc", "memory");
}
DI bf16x16 lo8(const f32x16& x) { f32x8 t = __builtin_shufflevector(x, x, 0, 1, 2, 3, 4, 5, 6, 7); return __builtin_bit_cast(bf16x16, t); }
DI bf16x16 hi8(const f32x16& x) { f32x8 t = __builtin_shufflevector(x, x, 8, 9, 10, 11, 12, 13, 14, 15); return __builtin_bit_cast(bf16x16, t); }
#define PRIO_HI() __builtin_amdgcn_s_setprio(2)
#define PRIO_LO() __builtin_amdgcn_s_setprio(0)
#define SMF(a, b, c) __builtin_amdgcn_smfmac_f32_32x32x32_bf16(a, b, c, sidx, 0, 0)
#define GLD16S(dst, voff, sbase) GLD16C(dst, voff, sbase, 0)

DI float max16(const f32x16& s) {
    float a = fmaxf(fmaxf(s[0], s[1]), s[2]), b = fmaxf(fmaxf(s[3], s[4]), s[5]);
    a = fmaxf(fmaxf(a, s[6]), s[7]); b = fmaxf(fmaxf(b, s[8]), s[9]);
    a = fmaxf(fmaxf(a, s[10]), s[11]); b = fmaxf(fmaxf(b, s[12]), s[13]);
    a = fmaxf(fmaxf(a, s[14]), s[15]);
    return fmaxf(a, b);
}
DI float xhalf_max(float m) {
    auto rr = __builtin_amdgcn_permlane32_swap(__float_as_uint(m), __float_as_uint(m), false, false);
    return fmaxf(__uint_as_float(rr[0]), __uint_as_float(rr[1]));
}

#define LD4C(d, p, voff, o0, o1, o2, o3) asm volatile("global_load_dwordx4 %0, %4, %5 offset:" #o0 " sc1\n\tglobal_load_dwordx4 %1, %4, %5 offset:" #o1 " sc1\n\t" \
        "global_load_dwordx4 %2, %4, %5 offset:" #o2 " sc1\n\tglobal_load_dwordx4 %3, %4, %5 offset:" #o3 " sc1\n\ts_waitcnt vmcnt(0)" \
        : "=&v"(d[0]), "=&v"(d[1]), "=&v"(d[2]), "=&v"(d[3]) : "v"(voff), "s"(p) : "memory")
DI void att_exact(const char* kg, const char* vg, bf16x8 q0, bf16x8 q1, f32x16& o0, f32x16& o1) {
    const unsigned lane = threadIdx.x & 63, l16 = lane * 16, vo = (lane >> 5) * 256 + (lane & 15) * 16;
    const int sidx = (int)0xDDDDDDDDu;
    const f32x16 z = splat16(0.f);
    float m0 = -INFINITY, m1 = -INFINITY;
#pragma unroll 1
    for (int t = 0; t < 64; t += 4) {
        bf16x8 kf[4];
        LD4C(kf, kg + t * 1024, l16, 0, 1024, 2048, 3072);
#pragma unroll
        for (int i = 0; i < 4; ++i) { m0 = fmaxf(m0, max16(MFMA32(kf[i], q0, z))); m1 = fmaxf(m1, max16(MFMA32(kf[i], q1, z))); }
    }
    const f32x16 c0 = splat16(-xhalf_max(m0)), c1 = splat16(-xhalf_max(m1));
    o0 = z; o1 = z;
    const u32x4 onesu = {0x3f803f80u, 0x3f803f80u, 0x3f803f80u, 0x3f803f80u};
#pragma unroll 1
    for (int t = 0; t < 64; t += 2) {
        bf16x8 kf[4]; u32x4 vf[4];
        LD4C(vf, vg + t * 1024, vo, 0, 512, 1024, 1536);
        LD4C(kf, kg + t * 1024, l16, 0, 1024, 0, 1024);
#pragma unroll
        for (int i = 0; i < 2; ++i) {
            const bf16x8 va = __builtin_bit_cast(bf16x8, (lane & 16) ? onesu : vf[2 * i]), vb = __builtin_bit_cast(bf16x8, (lane & 16) ? onesu : vf[2 * i + 1]);
            f32x16 s0 = MFMA32(kf[i], q0, c0), s1 = MFMA32(kf[i], q1, c1);
#pragma unroll
            for (int e = 0; e < 16; ++e) { s0[e] = __builtin_amdgcn_exp2f(s0[e]); s1[e] = __builtin_amdgcn_exp2f(s1[e]); }
            o0 = SMF(va, lo8(s0), o0); o0 = SMF(vb, hi8(s0), o0);
            o1 = SMF(va, lo8(s1), o1); o1 = SMF(vb, hi8(s1), o1);
        }
    }
}

DI void att_store(const f32x16& o, char* dst, unsigned l16) {
    const float rl = __builtin_amdgcn_rcpf(o[8]);
    unsigned p0 = pk2(o[0] * rl, o[1] * rl), p1 = pk2(o[2] * rl, o[3] * rl), p2 = pk2(o[4] * rl, o[5] * rl), p3 = pk2(o[6] * rl, o[7] * rl);
    auto s0 = __builtin_amdgcn_permlane32_swap(p0, p2, false, false);
    auto s1 = __builtin_amdgcn_permlane32_swap(p1, p3, false, false);
    u32x4 v = {s0[0], s1[0], s0[1], s1[1]};
    GST16(l16, v, dst);
}
DI float att_mag(const f32x16& o) {
    return ((fabsf(o[0]) + fabsf(o[1])) + (fabsf(o[2]) + fabsf(o[3]))) + ((fabsf(o[4]) + fabsf(o[5])) + (fabsf(o[6]) + fabsf(o[7]))) + o[8];
}

DI void att_role(int head, int qb, int w, char* __restrict__ ws, unsigned lds0) {
    const int lane = threadIdx.x & 63;
    const int qh = w & 1, kh = w >> 1;
    const char* kg = ws + WS_K + (size_t)head * 65536;
    const char* vg = ws + WS_V + (size_t)head * 65536;
    const char* qg = ws + WS_Q + (size_t)head * 65536 + (size_t)(qb * 8 + qh * 4) * 1024;
    const unsigned l16 = lane * 16;
    const unsigned vo = (lane >> 5) * 256 + (lane & 15) * 16;
    u32x4 qr[4], k0, ks;
    const unsigned qo = l16 + kh * 2048, qo2 = l16 + (kh ^ 1) * 2048;
    int sidx = (int)0xDDDDDDDDu; PIN(sidx);
    f32x16 oo[4] = {splat16(0.f), splat16(0.f), splat16(0.f), splat16(0.f)}, ss[4];
    ss[3] = splat16(0.f);
    u32x4 v0 = {0, 0, 0, 0}, v1 = {0, 0, 0, 0};
    asm volatile("" : "+{v[0:15]}"(oo[0]), "+{v[16:31]}"(oo[1]), "+{v[32:47]}"(oo[2]), "+{v[48:63]}"(oo[3]), "=&{v[144:159]}"(ss[1]), "=&{v[160:175]}"(ss[2]), "+{v[176:191]}"(ss[3]),
                      "+{v[212:215]}"(v0), "+{v[216:219]}"(v1));
    unsigned kaddr = lds0 + K2_K + kh * 31744 + l16;
    unsigned vaddr = (lane & 16) ? (lds0 + K2_ONES + vo) : (lds0 + K2_V + kh * 32768 + vo);
    unsigned vinc = (lane & 16) ? 0u : 1024u;
    u32x4 e0 = {0x3f80u, 0u, 0u, 0u};
    const unsigned cb = lds0 + (qh ? K2_V : K2_K) + 20 * 1024 + l16, cst = qh ? 32768 : 31744;
    unsigned cwa = cb + kh * cst, cra = cb + (kh ^ 1) * cst;
    char* dst = ws + WS_CAT + ((size_t)(qb * 8 + qh * 4 + kh * 2) * CAT_KS + head) * 1024;
    char* cnt = ws + WS_FLAG + (size_t)(CNT_REC + (qb * 4 + qh * 2 + kh) * 8 + 4 + (head >> 3)) * CNT_STRIDE;
    PIN(kaddr); PIN(vaddr); PIN(vinc); PIN(e0); PIN(cwa); PIN(cra);
    asm volatile("" : "+s"(dst), "+s"(cnt));
    lds_wait<1>(lds0 + K2_MISC + SY_QKV, 1);
    GLD16C(ks, l16, kg, 0);
    GLD16C(qr[0], qo, qg, 0); GLD16C(qr[1], qo, qg, 1024); GLD16C(qr[2], qo2, qg, 0); GLD16C(qr[3], qo2, qg, 1024);
    GLD16C(k0, l16, kg + kh * 32768, 0);
    {
        const __amdgpu_buffer_rsrc_t kr = __builtin_amdgcn_make_buffer_rsrc((void*)(ws + WS_K), (short)0, 4u << 20, 0x00020000);
        const unsigned so = head * 65536 + (qh ? (unsigned)(WS_V - WS_K) : 1024u) + kh * 32768, d = lds0 + (qh ? K2_V + kh * 32768 : K2_K + kh * 31744);
        BLDSC(kr, l16, so, 0, d, 0); BLDSC(kr, l16, so, 1024, d, 1024); BLDSC(kr, l16, so, 2048, d, 2048); BLDSC(kr, l16, so, 3072, d, 3072); BLDSC(kr, l16, so, 4096, d, 4096);
    }
    asm volatile("s_waitcnt vmcnt(5)" : "+v"(qr[0]), "+v"(qr[1]), "+v"(qr[2]), "+v"(qr[3]), "+v"(k0), "+v"(ks) :: "memory");
#define QF(i) __builtin_bit_cast(bf16x8, qr[i])
    f32x16 cc[4];
#pragma unroll
    for (int i = 0; i < 4; ++i) { cc[i] = MFMA32(__builtin_bit_cast(bf16x8, ks), QF(i), splat16(0.f)); PIN(cc[i]); SB(); }
#pragma unroll
    for (int i = 0; i < 4; ++i) {
        const float m = __builtin_fmaf(max16(cc[i]), -0.5f, -0.5f * REF_OFF);
        u32x4 mb = {__float_as_uint(m) >> 16, 0u, 0u, 0u};
        cc[i] = MFMA32(__builtin_bit_cast(bf16x8, e0), __builtin_bit_cast(bf16x8, mb), splat16(0.f));
        PIN(cc[i]);
    }
    u32x4 kf = k0;
    WAITV(0); lds_inc(lds0 + K2_MISC + SY_KVA);
    asm volatile("v_mfma_f32_32x32x16_bf16 v[128:143], v[208:211], v[192:195], v[64:79]" : "=&{v[128:143]}"(ss[0]) : "{v[208:211]}"(kf), "{v[192:195]}"(qr[0]), "{v[64:79]}"(cc[0]));
    lds_wait(lds0 + K2_MISC + SY_KVA, 4);
#pragma unroll 1
    for (int seg = 0; seg < 5; ++seg) {
        int n = 3;
        if (seg == 1) { n = 7; lds_wait(lds0 + K2_MISC + SY_KVB, 1); }
        else if (seg == 2) { n = 4; lds_wait(lds0 + K2_MISC + SY_KVB, 2); }
        else if (seg >= 3) { n = seg == 3 ? 9 : 4; lds_inc(lds0 + K2_MISC + (seg == 3 ? SY_P16 : SY_P26)); }
        asm volatile("s_nop 7\n\t.p2align 6\n1:\n\t"
            "v_mfma_f32_32x32x16_bf16 v[144:159], v[208:211], v[196:199], v[80:95]\n\tv_exp_f32 v128, v128\n\tv_exp_f32 v129, v129\n\tv_exp_f32 v130, v130\n\t"
            "v_exp_f32 v131, v131\n\tv_smfmac_f32_32x32x32_bf16 v[48:63], v[212:215], v[176:183], v220\n\tds_read_b128 v[212:215], v222\n\tv_exp_f32 v132, v132\n\t"
            "v_exp_f32 v133, v133\n\tv_exp_f32 v134, v134\n\tv_exp_f32 v135, v135\n\tv_smfmac_f32_32x32x32_bf16 v[48:63], v[216:219], v[184:191], v220\n\t"
            "ds_read_b128 v[216:219], v222 offset:512\n\tv_exp_f32 v136, v136\n\tv_exp_f32 v137, v137\n\tv_exp_f32 v138, v138\n\tv_exp_f32 v139, v139\n\t"
            "v_exp_f32 v140, v140\n\tv_exp_f32 v141, v141\n\tv_exp_f32 v142, v142\n\tv_exp_f32 v143, v143\n\t"
            "v_mfma_f32_32x32x16_bf16 v[160:175], v[208:211], v[200:203], v[96:111]\n\tv_exp_f32 v144, v144\n\tv_exp_f32 v145, v145\n\tv_exp_f32 v146, v146\n\t"
            "v_exp_f32 v147, v147\n\ts_waitcnt lgkmcnt(1)\n\tv_smfmac_f32_32x32x32_bf16 v[0:15], v[212:215], v[128:135], v220\n\tv_exp_f32 v148, v148\n\t"
            "v_exp_f32 v149, v149\n\tv_exp_f32 v150, v150\n\tv_exp_f32 v151, v151\n\ts_waitcnt lgkmcnt(0)\n\t"
            "v_smfmac_f32_32x32x32_bf16 v[0:15], v[216:219], v[136:143], v220\n\tv_add_u32 v222, v222, v223\n\tv_exp_f32 v152, v152\n\tv_exp_f32 v153, v153\n\t"
            "v_exp_f32 v154, v154\n\tv_exp_f32 v155, v155\n\tv_exp_f32 v156, v156\n\tv_exp_f32 v157, v157\n\tv_exp_f32 v158, v158\n\tv_exp_f32 v159, v159\n\t"
            "v_mfma_f32_32x32x16_bf16 v[176:191], v[208:211], v[204:207], v[112:127]\n\tds_read_b128 v[208:211], v221\n\tv_exp_f32 v160, v160\n\t"
            "v_exp_f32 v161, v161\n\tv_exp_f32 v162, v162\n\tv_exp_f32 v163, v163\n\tv_smfmac_f32_32x32x32_bf16 v[16:31], v[212:215], v[144:151], v220\n\t"
            "v_exp_f32 v164, v164\n\tv_exp_f32 v165, v165\n\tv_exp_f32 v166, v166\n\tv_exp_f32 v167, v167\n\t"
            "v_smfmac_f32_32x32x32_bf16 v[16:31], v[216:219], v[152:159], v220\n\tv_add_u32 v221, 0x400, v221\n\tv_exp_f32 v168, v168\n\tv_exp_f32 v169, v169\n\t"
            "v_exp_f32 v170, v170\n\tv_exp_f32 v171, v171\n\tv_exp_f32 v172, v172\n\tv_exp_f32 v173, v173\n\tv_exp_f32 v174, v174\n\tv_exp_f32 v175, v175\n\t"
            "s_waitcnt lgkmcnt(0)\n\tv_mfma_f32_32x32x16_bf16 v[128:143], v[208:211], v[192:195], v[64:79]\n\tv_exp_f32 v176, v176\n\tv_exp_f32 v177, v177\n\t"
            "v_exp_f32 v178, v178\n\tv_exp_f32 v179, v179\n\tv_smfmac_f32_32x32x32_bf16 v[32:47], v[212:215], v[160:167], v220\n\tv_exp_f32 v180, v180\n\t"
            "v_exp_f32 v181, v181\n\tv_exp_f32 v182, v182\n\tv_exp_f32 v183, v183\n\tv_smfmac_f32_32x32x32_bf16 v[32:47], v[216:219], v[168:175], v220\n\t"
            "v_exp_f32 v184, v184\n\tv_exp_f32 v185, v185\n\tv_exp_f32 v186, v186\n\tv_exp_f32 v187, v187\n\tv_exp_f32 v188, v188\n\tv_exp_f32 v189, v189\n\t"
            "s_sub_u32 %0, %0, 1\n\tv_exp_f32 v190, v190\n\tv_exp_f32 v191, v191\n\t"
            "s_cbranch_scc0 1b"
            : "+s"(n), "+{v[0:15]}"(oo[0]), "+{v[16:31]}"(oo[1]), "+{v[32:47]}"(oo[2]), "+{v[48:63]}"(oo[3]), "+{v[128:143]}"(ss[0]), "+{v[144:159]}"(ss[1]), "+{v[160:175]}"(ss[2]), "+{v[176:191]}"(ss[3]), "+{v[208:211]}"(kf), "+{v[212:215]}"(v0), "+{v[216:219]}"(v1), "+{v221}"(kaddr), "+{v222}"(vaddr)
            : "{v[64:79]}"(cc[0]), "{v[80:95]}"(cc[1]), "{v[96:111]}"(cc[2]), "{v[112:127]}"(cc[3]), "{v[192:195]}"(qr[0]), "{v[196:199]}"(qr[1]), "{v[200:203]}"(qr[2]), "{v[204:207]}"(qr[3]), "{v220}"(sidx), "{v223}"(vinc)
            : "scc", "memory");
    }
    asm volatile("v_smfmac_f32_32x32x32_bf16 v[48:63], v[212:215], v[176:183], v220\n\ts_nop 0\n\tv_smfmac_f32_32x32x32_bf16 v[48:63], v[216:219], v[184:191], v220\n\ts_nop 7\n\ts_nop 7"
            : "+{v[48:63]}"(oo[3]) : "{v[176:191]}"(ss[3]), "{v[212:215]}"(v0), "{v[216:219]}"(v1), "{v220}"(sidx));
    __builtin_amdgcn_s_setprio(3);
    lds_wait(lds0 + K2_MISC + SY_P26, 4);
#pragma unroll
    for (int i = 0; i < 2; ++i) {
        f32x4 a = {oo[2 + i][0], oo[2 + i][1], oo[2 + i][2], oo[2 + i][3]}, b = {oo[2 + i][4], oo[2 + i][5], oo[2 + i][6], oo[2 + i][7]};
        *(LAS f32x4*)(uintptr_t)(cwa + i * 2048) = a;
        *(LAS f32x4*)(uintptr_t)(cwa + i * 2048 + 1024) = b;
    }
    { f32x2 l2 = {oo[2][8], oo[3][8]}; *(LAS f32x2*)(uintptr_t)(cwa + 4096) = l2; }
    asm volatile("s_waitcnt lgkmcnt(0)" ::: "memory");
    lds_inc(lds0 + K2_MISC + SY_CMB + qh * 4);
    lds_wait(lds0 + K2_MISC + SY_CMB + qh * 4, 2);
    {
        const f32x2 l2 = *(LAS f32x2*)(uintptr_t)(cra + 4096);
#pragma unroll
        for (int i = 0; i < 2; ++i) {
            const f32x4 a = *(LAS f32x4*)(uintptr_t)(cra + i * 2048), b = *(LAS f32x4*)(uintptr_t)(cra + i * 2048 + 1024);
#pragma unroll
            for (int e = 0; e < 4; ++e) { oo[i][e] += a[e]; oo[i][4 + e] += b[e]; }
            oo[i][8] += l2[i];
            PIN(oo[i]);
        }
    }
    const bool ok = (att_mag(oo[0]) + att_mag(oo[1]) < 0x1p120f) & (fminf(oo[0][8], oo[1][8]) > 0x1p-100f);
    if (__builtin_expect(__builtin_amdgcn_ballot_w64(!ok) != 0, 0)) att_exact(kg, vg, QF(0), QF(1), oo[0], oo[1]);
    att_store(oo[0], dst, l16);
    att_store(oo[1], dst + CAT_KS * 1024, l16);
    publish(cnt);
}

template <int CTRL> DI float dppf(float x) { return __builtin_bit_cast(float, __builtin_amdgcn_mov_dpp(__builtin_bit_cast(int, x), CTRL, 0xf, 0xf, true)); }
DI float fold32(float a, float b) {
    auto r = __builtin_amdgcn_permlane32_swap(__float_as_uint(a), __float_as_uint(b), false, false);
    return __uint_as_float(r[0]) + __uint_as_float(r[1]);
}
DI float fold16(float a, float b) {
    auto r = __builtin_amdgcn_permlane16_swap(__float_as_uint(a), __float_as_uint(b), false, false);
    return __uint_as_float(r[0]) + __uint_as_float(r[1]);
}
DI float rowsum16(float v) { v += dppf<0xB1>(v); v += dppf<0x4E>(v); v += dppf<0x124>(v); v += dppf<0x128>(v); return v; }

DI void spg_role(int b, int head, int w, const char* __restrict__ wsp, const __hip_bfloat16* __restrict__ bsp, char* __restrict__ ws, unsigned lds0,
                 const char* __restrict__ x, const char* __restrict__ wo, const char* __restrict__ wout, char* __restrict__ out) {
    const int lane = threadIdx.x & 63;
    const int xc = b & 7, rr = b >> 3;
    const int tt = xc * 4 + (rr & 3), ft = rr >> 2;
    const bool active = ft < 7;
    const unsigned wring = lds0 + K2_W + w * 8192;
    const __amdgpu_buffer_rsrc_t wr = __builtin_amdgcn_make_buffer_rsrc((void*)wsp, (short)0, S_ * S_ * 2, 0x00020000);
    unsigned wso = tt * 64 * 4096 + w * 1024;
    unsigned wvoff[2];
#pragma unroll
    for (int p = 0; p < 2; ++p) wvoff[p] = (lane >> 3) * 4096 + (((lane & 7) ^ ((4 * p + (lane >> 4)) & 7)) << 4);
#define ISSUEW(i) BLDS(wr, wvoff[(i) & 1], wso, (i) * 32768, wring, (i) * 1024)
    auto poll = [&](unsigned idx, unsigned cnt, int n) {
        const unsigned fo = idx * CNT_STRIDE;
        char* fl = ws + WS_FLAG;
#pragma unroll 1
        for (int it = 0; it < SPIN_MAX; ++it) {
            unsigned f = cnt;
            if (lane < n) { const unsigned z = 0; asm volatile("global_atomic_add %0, %1, %2, %3 sc0\n\ts_waitcnt vmcnt(0)" : "=&v"(f) : "v"(fo), "v"(z), "s"(fl) : "memory"); }
            if (__all(f == cnt)) break;
            if ((it & 63) == 63) add16(fl + CNT_HB * CNT_STRIDE, 8);
            __builtin_amdgcn_s_sleep(2);
        }
    };
    f32x16 acc[4][2];
    unsigned zvoff[4], waddr[4];
#define SETUP() do { _Pragma("unroll") for (int j = 0; j < 4; ++j) { \
            acc[j][0] = splat16(0.f); acc[j][1] = splat16(0.f); PIN(acc[j][0]); PIN(acc[j][1]); \
            zvoff[j] = (lane >> 5) * (FH * 16) + (j ^ w) * 512 + (lane & 31) * 16; PIN(zvoff[j]); \
            waddr[j] = wring + (lane & 31) * 128 + (((j * 2 + (lane >> 5)) ^ ((lane >> 1) & 7)) << 4); PIN(waddr[j]); } \
        PIN(wvoff[0]); PIN(wvoff[1]); } while (0)
    ISSUEW(0); ISSUEW(1); ISSUEW(2); ISSUEW(3); ISSUEW(4); ISSUEW(5); ISSUEW(6); ISSUEW(7);
    if (w == 3) {
        const __amdgpu_buffer_rsrc_t kr = __builtin_amdgcn_make_buffer_rsrc((void*)(ws + WS_K), (short)0, 4u << 20, 0x00020000);
        const unsigned lo = lane * 16;
        unsigned kso = head * 65536 + 5 * 1024, kd = lds0 + 5 * 1024;
        poll(CNT_QKV + (head >> 4) * 16 + ((b >> 3) & 15), 48, 1);
        lds_inc(lds0 + K2_MISC + SY_QKV);
        SETUP();
#define KV4() do { BLDSC(kr, lo, kso, 0, kd, K2_K - 1024); BLDSC(kr, lo, kso, WS_V - WS_K, kd, K2_V); BLDSC(kr, lo, kso, 32768, kd, K2_K + 31744 - 1024); \
                   BLDSC(kr, lo, kso, WS_V - WS_K + 32768, kd, K2_V + 32768); kso += 1024; kd += 1024; } while (0)
#pragma unroll 1
        for (int t = 5; t < 20; ++t) KV4();
        WAITV(28);
        PRIO_HI(); lds_inc(lds0 + K2_MISC + SY_KVB); PRIO_LO();
#pragma unroll 1
        for (int t = 20; t < 32; ++t) { KV4(); WAITV(44); }
        PRIO_HI();
    } else {
        const char* src = ws + WS_Z2 + (size_t)b * FH * 16;
        u32x4 zc[5], z0;
        const unsigned o0 = w * 1024 + lane * 16, o4 = o0 + (w < 2 ? 12288 : 9216);
        const unsigned zero = 0;
        if (w == 0) { poll(CNT_Z2 + (b >> 4), 5, 1); lds_inc(lds0 + K2_MISC + SY_Z2); } else lds_wait<1>(lds0 + K2_MISC + SY_Z2, 1);
        GLD16C(zc[0], o0, src, 0); GLD16C(zc[1], o0, src, 3072); GLD16C(zc[2], o0, src + 6144, 0); GLD16C(zc[3], o0, src + 6144, 3072); GLD16C(zc[4], o4, src, 0); GLD16C(z0, zero, src, 0);
        SETUP();
        asm volatile("s_waitcnt vmcnt(0)" : "+v"(zc[0]), "+v"(zc[1]), "+v"(zc[2]), "+v"(zc[3]), "+v"(zc[4]), "+v"(z0) :: "memory");
        PRIO_HI();
        char* dst = ws + WS_Z2N + (size_t)b * FH * 16;
        float sh[8], s1[8], s2[8];
#pragma unroll
        for (int e = 0; e < 4; ++e) { sh[2 * e] = bflo(z0[e]); sh[2 * e + 1] = bfhi(z0[e]); }
        const float m4 = w < 2 ? 1.f : 0.f;
#pragma unroll
        for (int e = 0; e < 8; ++e) {
            float v[5];
#pragma unroll
            for (int j = 0; j < 5; ++j) v[j] = ((e & 1) ? bfhi(zc[j][e >> 1]) : bflo(zc[j][e >> 1])) - sh[e];
            v[4] *= m4;
            s1[e] = ((v[0] + v[1]) + (v[2] + v[3])) + v[4];
            s2[e] = __builtin_fmaf(v[4], v[4], (v[0] * v[0] + v[1] * v[1]) + (v[2] * v[2] + v[3] * v[3]));
        }
        f32x4 q;
#pragma unroll
        for (int i = 0; i < 4; ++i) q[i] = rowsum16(fold16(fold32(s1[i], s2[i]), fold32(s1[i + 4], s2[i + 4])));
        LAS float* part = (LAS float*)(uintptr_t)(lds0 + K2_MISC + 256);
        *(LAS f32x4*)(part + w * 16 + (lane >> 4) * 4) = q;
        asm volatile("s_waitcnt lgkmcnt(0)" ::: "memory");
        lds_inc(lds0 + K2_MISC + SY_LN);
        lds_wait(lds0 + K2_MISC + SY_LN, 3);
        float rs[8], nm[8];
        {
            f32x4 t[4];
#pragma unroll
            for (int i = 0; i < 4; ++i) t[i] = (*(LAS f32x4*)(part + i * 4) + *(LAS f32x4*)(part + 16 + i * 4)) + *(LAS f32x4*)(part + 32 + i * 4);
#pragma unroll
            for (int e = 0; e < 8; ++e) {
                float mean = t[e >> 2][e & 3] * (1.0f / FH);
                float var = fmaxf(t[2 + (e >> 2)][e & 3] * (1.0f / FH) - mean * mean, 0.f);
                rs[e] = __builtin_amdgcn_rsqf(var + 1e-5f);
                nm[e] = -(mean + sh[e]) * rs[e];
            }
        }
#pragma unroll
        for (int j = 0; j < 5; ++j) {
            u32x4 v;
#pragma unroll
            for (int e = 0; e < 4; ++e) v[e] = pk2(__builtin_fmaf(bflo(zc[j][e]), rs[2 * e], nm[2 * e]), __builtin_fmaf(bfhi(zc[j][e]), rs[2 * e + 1], nm[2 * e + 1]));
            if (j < 4) GST16(o0, v, dst + j * 3072); else GST16(o4, v, dst);
        }
        WAITV(0);
        unsigned old = 0;
        if (lane == 0) old = __hip_atomic_fetch_add((LAS unsigned*)(uintptr_t)(lds0 + K2_MISC + SY_LNST), 1u, __ATOMIC_RELAXED, __HIP_MEMORY_SCOPE_WORKGROUP);
        old = __builtin_amdgcn_readfirstlane(old);
        if (old == 2) {
            add16(ws + WS_FLAG + (size_t)(b >> 5) * CNT_STRIDE, 11);
            if (!active) { WAITV(0); lds_inc(lds0 + K2_MISC + SY_FLAG); }
        }
    }
    const unsigned l16 = lane * 16;
    auto reg = [&](int ww) { return lds0 + ((ww & 1) ? K2_V + (ww >> 1) * 32768 : K2_K + (ww >> 1) * 31744); };
    if (active) {
        const char* zb = ws + WS_Z2N + ((size_t)w * 64 * FH + ft * 128) * 16;
        u32x4 zf[4][4];
#define ZPTR(c, ks) (zb + (size_t)((c) * 4 + (ks)) * (2 * FH * 16))
        if (w == 0) { poll(lane < 8 ? ((b >> 3) & 15) * 8 + lane : CNT_Z1 + (tt >> 1), lane < 8 ? 32 : 5, 9); lds_inc(lds0 + K2_MISC + SY_FLAG); }
        else lds_wait<2>(lds0 + K2_MISC + SY_FLAG, 1);
        PRIO_LO();
#pragma unroll
        for (int ks = 0; ks < 4; ++ks)
#pragma unroll
            for (int j = 0; j < 4; ++j) GLD16S(zf[ks][j], zvoff[j], ZPTR(0, ks));
        if (w == 3) { WAITV(16); PRIO_HI(); lds_inc(lds0 + K2_MISC + SY_KVB); PRIO_LO(); }
#define ZWAIT(n, ks) asm volatile("s_waitcnt vmcnt(" #n ")" : "+v"(zf[ks][0]), "+v"(zf[ks][1]), "+v"(zf[ks][2]), "+v"(zf[ks][3]) :: "memory")
#define KPAIR(ks, j_, MORE) do { \
        acc[j_][0] = MFMA32(__builtin_bit_cast(bf16x8, zf[ks][j_]), wf[0][ks], acc[j_][0]); PIN(acc[j_][0]); SB(); \
        if (MORE && ks == 0) ISSUEW(2 * j_); \
        acc[j_][1] = MFMA32(__builtin_bit_cast(bf16x8, zf[ks][j_]), wf[1][ks], acc[j_][1]); PIN(acc[j_][1]); SB(); \
        if (MORE && ks == 0) ISSUEW(2 * j_ + 1); \
        if (MORE) GLD16S(zf[ks][j_], zvoff[j_], ZPTR(c + 1, ks)); } while (0)
#define KSTEP(ks, MORE) do { KPAIR(ks, 0, MORE); KPAIR(ks, 1, MORE); KPAIR(ks, 2, MORE); KPAIR(ks, 3, MORE); } while (0)
#define WREAD() _Pragma("unroll") for (int ks = 0; ks < 4; ++ks) { wf[0][ks] = ldsfrag(waddr[ks]); wf[1][ks] = ldsfrag(waddr[ks] + 4096); } \
        asm volatile("s_waitcnt lgkmcnt(0)" : "+v"(wf[0][0]), "+v"(wf[0][1]), "+v"(wf[0][2]), "+v"(wf[0][3]), "+v"(wf[1][0]), "+v"(wf[1][1]), "+v"(wf[1][2]), "+v"(wf[1][3]) :: "memory")
#pragma unroll 1
        for (int c = 0; c < 7; ++c) {
            bf16x8 wf[2][4];
            wso += 128;
            ZWAIT(12, 0);
            WREAD();
            KSTEP(0, true);
            ZWAIT(20, 1); KSTEP(1, true);
            ZWAIT(20, 2); KSTEP(2, true);
            ZWAIT(20, 3); KSTEP(3, true);
        }
        const int fs0 = ft * 8 + w * 2;
        const char* z1p = ws + WS_Z1 + ((size_t)tt * 2 * 56 + fs0) * 1024;
        const char* bp = (const char*)bsp + tt * 128;
        const unsigned bo = (lane & 31) * 2;
        unsigned braw[2];
        auto blk = [&](int dstw, int j) { return (j < 3 ? reg(dstw) + (j - 1) * 8192 : lds0 + K2_W + dstw * 8192) + l16; };
        {
            const int c = 7; (void)c;
            bf16x8 wf[2][4];
            ZWAIT(12, 0);
            WREAD();
            PRIO_HI();
            lds_inc(lds0 + K2_MISC + SY_MAIN);
            lds_wait(lds0 + K2_MISC + SY_P16, 4);
            unsigned wa[3] = {blk(1 ^ w, 1), blk(2 ^ w, 2), blk(3 ^ w, 3)};
            PIN(wa[0]); PIN(wa[1]); PIN(wa[2]); SB();
            PRIO_LO();
            KSTEP(0, false);
            GLD16C(zf[0][0], l16, z1p, 0); GLD16C(zf[0][1], l16, z1p, 1024); GLD16C(zf[0][2], l16, z1p + 56 * 1024, 0); GLD16C(zf[0][3], l16, z1p + 56 * 1024, 1024);
            asm volatile("global_load_ushort %0, %1, %2" : "=v"(braw[0]) : "v"(bo), "s"(bp) : "memory");
            asm volatile("global_load_ushort %0, %1, %2 offset:64" : "=v"(braw[1]) : "v"(bo), "s"(bp) : "memory");
            ZWAIT(14, 1); KSTEP(1, false);
            ZWAIT(10, 2); ZWAIT(6, 3);
            sfor<16>(LAMBDA(N) {
                constexpr int n = N, j = ((n >> 2) + 1) & 3, ks = 2 + ((n >> 1) & 1), t = n & 1, jw = n >> 2;
                if (n == 12) { PRIO_HI(); lds_wait(lds0 + K2_MISC + SY_MAIN, 4); PRIO_LO(); }
                acc[j][t] = MFMA32(__builtin_bit_cast(bf16x8, zf[ks][j]), wf[t][ks], acc[j][t]); PIN(acc[j][t]); SB();
                if constexpr (jw > 0) {
#pragma unroll
                    for (int u = 0; u < 2; ++u) {
                        constexpr int tw = (n >> 1) & 1; const int q = (n & 1) * 2 + u;
                        f32x4 v = {acc[jw][tw][4 * q], acc[jw][tw][4 * q + 1], acc[jw][tw][4 * q + 2], acc[jw][tw][4 * q + 3]};
                        *(LAS f32x4*)(uintptr_t)(wa[jw - 1] + (tw * 4 + q) * 1024) = v;
                    }
                    SB();
                }
            });
        }
        asm volatile("s_waitcnt lgkmcnt(0)" ::: "memory");
        PRIO_HI();
        lds_inc(lds0 + K2_MISC + SY_RED);
        lds_wait(lds0 + K2_MISC + SY_RED, 4);
#pragma unroll
        for (int j = 1; j < 4; ++j) {
            const unsigned a = blk(w, j);
#pragma unroll
            for (int t = 0; t < 2; ++t)
#pragma unroll
                for (int q = 0; q < 4; ++q) {
                    f32x4 v = *(LAS f32x4*)(uintptr_t)(a + (t * 4 + q) * 1024);
#pragma unroll
                    for (int e = 0; e < 4; ++e) acc[0][t][4 * q + e] += v[e];
                }
        }
        asm volatile("s_waitcnt vmcnt(0)" : "+v"(zf[0][0]), "+v"(zf[0][1]), "+v"(zf[0][2]), "+v"(zf[0][3]), "+v"(braw[0]), "+v"(braw[1]) :: "memory");
        const float bias[2] = {bflo(braw[0]), bflo(braw[1])};
#pragma unroll
        for (int t = 0; t < 2; ++t)
#pragma unroll
            for (int s = 0; s < 2; ++s) {
                auto a0 = __builtin_amdgcn_permlane32_swap(zf[0][2 * t + s][0], zf[0][2 * t + s][2], false, false);
                auto a1 = __builtin_amdgcn_permlane32_swap(zf[0][2 * t + s][1], zf[0][2 * t + s][3], false, false);
                const unsigned zc[4] = {a0[0], a1[0], a0[1], a1[1]};
                unsigned p[4];
#pragma unroll
                for (int e = 0; e < 4; ++e)
                    p[e] = pk2(bflo(zc[e]) * (acc[0][t][8 * s + 2 * e] + bias[t]), bfhi(zc[e]) * (acc[0][t][8 * s + 2 * e + 1] + bias[t]));
                auto s0 = __builtin_amdgcn_permlane32_swap(p[0], p[2], false, false);
                auto s1 = __builtin_amdgcn_permlane32_swap(p[1], p[3], false, false);
                u32x4 v = {s0[0], s1[0], s0[1], s1[1]};
                GST16(l16, v, ws + WS_CAT + ((size_t)(tt * 2 + t) * CAT_KS + 32 + fs0 + s) * 1024);
            }
        publish(ws + WS_FLAG + (size_t)(CNT_REC + tt * 8 + (ft * 4 + w) / 7) * CNT_STRIDE);
    } else {
        const int id = (rr & 3) * 4 + w;
        if (w == 3) { WAITV(0); lds_inc(lds0 + K2_MISC + SY_KVB); }
        lds_wait<4>(lds0 + K2_MISC + SY_KVB, 2);
        lds_wait<4>(lds0 + K2_MISC + SY_FLAG, 1);
        PRIO_LO();
#pragma unroll 1
        for (int c = 1; c < 8; ++c) { wso += 128; ISSUEW(0); ISSUEW(1); ISSUEW(2); ISSUEW(3); ISSUEW(4); ISSUEW(5); ISSUEW(6); ISSUEW(7); WAITV(32); }
        lds_wait<32>(lds0 + K2_MISC + SY_P16, 4);
#pragma unroll 1
        for (int i = 0; i < 56; ++i) { glds16(wout + (size_t)(id * 56 + i) * 1024, l16, wring); if ((i & 7) == 7) WAITV(32); }
#pragma unroll 1
        for (int i = 0; i < 32; ++i) { glds16(wo + (size_t)(id * 32 + i) * 1024, l16, wring); if ((i & 7) == 7) WAITV(32); }
#pragma unroll 1
        for (int i = 0; i < 16; ++i) glds16(x + (size_t)(xc * 256 + id * 16 + i) * 1024, l16, wring);
        WAITV(0);
        PRIO_HI();
    }
    {
        const __amdgpu_buffer_rsrc_t hr = __builtin_amdgcn_make_buffer_rsrc((void*)wout, (short)0, FH * 1024, 0x00020000);
        const __amdgpu_buffer_rsrc_t orr = __builtin_amdgcn_make_buffer_rsrc((void*)wo, (short)0, D_ * 1024, 0x00020000);
        unsigned bvoff = ((lane >> 2) & 7) * 1024 + (lane >> 5) * 64 + (lane & 3) * 16;
        const unsigned hso = ft * 128 + w * 14 * 16384, oso = ft * 128 + w * 8 * 16384;
        const unsigned rg = reg(w);
#define FB(rs, so, s, base, off) do { BLDS(rs, bvoff, so, (s) * 16384, base, off); BLDS(rs, bvoff, so, (s) * 16384 + 8192, base, (off) + 1024); } while (0)
        const unsigned bl = (lane >> 5) * 1024 + (w & 1) * 512 + ((lane & 15) >> 2) * 64 + ((lane >> 4) & 1) * 32 + (lane & 3) * 8;
        unsigned br0 = rg + bl, br1 = rg + (bl ^ 512), bw0 = wring + bl, bw1 = wring + (bl ^ 512);
        unsigned xo = (lane & 31) * 1024 + (lane >> 5) * 16;
        PIN(bvoff); PIN(br0); PIN(br1); PIN(bw0); PIN(bw1); PIN(xo); SB();
        PRIO_LO();
        FB(hr, hso, 0, rg, 0); FB(hr, hso, 1, rg, 2048); FB(hr, hso, 2, rg, 4096); FB(hr, hso, 3, rg, 6144);
        FB(hr, hso, 4, rg, 8192); FB(hr, hso, 5, rg, 10240); FB(hr, hso, 6, rg, 12288); FB(hr, hso, 7, rg, 14336);
        FB(hr, hso, 8, wring, 0); FB(hr, hso, 9, wring, 2048); FB(hr, hso, 10, wring, 4096); FB(hr, hso, 11, wring, 6144);
        const size_t oo_ = (size_t)(tt * 64 + (w >> 1) * 32) * 1024 + (ft * 64 + (w & 1) * 32) * 2;
        u32x4 xv[2];
        GLD16(xv[0], xo, x + oo_, 0); GLD16(xv[1], xo, x + oo_, 32);
        PRIO_HI();
        lds_wait<8>(lds0 + K2_MISC + SY_P26, 4);
        FB(hr, hso, 12, rg, 16384); FB(hr, hso, 13, rg, 18432);
        const char* a0p = ws + WS_CAT + (size_t)(tt * 2 + (w >> 1)) * CAT_KS * 1024;
        const char* a1p = ws + WS_CAT + (size_t)(tt * 2 + ((w >> 1) ^ 1)) * CAT_KS * 1024;
        u32x4 af[14][2];
        f32x16 fc[2][2];
        bf16x8 bq[2][2];
#define SLOT0(s) ((s) < 8 ? br0 + (s) * 2048 : (s) < 12 ? bw0 + ((s) - 8) * 2048 : br0 + 16384 + ((s) - 12) * 2048)
#define SLOT1(s) ((s) < 8 ? br1 + (s) * 2048 : (s) < 12 ? bw1 + ((s) - 8) * 2048 : br1 + 16384 + ((s) - 12) * 2048)
#define BQ(s) do { bq[(s) & 1][0] = trfrag(SLOT0(s), SLOT0(s) + 256); bq[(s) & 1][1] = trfrag(SLOT1(s), SLOT1(s) + 256); } while (0)
#define AWAIT(n, s) asm volatile("s_waitcnt vmcnt(%c2)" : "+v"(af[s][0]), "+v"(af[s][1]) : "i"(n) : "memory")
#define FMM(s, FIRST) do { _Pragma("unroll") for (int i_ = 0; i_ < 2; ++i_) _Pragma("unroll") for (int j_ = 0; j_ < 2; ++j_) { \
            fc[i_][j_] = MFMA32(bq[(s) & 1][j_], __builtin_bit_cast(bf16x8, af[s][i_]), (FIRST) ? splat16(0.f) : fc[i_][j_]); PIN(fc[i_][j_]); } SB(); } while (0)
        poll(CNT_REC + tt * 8 + w, 7, 1);
        PRIO_LO();
#pragma unroll
        for (int s = 0; s < 14; ++s) { GLD16S(af[s][0], l16, a0p + (32 + w * 14 + s) * 1024); GLD16S(af[s][1], l16, a1p + (32 + w * 14 + s) * 1024); }
        BQ(0);
#define HST(s) do { if (s < 13) BQ(s + 1); AWAIT(2 * (13 - s) + (s > 11 ? 16 : 0), s); FMM(s, s == 0); } while (0)
        HST(0); HST(1); HST(2); HST(3); HST(4); HST(5); HST(6); HST(7); HST(8);
        HST(9); HST(10); HST(11);
        FB(orr, oso, 0, rg, 0); FB(orr, oso, 1, rg, 2048); FB(orr, oso, 2, rg, 4096); FB(orr, oso, 3, rg, 6144);
        FB(orr, oso, 4, rg, 8192); FB(orr, oso, 5, rg, 10240); FB(orr, oso, 6, rg, 12288); FB(orr, oso, 7, rg, 14336);
        HST(12); HST(13);
        PRIO_HI();
        poll(CNT_REC + tt * 8 + 4 + w, 8, 1);
#pragma unroll
        for (int s = 0; s < 8; ++s) { GLD16S(af[s][0], l16, a0p + (w * 8 + s) * 1024); GLD16S(af[s][1], l16, a1p + (w * 8 + s) * 1024); }
        BQ(0);
#define OST(s) do { if (s < 7) BQ(s + 1); AWAIT(2 * (7 - s), s); FMM(s, false); } while (0)
        OST(0); OST(1); OST(2); OST(3); OST(4); OST(5); OST(6); OST(7);
#pragma unroll
        for (int j = 1; j < 4; ++j)
#pragma unroll
            for (int q = 0; q < 4; ++q) {
                const f32x16& t = fc[j >> 1][j & 1];
                f32x4 v = {t[4 * q], t[4 * q + 1], t[4 * q + 2], t[4 * q + 3]};
                *(LAS f32x4*)(uintptr_t)(rg + (j * 4 + q) * 1024 + l16) = v;
            }
        asm volatile("s_waitcnt lgkmcnt(0)" ::: "memory");
        lds_inc(lds0 + K2_MISC + SY_FIN);
        lds_wait(lds0 + K2_MISC + SY_FIN, 4);
#pragma unroll
        for (int j = 1; j < 4; ++j)
#pragma unroll
            for (int q = 0; q < 4; ++q) {
                f32x4 v = *(LAS f32x4*)(uintptr_t)(reg(w ^ j) + (j * 4 + q) * 1024 + l16);
#pragma unroll
                for (int e = 0; e < 4; ++e) fc[0][0][4 * q + e] += v[e];
            }
        asm volatile("" : "+v"(xv[0]), "+v"(xv[1]));
#pragma unroll
        for (int s = 0; s < 2; ++s) {
            auto a0 = __builtin_amdgcn_permlane32_swap(xv[s][0], xv[s][2], false, false);
            auto a1 = __builtin_amdgcn_permlane32_swap(xv[s][1], xv[s][3], false, false);
            const unsigned xc4[4] = {a0[0], a1[0], a0[1], a1[1]};
            unsigned p[4];
#pragma unroll
            for (int e = 0; e < 4; ++e) p[e] = pk2(bflo(xc4[e]) + fc[0][0][8 * s + 2 * e], bfhi(xc4[e]) + fc[0][0][8 * s + 2 * e + 1]);
            auto s0 = __builtin_amdgcn_permlane32_swap(p[0], p[2], false, false);
            auto s1 = __builtin_amdgcn_permlane32_swap(p[1], p[3], false, false);
            u32x4 v = {s0[0], s1[0], s0[1], s1[1]};
            *(u32x4*)(out + oo_ + xo + s * 32) = v;
        }
    }
}

__global__ __launch_bounds__(512, 2) NOPK void k_block(const char* __restrict__ x, const char* __restrict__ wqkv, const char* __restrict__ wo, const char* __restrict__ win,
                                                       const char* __restrict__ wsp, const __hip_bfloat16* __restrict__ bsp, const char* __restrict__ wout,
                                                       char* __restrict__ ws, char* __restrict__ out) {
    extern __shared__ __attribute__((aligned(16))) char smem[];
    const unsigned lds0 = (unsigned)(uintptr_t)(LAS char*)smem;
    phase1(x, wqkv, win, ws, wsp, lds0);
    const int wid = __builtin_amdgcn_readfirstlane(threadIdx.x >> 6);
    const int b = blockIdx.x;
    const int head = (b & 7) * 4 + (b >> 6);
    if (wid < 4) att_role(head, (b >> 3) & 7, wid, ws, lds0);
    else spg_role(b, head, wid - 4, wsp, bsp, ws, lds0, x, wo, wout, out);
}

extern "C" void kernel_launch(void* const* d_in, const int* in_sizes, int n_in, void* d_out, int out_size, void* d_ws, size_t ws_size,
                              hipStream_t stream) {
    (void)hipFuncSetAttribute((const void*)k_block, hipFuncAttributeMaxDynamicSharedMemorySize, K2_LDS);
    hipLaunchKernelGGL(k_block, dim3(256), dim3(512), K2_LDS, stream, (const char*)d_in[0], (const char*)d_in[1], (const char*)d_in[2], (const char*)d_in[4],
                       (const char*)d_in[5], (const __hip_bfloat16*)d_in[6], (const char*)d_in[7], (char*)d_ws, (char*)d_out);
}
```

```cpp
#include <hip/hip_runtime.h>
#include <hip/hip_bf16.h>
#include <stdint.h>
#include <utility>

typedef __attribute__((ext_vector_type(2))) __bf16 bf16x2;
typedef __attribute__((ext_vector_type(8))) __bf16 bf16x8;
typedef __attribute__((ext_vector_type(16))) __bf16 bf16x16;
typedef __attribute__((ext_vector_type(2))) float f32x2;
typedef __attribute__((ext_vector_type(4))) float f32x4;
typedef __attribute__((ext_vector_type(8))) float f32x8;
typedef __attribute__((ext_vector_type(16))) float f32x16;
typedef __attribute__((ext_vector_type(4))) unsigned u32x4;
typedef __attribute__((ext_vector_type(4))) short s16x4;
#define LAS __attribute__((address_space(3)))
#define DI __device__ __forceinline__
#if defined(__HIP_DEVICE_COMPILE__)
#define NOPK __attribute__((target("no-packed-fp32-ops")))
#else
#define NOPK
#endif
#define SB() __builtin_amdgcn_sched_barrier(0)
#define PIN(x) asm volatile("" : "+v"(x))
#define WAITV(n) asm volatile("s_waitcnt vmcnt(" #n ")" ::: "memory")
#define MFMA16(a, b, c) __builtin_amdgcn_mfma_f32_16x16x32_bf16(a, b, c, 0, 0, 0)
#define MFMA32(a, b, c) __builtin_amdgcn_mfma_f32_32x32x16_bf16(a, b, c, 0, 0, 0)

constexpr int S_ = 2048, D_ = 512, FH = 896;
constexpr size_t WS_Q = 0, WS_K = 2u << 20, WS_V = 4u << 20, WS_Z1 = 6u << 20, WS_Z2 = 10u << 20, WS_Z2N = 14u << 20,
                 WS_CAT = 18u << 20, WS_FLAG = 24u << 20;
constexpr int CNT_STRIDE = 256, CNT_REC = 256, CNT_QKV = 512, CNT_Z2 = 544, CNT_Z1 = 560, CNT_HB = 576;
__device__ __forceinline__ int id1_of(int b) { return ((b & 1) * 8 + ((b >> 2) & 1) * 4 + (b >> 6)) * 16 + ((b >> 1) & 1) * 8 + ((b >> 3) & 7); }
__device__ __forceinline__ int grp_of(int id1) { return (id1 & 3) * 64 + (id1 < 96 ? 40 + (id1 >> 2) : (id1 - 96) >> 2); }
constexpr int CAT_KS = 88;

#define GST16(voff, val, sbase) asm volatile("global_store_dwordx4 %0, %1, %2 sc1\n\ts_nop 1" :: "v"(voff), "v"(val), "s"(sbase) : "memory")

#define GLD16(dst, voff, sbase, IMM) asm volatile("global_load_dwordx4 %0, %1, %2 offset:" #IMM : "=v"(dst) : "v"(voff), "s"(sbase) : "memory")

DI void glds16(const void* sbase, unsigned voff, unsigned ldsdst) {
    unsigned keep;
    asm volatile("s_mov_b32 %0, m0\n\ts_mov_b32 m0, %3\n\ts_nop 0\n\tglobal_load_lds_dwordx4 %1, %2\n\ts_mov_b32 m0, %0"
                 : "=&s"(keep) : "v"(voff), "s"(sbase), "s"(ldsdst) : "memory");
}
DI unsigned pk2(float a, float b) { f32x2 v = {a, b}; return __builtin_bit_cast(unsigned, __builtin_convertvector(v, bf16x2)); }
DI float bflo(unsigned u) { return __uint_as_float(u << 16); }
DI float bfhi(unsigned u) { return __uint_as_float(u & 0xffff0000u); }
DI s16x4 trread(unsigned addr) { return __builtin_amdgcn_ds_read_tr16_b64_v4i16((LAS s16x4*)(uintptr_t)addr); }
DI bf16x8 trfrag(unsigned addr0, unsigned addr1) {
    s16x4 lo = trread(addr0), hi = trread(addr1);
    return __builtin_bit_cast(bf16x8, __builtin_shufflevector(lo, hi, 0, 1, 2, 3, 4, 5, 6, 7));
}
DI bf16x8 ldsfrag(unsigned addr) { return *(const LAS bf16x8*)(uintptr_t)addr; }
DI float gelu_t(float u) {
    const float k1 = -2.3022081985f;
    float w = u * __builtin_fmaf(u * u, k1 * 0.044715f, k1);
    float e = __builtin_amdgcn_exp2f(w);
    return u * __builtin_amdgcn_rcpf(1.0f + e);
}

constexpr int K2_K = 0, K2_V = 62 * 1024, K2_W = 126 * 1024, K2_ONES = K2_W + 32768, K2_MISC = K2_ONES + 1024, K2_LDS = K2_MISC + 1024;
enum { SY_KVA = 0, SY_KVB = 4, SY_P26 = 12, SY_LN = 16, SY_LNST = 20, SY_RED = 24, SY_FLAG = 28, SY_CMB = 36  , SY_P16 = 44, SY_MAIN = 48, SY_FIN = 52, SY_QKV = 56, SY_Z2 = 60, SY_HB = 128   };
constexpr int SPIN_MAX = 1 << 21, LSPIN_MAX = 1 << 24;

#define GLD16C(dst, voff, sbase, IMM) asm volatile("global_load_dwordx4 %0, %1, %2 offset:" #IMM " sc1" : "=v"(dst) : "v"(voff), "s"(sbase) : "memory")
DI void hb_sample(const char* p, unsigned ldsdst) {
    unsigned t;
    asm volatile("v_mov_b32 %0, 0\n\ts_mov_b32 m0, %2\n\ts_mov_b64 exec, 1\n\tglobal_load_lds_dword %0, %1 sc1\n\ts_mov_b64 exec, -1" : "=&v"(t) : "s"(p), "s"(ldsdst) : "memory");
}
DI void publish(char* p) {
    unsigned t0, t1;
    asm volatile("v_mov_b32 %0, 0\n\tv_mov_b32 %1, 1\n\ts_waitcnt vmcnt(0)\n\ts_mov_b64 exec, 1\n\tglobal_atomic_add %0, %1, %2\n\ts_mov_b64 exec, -1" : "=&v"(t0), "=&v"(t1) : "s"(p) : "memory");
}
DI void lds_inc(unsigned addr) {
    unsigned t0, t1;
    asm volatile("v_mov_b32 %0, %2\n\tv_mov_b32 %1, 1\n\ts_mov_b64 exec, 1\n\tds_add_u32 %0, %1\n\ts_mov_b64 exec, -1" : "=&v"(t0), "=&v"(t1) : "s"(addr) : "memory");
}
DI void addn(char* p, unsigned stride_shift, unsigned long long lanes) {
    unsigned t0, t1;
    asm volatile("v_mbcnt_lo_u32_b32 %0, -1, 0\n\tv_mov_b32 %1, 1\n\tv_lshlrev_b32 %0, %3, %0\n\ts_mov_b64 exec, %4\n\tglobal_atomic_add %0, %1, %2\n\ts_mov_b64 exec, -1"
                 : "=&v"(t0), "=&v"(t1) : "s"(p), "s"(stride_shift), "s"(lanes) : "memory");
}
DI void add16(char* p, unsigned stride_shift) { addn(p, stride_shift, 0xffff); }

constexpr int K1_MISC = 9 * 16384;
constexpr int K1_SUM = K1_MISC;
constexpr int K1_CS = K1_MISC + 1024;
constexpr int K1_PRIV = K1_MISC + 1024 + 2048;
constexpr int K1_JUNK = K1_PRIV + 8 * 256;
constexpr int K1_LDS = K1_JUNK + 1024;
static_assert(K1_LDS <= K2_ONES, "phase 2 words are initialised while phase 1 runs");
template <class F, int... I> DI void sfor_(F&& f, std::integer_sequence<int, I...>) { (f(std::integral_constant<int, I>{}), ...); }
template <int N, class F> DI void sfor(F&& f) { sfor_(f, std::make_integer_sequence<int, N>{}); }
#define LAMBDA(a) [&](auto a) __attribute__((always_inline))
DI unsigned word(bf16x8 v, int e) { const u32x4 u = __builtin_bit_cast(u32x4, v); return u[e]; }
DI f32x16 splat16(float v) { f32x16 r; for (int i = 0; i < 16; ++i) r[i] = v; return r; }

#define BLDS(rsrc, voff, soff, SIMM, m0b, MIMM) do { unsigned t_; \
    asm volatile("s_add_u32 m0, %4, %c6\n\ts_add_u32 %0, %3, %c5\n\tbuffer_load_dwordx4 %1, %2, %0 offen lds" \
                 : "=&s"(t_) : "v"(voff), "s"(rsrc), "s"(soff), "s"(m0b), "i"(SIMM), "i"(MIMM) : "scc", "memory"); } while (0)

#define BLDSC(rsrc, voff, soff, SIMM, m0b, MIMM) do { unsigned t_; \
    asm volatile("s_add_u32 m0, %4, %c6\n\ts_add_u32 %0, %3, %c5\n\tbuffer_load_dwordx4 %1, %2, %0 offen sc1 lds" \
                 : "=&s"(t_) : "v"(voff), "s"(rsrc), "s"(soff), "s"(m0b), "i"(SIMM), "i"(MIMM) : "scc", "memory"); } while (0)

DI void hb_check(const char* hbw, unsigned lds0) {
    const unsigned h0 = __builtin_amdgcn_readfirstlane(*(volatile LAS unsigned*)(uintptr_t)(lds0 + K2_MISC + SY_HB));
#pragma unroll 1
    for (int it = 0; it < SPIN_MAX && __builtin_amdgcn_readfirstlane(*(volatile LAS unsigned*)(uintptr_t)(lds0 + K2_MISC + SY_HB + 4)) == h0; ++it) {
        __builtin_amdgcn_s_sleep(8);
        hb_sample(hbw, lds0 + K2_MISC + SY_HB + 4);
        WAITV(0);
    }
}

template <bool ORI2>
DI bool k1_body(const char* __restrict__ x, const char* __restrict__ wqkv, const char* __restrict__ win, int c  , int rp,
                char* __restrict__ ws, unsigned lds0, const char* __restrict__ wsp) {
    constexpr int LDA = 3072, LDB = 3584, NA = ORI2 ? 1 : 2;
    const int tid = threadIdx.x, lane = tid & 63;
    const int wid = __builtin_amdgcn_readfirstlane(tid >> 6);
    const int wm = wid & 3, wn = wid >> 2;
    const int r = lane & 31, h = lane >> 5;
    const bool leader = blockIdx.x == 0 && wid == 1;
    char* hb = ws + WS_FLAG + CNT_HB * CNT_STRIDE;
    const char* hbw = hb + (blockIdx.x & 15) * CNT_STRIDE;
    const bool two = c < 6;
    const int zc = two ? c * 128 : 768 + (c - 6) * 64;
    const unsigned colA = ORI2 ? 2048 + c * 128 : c * 256, colB = ((ORI2 ? FH : 0) + zc) * 2;
    const __amdgpu_buffer_rsrc_t xr = __builtin_amdgcn_make_buffer_rsrc((void*)x, (short)0, S_ * D_ * 2, 0x00020000);
    const __amdgpu_buffer_rsrc_t ar = __builtin_amdgcn_make_buffer_rsrc((void*)wqkv, (short)0, D_ * LDA, 0x00020000);
    const __amdgpu_buffer_rsrc_t br = __builtin_amdgcn_make_buffer_rsrc((void*)win, (short)0, D_ * LDB, 0x00020000);
    const unsigned xso = rp * 131072 + wid * 16384, aso = colA + wid * 16 * LDA;
    unsigned bso = colB + wid * 16 * LDB;
    unsigned xvoff[2];
#pragma unroll
    for (int i = 0; i < 2; ++i) xvoff[i] = (i * 8 + (lane >> 3)) * 1024 + (((lane & 7) ^ ((4 * i + (lane >> 4)) & 7)) << 4);
    const unsigned avoff = ((lane >> 2) & 7) * LDA + (lane >> 5) * 64 + (lane & 3) * 16, bvoff = ((lane >> 2) & 7) * LDB + (lane >> 5) * 64 + (lane & 3) * 16;
    const unsigned sdst = lds0 + wid * 2048;
#define ISSX1(cc, slot, i) BLDS(xr, xvoff[i], xso, (cc) * 128, sdst, (slot) * 16384 + (i) * 1024)
#define ISSA1(pp, st, slot, i) BLDS(ar, avoff, aso, (pp) * 128 + ((st) * 128 + (i) * 8) * LDA, sdst, (slot) * 16384 + (i) * 1024)
#define ISSB1(st, base, off, i) BLDS(br, bvoff, bso, ((st) * 128 + (i) * 8) * LDB, base, (off) + (i) * 1024)
#define ISS11(st, slot, i) do { if (ORI2) ISSB1(st, sdst, (slot) * 16384, i); else ISSA1(1, st, slot, i); } while (0)
    ISSX1(0, 0, 0); ISSX1(0, 0, 1); ISSX1(1, 1, 0); ISSX1(1, 1, 1); ISSA1(0, 0, 2, 0); ISSA1(0, 0, 2, 1);
    ISSX1(2, 3, 0); ISSX1(2, 3, 1); ISSX1(3, 4, 0); ISSX1(3, 4, 1); ISSA1(0, 1, 5, 0); ISSA1(0, 1, 5, 1);
#define PERM(q) ((q) == 0 ? 0 : (q) == 1 ? 4 : (q) < 5 ? (q) - 1 : (q))
    unsigned xad[4], bad[4];
#pragma unroll
    for (int t = 0; t < 4; ++t) {
        xad[t] = lds0 + (wm * 32 + r) * 128 + (((2 * (t ^ wm) + h) ^ ((r >> 1) & 7)) << 4);
        bad[t] = lds0 + (2 * (t ^ wm) + h) * 1024 + wn * 512 + ((lane & 15) >> 2) * 64 + ((lane >> 4) & 1) * 32 + (lane & 3) * 8;
    }
    const unsigned selw = ((lane & 15) == ((lane >> 4) & 1)) ? 0x3f803f80u : 0u;
    u32x4 selu = {selw, selw, selw, selw};
    const bf16x8 sel = __builtin_bit_cast(bf16x8, selu);
    bf16x8 af[32];
    f32x16 accA, accB;
    float st0 = 0.f, st1 = 0.f;
    const unsigned one2 = 0x3f803f80u;
    f32x4 csum;
    const f32x4 z4 = {0, 0, 0, 0};
#define MM(bf, a, c) (ORI2 ? MFMA32(a, bf, c) : MFMA32(bf, a, c))
#define MM16(bf, a) asm volatile("v_mfma_f32_32x32x16_bf16 %0, %1, %2, %3" : "+v"(accB) : "v"(ORI2 ? (a) : (bf)), "v"(ORI2 ? (bf) : (a)), "v"(accA))
#define BFR(q, off) trfrag(bad[PERM(q) & 3] + (off) + (PERM(q) >> 2) * 8192, bad[PERM(q) & 3] + (off) + (PERM(q) >> 2) * 8192 + 256)
#define MAIN(i, bf) do { if ((i) == 0) accA = MM(bf, af[i], splat16(0.f)); else if ((i) < 16) accA = MM(bf, af[i], accA); else if ((i) == 16) MM16(bf, af[i]); else accB = MM(bf, af[i], accB); \
        if ((i) < 16) PIN(accA); else PIN(accB); \
        if ((i) == 18) asm volatile("" :: "v"(accA));        \
        if (((i) & 7) < 2) { csum = MFMA16(sel, bf, (i) == 0 ? z4 : csum); PIN(csum); } } while (0)
#define DA(k, l) "v_dot2c_f32_bf16 %0, %" #k ", %" #k "\n\tv_dot2c_f32_bf16 %1, %" #l ", %" #l "\n\t"
#define DB(k, l) "v_dot2c_f32_bf16 %0, %" #k ", %2\n\tv_dot2c_f32_bf16 %1, %" #l ", %2\n\t"
#define AW(i, e) "v"(word(af[i], e))
#define STATS4(i) asm volatile("s_cmp_lg_u32 %3, 0\n\ts_cbranch_scc1 1f\n\t" DA(4, 5) DA(6, 7) DA(8, 9) DA(10, 11) DA(12, 13) DA(14, 15) DA(16, 17) DA(18, 19) "s_branch 2f\n1:\n\t" \
            DB(4, 5) DB(6, 7) DB(8, 9) DB(10, 11) DB(12, 13) DB(14, 15) DB(16, 17) DB(18, 19) "2:" : "+v"(st0), "+v"(st1) : "v"(one2), "s"(wn), \
            AW(i, 0), AW(i, 1), AW(i, 2), AW(i, 3), AW(i + 1, 0), AW(i + 1, 1), AW(i + 1, 2), AW(i + 1, 3), AW(i + 2, 0), AW(i + 2, 1), AW(i + 2, 2), AW(i + 2, 3), \
            AW(i + 3, 0), AW(i + 3, 1), AW(i + 3, 2), AW(i + 3, 3) : "scc")
#define BAR(n) asm volatile("s_waitcnt vmcnt(" #n ") lgkmcnt(0)\n\ts_barrier" ::: "memory")
    asm volatile("" : "=v"(accB));
    const unsigned csw = lds0 + K1_CS + wn * 512 + (lane & 15) * 16 + wm * 4;
    const unsigned csr = lds0 + K1_CS + wn * 512 + r * 16;
    const unsigned priv = lds0 + K1_PRIV + wid * 256;
#define CSWRITE(par) do { if (lane < 16) { *(LAS float*)(uintptr_t)(csw + (par)) = csum[0]; *(LAS float*)(uintptr_t)(csw + (par) + 256) = csum[1]; } } while (0)
    bf16x8 bq[4];
    {
        const unsigned prow = (rp & 7) * 64 + wid * 8, pa = colA + prow * LDA, pb = colB + prow * LDB, junk = lds0 + K1_JUNK;
#pragma unroll
        for (int t = 0; t < 4; ++t) bad[t] += 2 * 16384;
        sfor<35>(LAMBDA(U) {
            constexpr int u = U, i = u - 3, st = u >> 3, q = u & 7, ip = i < 0 ? 0 : i, s4 = st & 3;
            if (q == 0) {
                if (st == 4) {
                    const float t = st0 + st1;
                    auto rr = __builtin_amdgcn_permlane32_swap(__float_as_uint(t), __float_as_uint(t), false, false);
                    *(LAS float*)(uintptr_t)(lds0 + K1_SUM + wn * 512 + (wm * 32 + r) * 4) = __uint_as_float(rr[0]) + __uint_as_float(rr[1]);
                    CSWRITE(0);
                }
                if (st == 0) BAR(6); else if (st < 3) BAR(7); else if (st == 3) BAR(6); else BAR(3);
                if (st > 0) {
#pragma unroll
                    for (int t = 0; t < 4; ++t) { if (st < 4) xad[t] += st == 3 ? -6 * 16384 : 3 * 16384; bad[t] += st == 3 ? -6 * 16384 : st == 4 ? 16384 : 3 * 16384; }
                }
                if (st == 4 && leader) { WAITV(0); add16(hb, 8); }
            }
            if constexpr (st < 4 && q < 4) { af[st * 8 + 2 * q] = ldsfrag(xad[PERM(2 * q) & 3] + (PERM(2 * q) >> 2) * 16384); af[st * 8 + 2 * q + 1] = ldsfrag(xad[PERM(2 * q + 1) & 3] + (PERM(2 * q + 1) >> 2) * 16384); }
            bq[u & 3] = BFR(q, 0);
            if (i >= 0) MAIN(ip, bq[ip & 3]);
            if (st == 0) {
                if (q < 2) ISSX1(4, 6, q & 1); else if (q < 4) ISSX1(5, 7, q & 1); else if (q < 6) ISSA1(0, 2, 8, q & 1);
                else if (q == 6) { if (ORI2) BLDS(br, bvoff, pb, 0, junk, 0); else BLDS(ar, avoff, pa, 128, junk, 0); }
            }
            if (st == 1) { if (q < 2) ISSX1(6, 0, q & 1); else if (q < 4) ISSX1(7, 1, q & 1); else if (q < 6) ISSA1(0, 3, 2, q & 1); }
            if (st == 2 && q < 6) ISS11(q >> 1, 3 + (q >> 1), q & 1);
            if (st == 3) {
                if (q < 2) ISS11(3, 6, q & 1);
                if (q == 2) BLDS(br, bvoff, pb, ORI2 ? 128 : 0, junk, 0);
                if (q == 3) BLDS(br, bvoff, pb, 128, junk, 0);
                if (q == 4) {
                    const __amdgpu_buffer_rsrc_t sr = __builtin_amdgcn_make_buffer_rsrc((void*)wsp, (short)0, S_ * S_ * 2, 0x00020000);
                    const unsigned so = ((blockIdx.x & 7) * 32 + (blockIdx.x >> 3)) * 32768 + wm * 1024 + wn * 128, so_v = (lane >> 3) * 4096 + (lane & 7) * 16;
                    BLDS(sr, so_v, so, 0, junk, 0);
                }
            }
            if constexpr (st < 4 && (q == 4 || q == 6)) STATS4(s4 * 8 + (q - 4) * 2);
            SB();
        });
    }
    float rs, nm, rsz, nmz;
    {
        const float ss = *(LAS float*)(uintptr_t)(lds0 + K1_SUM + (wm * 32 + r) * 4), sm = *(LAS float*)(uintptr_t)(lds0 + K1_SUM + 512 + (wm * 32 + r) * 4);
        const float mean = sm * (1.0f / 512.0f);
        const float var = fmaxf(ss * (1.0f / 512.0f) - mean * mean, 0.0f);
        rsz = __builtin_amdgcn_rsqf(var + 1e-5f);
        nmz = -mean * rsz;
        rs = rsz * (c < 4 ? 0.25f * 1.4426950408889634f : 1.0f);
        nm = -mean * rs;
        if (ORI2) *(LAS float*)(uintptr_t)(priv + h * 128 + r * 4) = h ? nmz : rsz;
    }
    const int rt = rp * 4 + wm;
    char* dst = ws + (ORI2 ? WS_V + ((size_t)(c * 4 + wn * 2) * 64 + rt) * 1024 : WS_Q + ((size_t)(c * 8 + wn * 2) * 64 + rt) * 1024);
    unsigned dvoff = ORI2 ? ((lane >> 4) & 1) * 65536 + (lane >> 5) * 256 + (lane & 15) * 16 : lane * 16;
    f32x4 ea[2], eb[2], c4; float cs; unsigned pk[4];
#define EPI_BEGIN0(par) c4 = *(LAS f32x4*)(uintptr_t)(csr + (par))
#define EPI_BEGIN1() do { cs = (c4[0] + c4[1]) + (c4[2] + c4[3]); PIN(cs); if (!ORI2) *(LAS float*)(uintptr_t)(priv + r * 4) = cs; } while (0)
#define EPI_LOAD(q) do { ea[(q) & 1] = *(LAS f32x4*)(uintptr_t)(priv + (q) * 32 + h * 16); if (ORI2) eb[(q) & 1] = *(LAS f32x4*)(uintptr_t)(priv + 128 + (q) * 32 + h * 16); } while (0)
#define EPI_Y(V, e) (ORI2 ? __builtin_fmaf(eb[((e) >> 2) & 1][(e) & 3], cs, (V) * ea[((e) >> 2) & 1][(e) & 3]) : __builtin_fmaf(GELU ? nmz : nm, ea[((e) >> 2) & 1][(e) & 3], (V) * (GELU ? rsz : rs)))
#define EPI_PAIR(V0, V1, e) do { float y0_ = EPI_Y(V0, e), y1_ = EPI_Y(V1, (e) + 1); if (GELU) { y0_ = gelu_t(y0_); y1_ = gelu_t(y1_); } \
        pk[((e) >> 1) & 3] = pk2(y0_, y1_); PIN(pk[((e) >> 1) & 3]); } while (0)
#define EPI_STORE(j) do { auto s0_ = __builtin_amdgcn_permlane32_swap(pk[0], pk[2], false, false); auto s1_ = __builtin_amdgcn_permlane32_swap(pk[1], pk[3], false, false); \
        u32x4 v_ = {s0_[0], s1_[0], s0_[1], s1_[1]}; GST16(dvoff, v_, dst + (j) * (GELU ? (ORI2 ? 2 * FH * 16 : 1024) : (ORI2 ? 512 : 65536))); } while (0)
    char* cntA = ws + WS_FLAG + (size_t)(CNT_QKV + (ORI2 ? c >> 1 : c & 3) * 8) * CNT_STRIDE;
    EPI_BEGIN0(0);
    unsigned par = 0, rslot = 3, wslot = 7;
    auto pass = [&](auto G, bool more, bool lastA) __attribute__((always_inline)) {
        constexpr bool GELU = G;
        unsigned wd[4];
#pragma unroll
        for (int s = 0; s < 4; ++s) { wd[s] = sdst + wslot * 16384; wslot = wslot == 8 ? 0 : wslot + 1; }
        float hi[8];
        SB();
        sfor<32>(LAMBDA(I) {
            constexpr int i = I;
            if (GELU && i == 6) {
                WAITV(0);
                if (wid == 0) hb_check(hbw, lds0);
                asm volatile("s_barrier" ::: "memory");
                if (wid == 7) addn(cntA, 8, 0xff);
            }
            if (i == 29) {
                par ^= 1024;
                if (more) WAITV(2);
                asm volatile("s_waitcnt lgkmcnt(0)\n\ts_barrier" ::: "memory");
                EPI_BEGIN0(par);
                if (leader) add16(hb, 8);
                if (!GELU && wid == 0 && lastA) hb_sample(hbw, lds0 + K2_MISC + SY_HB + 4);
            }
            if (((i + 3) & 7) == 0) {
                const unsigned d = rslot == 8 ? (unsigned)(-8 * 16384) : 16384u;
                rslot = rslot == 8 ? 0 : rslot + 1;
#pragma unroll
                for (int t = 0; t < 4; ++t) bad[t] += d;
            }
            bq[(i + 3) & 3] = BFR((i + 3) & 7, 0);
            if constexpr (i >= 4 && i < 12) asm volatile("v_mov_b32 %0, %1" : "=&v"(hi[i - 4]) : "v"(accB[8 + i - 4]));
            if (i == 0) EPI_BEGIN1();
            if (i == 1 || i == 7 || i == 13 || i == 19) EPI_LOAD((i - 1) / 6);
            if constexpr (i >= 5 && i < 28 && (i - 5) % 3 == 0) { constexpr int e = ((i - 5) / 3) * 2; EPI_PAIR(e < 8 ? accB[e & 7] : hi[e & 7], e < 8 ? accB[(e + 1) & 7] : hi[(e + 1) & 7], e); }
            MAIN(i, bq[i & 3]);
            if constexpr (!GELU && i < 16 && !(i & 1)) ISSB1(i >> 2, wd[i >> 2], 0, (i >> 1) & 1);
            if (i == 15 || i == 27) EPI_STORE(i == 27);
            if (i == 27) CSWRITE(par ^ 1024);
            SB();
        });
    };
#pragma unroll 1
    for (int p = 1; p <= NA; ++p) {
        const bool lastA = p == NA, more = !lastA || two;
        if (lastA) bso = two ? bso + 128 : 0x40000000u;
        pass(std::false_type{}, more, lastA);
        dst += 4 * 65536;
    }
    dst = ws + (ORI2 ? WS_Z2 + ((size_t)rt * 4 * FH + zc + wn * 32) * 16 : WS_Z1 + ((size_t)rt * 56 + (zc >> 4) + wn * 2) * 1024);
    if (ORI2) dvoff = (lane >> 5) * (FH * 16) + (lane & 31) * 16;
    if (two) { pass(std::true_type{}, false, false); dst += ORI2 ? 1024 : 4096; }
    unsigned look = 0;
    const bool looks = two && (wid == 4 || wid == 7);
    if (looks) {
        const int bb = blockIdx.x, head = (bb & 7) * 4 + (bb >> 6);
        char* lp = ws + WS_FLAG + (size_t)(wid == 4 ? CNT_Z2 + (grp_of(id1_of(bb)) >> 4) : CNT_QKV + (head >> 3) * 8 + ((bb >> 3) & 7)) * CNT_STRIDE;
        asm volatile("s_mov_b64 exec, 1\n\tglobal_atomic_add %0, %0, %0, %1 sc0\n\ts_mov_b64 exec, -1" : "+v"(look) : "s"(lp) : "memory");
    }
    {
        constexpr bool GELU = true;
        EPI_BEGIN1();
        EPI_LOAD(0);
#pragma unroll
        for (int q = 0; q < 4; ++q) {
            if (q < 3) EPI_LOAD(q + 1);
#pragma unroll
            for (int e = 0; e < 4; e += 2) EPI_PAIR(accB[q * 4 + e], accB[q * 4 + e + 1], q * 4 + e);
            if (q & 1) EPI_STORE(q >> 1);
        }
    }
    asm volatile("s_waitcnt vmcnt(0)" : "+v"(look) :: "memory");
    const bool seen = looks && __builtin_amdgcn_readfirstlane(look) == (wid == 4 ? 8u : 64u);
    if (seen) lds_inc(lds0 + K2_MISC + (wid == 4 ? SY_Z2 : SY_QKV));
    if (!two && wid == 0) hb_check(hbw, lds0);
    asm volatile("s_waitcnt lgkmcnt(0)\n\ts_barrier" ::: "memory");
    if (wid == 7) {
        if (!two) addn(cntA, 8, 0xff);
        addn(ws + WS_FLAG + (size_t)((ORI2 ? CNT_Z2 : CNT_Z1) + rp) * CNT_STRIDE, 8, 1);
    }
    return seen;
}

DI bool phase1(const char* __restrict__ x, const char* __restrict__ wqkv, const char* __restrict__ win, char* __restrict__ ws, const char* __restrict__ wsp, unsigned lds0) {
    const int b = blockIdx.x, rp = id1_of(b) & 15, ct = id1_of(b) >> 4;
    const int lane = threadIdx.x & 63, wid = __builtin_amdgcn_readfirstlane(threadIdx.x >> 6);
    if (wid == 0) hb_sample(ws + WS_FLAG + (CNT_HB + (b & 15)) * CNT_STRIDE, lds0 + K2_MISC + SY_HB);
    if (b == 0 && wid == 1) {
#pragma unroll
        for (int i = 0; i < 9; ++i) __hip_atomic_store((unsigned*)(ws + WS_FLAG + (size_t)(i * 64 + lane) * CNT_STRIDE), 0u, __ATOMIC_RELAXED, __HIP_MEMORY_SCOPE_AGENT);
    }
    if (threadIdx.x < 256) *(LAS unsigned*)(uintptr_t)(lds0 + K2_ONES + threadIdx.x * 4) = ((threadIdx.x >> 2) & 11) == 0 ? 0x3f803f80u : 0u;
    else if (threadIdx.x < 256 + 32) *(LAS unsigned*)(uintptr_t)(lds0 + K2_MISC + (threadIdx.x - 256) * 4) = 0u;
    if (__builtin_expect(ct < 8, 1)) return k1_body<false>(x, wqkv, win, ct, rp, ws, lds0, wsp);
    return k1_body<true>(x, wqkv, win, ct - 8, rp, ws, lds0, wsp);
}

constexpr float REF_OFF = 96.0f;

template <int SLEEP = 0> DI void lds_wait(unsigned addr, unsigned n) {
    unsigned t0, t1, c, v;
    if (SLEEP == 0)
        asm volatile("v_mov_b32 %0, %4\n\ts_mov_b32 %2, %6\n1:\n\tds_read_b32 %1, %0\n\ts_sub_u32 %2, %2, 1\n\ts_waitcnt lgkmcnt(0)\n\tv_readfirstlane_b32 %3, %1\n\ts_nop 0\n\t"
                     "s_cselect_b32 %3, %5, %3\n\ts_cmp_lt_u32 %3, %5\n\ts_cbranch_scc1 1b"
                     : "=&v"(t0), "=&v"(t1), "=&s"(c), "=&s"(v) : "s"(addr), "s"(n), "i"(LSPIN_MAX) : "scc", "memory");
    else
        asm volatile("v_mov_b32 %0, %4\n\ts_mov_b32 %2, %6\n\ts_branch 2f\n1:\n\ts_sleep %c7\n2:\n\tds_read_b32 %1, %0\n\ts_sub_u32 %2, %2, 1\n\ts_waitcnt lgkmcnt(0)\n\tv_readfirstlane_b32 %3, %1\n\ts_nop 0\n\t"
                     "s_cselect_b32 %3, %5, %3\n\ts_cmp_lt_u32 %3, %5\n\ts_cbranch_scc1 1b"
                     : "=&v"(t0), "=&v"(t1), "=&s"(c), "=&s"(v) : "s"(addr), "s"(n), "i"(LSPIN_MAX), "i"(SLEEP) : "scc", "memory");
}
DI bf16x16 lo8(const f32x16& x) { f32x8 t = __builtin_shufflevector(x, x, 0, 1, 2, 3, 4, 5, 6, 7); return __builtin_bit_cast(bf16x16, t); }
DI bf16x16 hi8(const f32x16& x) { f32x8 t = __builtin_shufflevector(x, x, 8, 9, 10, 11, 12, 13, 14, 15); return __builtin_bit_cast(bf16x16, t); }
#define PRIO_HI() __builtin_amdgcn_s_setprio(2)
#define PRIO_LO() __builtin_amdgcn_s_setprio(0)
#define SMF(a, b, c) __builtin_amdgcn_smfmac_f32_32x32x32_bf16(a, b, c, sidx, 0, 0)
#define GLD16S(dst, voff, sbase) GLD16C(dst, voff, sbase, 0)

DI float max16(const f32x16& s) {
    float a = fmaxf(fmaxf(s[0], s[1]), s[2]), b = fmaxf(fmaxf(s[3], s[4]), s[5]);
    a = fmaxf(fmaxf(a, s[6]), s[7]); b = fmaxf(fmaxf(b, s[8]), s[9]);
    a = fmaxf(fmaxf(a, s[10]), s[11]); b = fmaxf(fmaxf(b, s[12]), s[13]);
    a = fmaxf(fmaxf(a, s[14]), s[15]);
    return fmaxf(a, b);
}
DI float xhalf_max(float m) {
    auto rr = __builtin_amdgcn_permlane32_swap(__float_as_uint(m), __float_as_uint(m), false, false);
    return fmaxf(__uint_as_float(rr[0]), __uint_as_float(rr[1]));
}

#define LD4C(d, p, voff, o0, o1, o2, o3) asm volatile("global_load_dwordx4 %0, %4, %5 offset:" #o0 " sc1\n\tglobal_load_dwordx4 %1, %4, %5 offset:" #o1 " sc1\n\t" \
        "global_load_dwordx4 %2, %4, %5 offset:" #o2 " sc1\n\tglobal_load_dwordx4 %3, %4, %5 offset:" #o3 " sc1\n\ts_waitcnt vmcnt(0)" \
        : "=&v"(d[0]), "=&v"(d[1]), "=&v"(d[2]), "=&v"(d[3]) : "v"(voff), "s"(p) : "memory")
DI void att_exact(const char* kg, const char* vg, bf16x8 q0, bf16x8 q1, f32x16& o0, f32x16& o1) {
    const unsigned lane = threadIdx.x & 63, l16 = lane * 16, vo = (lane >> 5) * 256 + (lane & 15) * 16;
    const int sidx = (int)0xDDDDDDDDu;
    const f32x16 z = splat16(0.f);
    float m0 = -INFINITY, m1 = -INFINITY;
#pragma unroll 1
    for (int t = 0; t < 64; t += 4) {
        bf16x8 kf[4];
        LD4C(kf, kg + t * 1024, l16, 0, 1024, 2048, 3072);
#pragma unroll
        for (int i = 0; i < 4; ++i) { m0 = fmaxf(m0, max16(MFMA32(kf[i], q0, z))); m1 = fmaxf(m1, max16(MFMA32(kf[i], q1, z))); }
    }
    const f32x16 c0 = splat16(-xhalf_max(m0)), c1 = splat16(-xhalf_max(m1));
    o0 = z; o1 = z;
    const u32x4 onesu = {0x3f803f80u, 0x3f803f80u, 0x3f803f80u, 0x3f803f80u};
#pragma unroll 1
    for (int t = 0; t < 64; t += 2) {
        bf16x8 kf[4]; u32x4 vf[4];
        LD4C(vf, vg + t * 1024, vo, 0, 512, 1024, 1536);
        LD4C(kf, kg + t * 1024, l16, 0, 1024, 0, 1024);
#pragma unroll
        for (int i = 0; i < 2; ++i) {
            const bf16x8 va = __builtin_bit_cast(bf16x8, (lane & 16) ? onesu : vf[2 * i]), vb = __builtin_bit_cast(bf16x8, (lane & 16) ? onesu : vf[2 * i + 1]);
            f32x16 s0 = MFMA32(kf[i], q0, c0), s1 = MFMA32(kf[i], q1, c1);
#pragma unroll
            for (int e = 0; e < 16; ++e) { s0[e] = __builtin_amdgcn_exp2f(s0[e]); s1[e] = __builtin_amdgcn_exp2f(s1[e]); }
            o0 = SMF(va, lo8(s0), o0); o0 = SMF(vb, hi8(s0), o0);
            o1 = SMF(va, lo8(s1), o1); o1 = SMF(vb, hi8(s1), o1);
        }
    }
}

DI void att_store(const f32x16& o, char* dst, unsigned l16) {
    const float rl = __builtin_amdgcn_rcpf(o[8]);
    unsigned p0 = pk2(o[0] * rl, o[1] * rl), p1 = pk2(o[2] * rl, o[3] * rl), p2 = pk2(o[4] * rl, o[5] * rl), p3 = pk2(o[6] * rl, o[7] * rl);
    auto s0 = __builtin_amdgcn_permlane32_swap(p0, p2, false, false);
    auto s1 = __builtin_amdgcn_permlane32_swap(p1, p3, false, false);
    u32x4 v = {s0[0], s1[0], s0[1], s1[1]};
    GST16(l16, v, dst);
}
DI float att_mag(const f32x16& o) {
    return ((fabsf(o[0]) + fabsf(o[1])) + (fabsf(o[2]) + fabsf(o[3]))) + ((fabsf(o[4]) + fabsf(o[5])) + (fabsf(o[6]) + fabsf(o[7]))) + o[8];
}

DI void att_role(int head, int qb, int w, char* __restrict__ ws, unsigned lds0) {
    const int lane = threadIdx.x & 63;
    const int qh = w & 1, kh = w >> 1;
    const char* kg = ws + WS_K + (size_t)head * 65536;
    const char* vg = ws + WS_V + (size_t)head * 65536;
    const char* qg = ws + WS_Q + (size_t)head * 65536 + (size_t)(qb * 8 + qh * 4) * 1024;
    const unsigned l16 = lane * 16;
    const unsigned vo = (lane >> 5) * 256 + (lane & 15) * 16;
    u32x4 qr[4], k0, ks;
    const unsigned qo = l16 + kh * 2048, qo2 = l16 + (kh ^ 1) * 2048;
    int sidx = (int)0xDDDDDDDDu; PIN(sidx);
    f32x16 oo[4] = {splat16(0.f), splat16(0.f), splat16(0.f), splat16(0.f)}, ss[4];
    ss[3] = splat16(0.f);
    u32x4 v0 = {0, 0, 0, 0}, v1 = {0, 0, 0, 0};
    asm volatile("" : "+{v[0:15]}"(oo[0]), "+{v[16:31]}"(oo[1]), "+{v[32:47]}"(oo[2]), "+{v[48:63]}"(oo[3]), "=&{v[144:159]}"(ss[1]), "=&{v[160:175]}"(ss[2]), "+{v[176:191]}"(ss[3]),
                      "+{v[212:215]}"(v0), "+{v[216:219]}"(v1));
    unsigned kaddr = lds0 + K2_K + kh * 31744 + l16;
    unsigned vaddr = (lane & 16) ? (lds0 + K2_ONES + vo) : (lds0 + K2_V + kh * 32768 + vo);
    unsigned vinc = (lane & 16) ? 0u : 1024u;
    u32x4 e0 = {0x3f80u, 0u, 0u, 0u};
    const unsigned cb = lds0 + (qh ? K2_V : K2_K) + 20 * 1024 + l16, cst = qh ? 32768 : 31744;
    unsigned cwa = cb + kh * cst, cra = cb + (kh ^ 1) * cst;
    char* dst = ws + WS_CAT + ((size_t)(qb * 8 + qh * 4 + kh * 2) * CAT_KS + head) * 1024;
    char* cnt = ws + WS_FLAG + (size_t)(CNT_REC + (qb * 4 + qh * 2 + kh) * 8 + 4 + (head >> 3)) * CNT_STRIDE;
    PIN(kaddr); PIN(vaddr); PIN(vinc); PIN(e0); PIN(cwa); PIN(cra);
    asm volatile("" : "+s"(dst), "+s"(cnt));
    lds_wait<1>(lds0 + K2_MISC + SY_QKV, 1);
    GLD16C(ks, l16, kg, 0);
    GLD16C(qr[0], qo, qg, 0); GLD16C(qr[1], qo, qg, 1024); GLD16C(qr[2], qo2, qg, 0); GLD16C(qr[3], qo2, qg, 1024);
    GLD16C(k0, l16, kg + kh * 32768, 0);
    {
        const __amdgpu_buffer_rsrc_t kr = __builtin_amdgcn_make_buffer_rsrc((void*)(ws + WS_K), (short)0, 4u << 20, 0x00020000);
        const unsigned so = head * 65536 + (qh ? (unsigned)(WS_V - WS_K) : 1024u) + kh * 32768, d = lds0 + (qh ? K2_V + kh * 32768 : K2_K + kh * 31744);
        BLDSC(kr, l16, so, 0, d, 0); BLDSC(kr, l16, so, 1024, d, 1024); BLDSC(kr, l16, so, 2048, d, 2048); BLDSC(kr, l16, so, 3072, d, 3072); BLDSC(kr, l16, so, 4096, d, 4096);
    }
    asm volatile("s_waitcnt vmcnt(5)" : "+v"(qr[0]), "+v"(qr[1]), "+v"(qr[2]), "+v"(qr[3]), "+v"(k0), "+v"(ks) :: "memory");
#define QF(i) __builtin_bit_cast(bf16x8, qr[i])
    f32x16 cc[4];
#pragma unroll
    for (int i = 0; i < 4; ++i) { cc[i] = MFMA32(__builtin_bit_cast(bf16x8, ks), QF(i), splat16(0.f)); PIN(cc[i]); SB(); }
#pragma unroll
    for (int i = 0; i < 4; ++i) {
        const float m = __builtin_fmaf(max16(cc[i]), -0.5f, -0.5f * REF_OFF);
        u32x4 mb = {__float_as_uint(m) >> 16, 0u, 0u, 0u};
        cc[i] = MFMA32(__builtin_bit_cast(bf16x8, e0), __builtin_bit_cast(bf16x8, mb), splat16(0.f));
        PIN(cc[i]);
    }
    u32x4 kf = k0;
    WAITV(0); lds_inc(lds0 + K2_MISC + SY_KVA);
    asm volatile("v_mfma_f32_32x32x16_bf16 v[128:143], v[208:211], v[192:195], v[64:79]" : "=&{v[128:143]}"(ss[0]) : "{v[208:211]}"(kf), "{v[192:195]}"(qr[0]), "{v[64:79]}"(cc[0]));
    lds_wait(lds0 + K2_MISC + SY_KVA, 4);
#pragma unroll 1
    for (int seg = 0; seg < 5; ++seg) {
        int n = 3;
        if (seg == 1) { n = 7; lds_wait(lds0 + K2_MISC + SY_KVB, 1); }
        else if (seg == 2) { n = 4; lds_wait(lds0 + K2_MISC + SY_KVB, 2); }
        else if (seg >= 3) { n = seg == 3 ? 9 : 4; lds_inc(lds0 + K2_MISC + (seg == 3 ? SY_P16 : SY_P26)); }
        asm volatile("s_nop 7\n\t.p2align 6\n1:\n\t"
            "v_mfma_f32_32x32x16_bf16 v[144:159], v[208:211], v[196:199], v[80:95]\n\tv_exp_f32 v128, v128\n\tv_exp_f32 v129, v129\n\tv_exp_f32 v130, v130\n\t"
            "v_exp_f32 v131, v131\n\tv_smfmac_f32_32x32x32_bf16 v[48:63], v[212:215], v[176:183], v220\n\tds_read_b128 v[212:215], v222\n\tv_exp_f32 v132, v132\n\t"
            "v_exp_f32 v133, v133\n\tv_exp_f32 v134, v134\n\tv_exp_f32 v135, v135\n\tv_smfmac_f32_32x32x32_bf16 v[48:63], v[216:219], v[184:191], v220\n\t"
            "ds_read_b128 v[216:219], v222 offset:512\n\tv_exp_f32 v136, v136\n\tv_exp_f32 v137, v137\n\tv_exp_f32 v138, v138\n\tv_exp_f32 v139, v139\n\t"
            "v_exp_f32 v140, v140\n\tv_exp_f32 v141, v141\n\tv_exp_f32 v142, v142\n\tv_exp_f32 v143, v143\n\t"
            "v_mfma_f32_32x32x16_bf16 v[160:175], v[208:211], v[200:203], v[96:111]\n\tv_exp_f32 v144, v144\n\tv_exp_f32 v145, v145\n\tv_exp_f32 v146, v146\n\t"
            "v_exp_f32 v147, v147\n\ts_waitcnt lgkmcnt(1)\n\tv_smfmac_f32_32x32x32_bf16 v[0:15], v[212:215], v[128:135], v220\n\tv_exp_f32 v148, v148\n\t"
            "v_exp_f32 v149, v149\n\tv_exp_f32 v150, v150\n\tv_exp_f32 v151, v151\n\ts_waitcnt lgkmcnt(0)\n\t"
            "v_smfmac_f32_32x32x32_bf16 v[0:15], v[216:219], v[136:143], v220\n\tv_add_u32 v222, v222, v223\n\tv_exp_f32 v152, v152\n\tv_exp_f32 v153, v153\n\t"
            "v_exp_f32 v154, v154\n\tv_exp_f32 v155, v155\n\tv_exp_f32 v156, v156\n\tv_exp_f32 v157, v157\n\tv_exp_f32 v158, v158\n\tv_exp_f32 v159, v159\n\t"
            "v_mfma_f32_32x32x16_bf16 v[176:191], v[208:211], v[204:207], v[112:127]\n\tds_read_b128 v[208:211], v221\n\tv_exp_f32 v160, v160\n\t"
            "v_exp_f32 v161, v161\n\tv_exp_f32 v162, v162\n\tv_exp_f32 v163, v163\n\tv_smfmac_f32_32x32x32_bf16 v[16:31], v[212:215], v[144:151], v220\n\t"
            "v_exp_f32 v164, v164\n\tv_exp_f32 v165, v165\n\tv_exp_f32 v166, v166\n\tv_exp_f32 v167, v167\n\t"
            "v_smfmac_f32_32x32x32_bf16 v[16:31], v[216:219], v[152:159], v220\n\tv_add_u32 v221, 0x400, v221\n\tv_exp_f32 v168, v168\n\tv_exp_f32 v169, v169\n\t"
            "v_exp_f32 v170, v170\n\tv_exp_f32 v171, v171\n\tv_exp_f32 v172, v172\n\tv_exp_f32 v173, v173\n\tv_exp_f32 v174, v174\n\tv_exp_f32 v175, v175\n\t"
            "s_waitcnt lgkmcnt(0)\n\tv_mfma_f32_32x32x16_bf16 v[128:143], v[208:211], v[192:195], v[64:79]\n\tv_exp_f32 v176, v176\n\tv_exp_f32 v177, v177\n\t"
            "v_exp_f32 v178, v178\n\tv_exp_f32 v179, v179\n\tv_smfmac_f32_32x32x32_bf16 v[32:47], v[212:215], v[160:167], v220\n\tv_exp_f32 v180, v180\n\t"
            "v_exp_f32 v181, v181\n\tv_exp_f32 v182, v182\n\tv_exp_f32 v183, v183\n\tv_smfmac_f32_32x32x32_bf16 v[32:47], v[216:219], v[168:175], v220\n\t"
            "v_exp_f32 v184, v184\n\tv_exp_f32 v185, v185\n\tv_exp_f32 v186, v186\n\tv_exp_f32 v187, v187\n\tv_exp_f32 v188, v188\n\tv_exp_f32 v189, v189\n\t"
            "s_sub_u32 %0, %0, 1\n\tv_exp_f32 v190, v190\n\tv_exp_f32 v191, v191\n\t"
            "s_cbranch_scc0 1b"
            : "+s"(n), "+{v[0:15]}"(oo[0]), "+{v[16:31]}"(oo[1]), "+{v[32:47]}"(oo[2]), "+{v[48:63]}"(oo[3]), "+{v[128:143]}"(ss[0]), "+{v[144:159]}"(ss[1]), "+{v[160:175]}"(ss[2]), "+{v[176:191]}"(ss[3]), "+{v[208:211]}"(kf), "+{v[212:215]}"(v0), "+{v[216:219]}"(v1), "+{v221}"(kaddr), "+{v222}"(vaddr)
            : "{v[64:79]}"(cc[0]), "{v[80:95]}"(cc[1]), "{v[96:111]}"(cc[2]), "{v[112:127]}"(cc[3]), "{v[192:195]}"(qr[0]), "{v[196:199]}"(qr[1]), "{v[200:203]}"(qr[2]), "{v[204:207]}"(qr[3]), "{v220}"(sidx), "{v223}"(vinc)
            : "scc", "memory");
    }
    asm volatile("v_smfmac_f32_32x32x32_bf16 v[48:63], v[212:215], v[176:183], v220\n\ts_nop 0\n\tv_smfmac_f32_32x32x32_bf16 v[48:63], v[216:219], v[184:191], v220\n\ts_nop 7\n\ts_nop 7"
            : "+{v[48:63]}"(oo[3]) : "{v[176:191]}"(ss[3]), "{v[212:215]}"(v0), "{v[216:219]}"(v1), "{v220}"(sidx));
    __builtin_amdgcn_s_setprio(3);
    lds_wait(lds0 + K2_MISC + SY_P26, 4);
#pragma unroll
    for (int i = 0; i < 2; ++i) {
        f32x4 a = {oo[2 + i][0], oo[2 + i][1], oo[2 + i][2], oo[2 + i][3]}, b = {oo[2 + i][4], oo[2 + i][5], oo[2 + i][6], oo[2 + i][7]};
        *(LAS f32x4*)(uintptr_t)(cwa + i * 2048) = a;
        *(LAS f32x4*)(uintptr_t)(cwa + i * 2048 + 1024) = b;
    }
    { f32x2 l2 = {oo[2][8], oo[3][8]}; *(LAS f32x2*)(uintptr_t)(cwa + 4096) = l2; }
    asm volatile("s_waitcnt lgkmcnt(0)" ::: "memory");
    lds_inc(lds0 + K2_MISC + SY_CMB + qh * 4);
    lds_wait(lds0 + K2_MISC + SY_CMB + qh * 4, 2);
    {
        const f32x2 l2 = *(LAS f32x2*)(uintptr_t)(cra + 4096);
#pragma unroll
        for (int i = 0; i < 2; ++i) {
            const f32x4 a = *(LAS f32x4*)(uintptr_t)(cra + i * 2048), b = *(LAS f32x4*)(uintptr_t)(cra + i * 2048 + 1024);
#pragma unroll
            for (int e = 0; e < 4; ++e) { oo[i][e] += a[e]; oo[i][4 + e] += b[e]; }
            oo[i][8] += l2[i];
            PIN(oo[i]);
        }
    }
    const bool ok = (att_mag(oo[0]) + att_mag(oo[1]) < 0x1p120f) & (fminf(oo[0][8], oo[1][8]) > 0x1p-100f);
    if (__builtin_expect(__builtin_amdgcn_ballot_w64(!ok) != 0, 0)) att_exact(kg, vg, QF(0), QF(1), oo[0], oo[1]);
    att_store(oo[0], dst, l16);
    att_store(oo[1], dst + CAT_KS * 1024, l16);
    publish(cnt);
}

template <int CTRL> DI float dppf(float x) { return __builtin_bit_cast(float, __builtin_amdgcn_mov_dpp(__builtin_bit_cast(int, x), CTRL, 0xf, 0xf, true)); }
DI float fold32(float a, float b) {
    auto r = __builtin_amdgcn_permlane32_swap(__float_as_uint(a), __float_as_uint(b), false, false);
    return __uint_as_float(r[0]) + __uint_as_float(r[1]);
}
DI float fold16(float a, float b) {
    auto r = __builtin_amdgcn_permlane16_swap(__float_as_uint(a), __float_as_uint(b), false, false);
    return __uint_as_float(r[0]) + __uint_as_float(r[1]);
}
DI float rowsum16(float v) { v += dppf<0xB1>(v); v += dppf<0x4E>(v); v += dppf<0x124>(v); v += dppf<0x128>(v); return v; }

DI void spg_role(int b, int head, int w, const char* __restrict__ wsp, const __hip_bfloat16* __restrict__ bsp, char* __restrict__ ws, unsigned lds0,
                 const char* __restrict__ x, const char* __restrict__ wo, const char* __restrict__ wout, char* __restrict__ out, bool seen) {
    const int lane = threadIdx.x & 63;
    const int xc = b & 7, rr = b >> 3;
    const int tt = xc * 4 + (rr & 3), ft = rr >> 2;
    const bool active = ft < 7;
    const unsigned wring = lds0 + K2_W + w * 8192;
    const __amdgpu_buffer_rsrc_t wr = __builtin_amdgcn_make_buffer_rsrc((void*)wsp, (short)0, S_ * S_ * 2, 0x00020000);
    unsigned wso = tt * 64 * 4096 + w * 1024;
    unsigned wvoff[2];
#pragma unroll
    for (int p = 0; p < 2; ++p) wvoff[p] = (lane >> 3) * 4096 + (((lane & 7) ^ ((4 * p + (lane >> 4)) & 7)) << 4);
#define ISSUEW(i) BLDS(wr, wvoff[(i) & 1], wso, (i) * 32768, wring, (i) * 1024)
    auto poll = [&](unsigned idx, unsigned cnt, int n) {
        const unsigned fo = idx * CNT_STRIDE;
        char* fl = ws + WS_FLAG;
#pragma unroll 1
        for (int it = 0; it < SPIN_MAX; ++it) {
            unsigned f = cnt;
            if (lane < n) { const unsigned z = 0; asm volatile("global_atomic_add %0, %1, %2, %3 sc0\n\ts_waitcnt vmcnt(0)" : "=&v"(f) : "v"(fo), "v"(z), "s"(fl) : "memory"); }
            if (__all(f == cnt)) break;
            if ((it & 63) == 63) add16(fl + CNT_HB * CNT_STRIDE, 8);
            __builtin_amdgcn_s_sleep(2);
        }
    };
    f32x16 acc[4][2];
    unsigned zvoff[4], waddr[4], lco, lc;
#define SETUP() do { _Pragma("unroll") for (int j = 0; j < 4; ++j) { \
            acc[j][0] = splat16(0.f); acc[j][1] = splat16(0.f); PIN(acc[j][0]); PIN(acc[j][1]); \
            zvoff[j] = (lane >> 5) * (FH * 16) + (j ^ w) * 512 + (lane & 31) * 16; PIN(zvoff[j]); \
            waddr[j] = wring + (lane & 31) * 128 + (((j * 2 + (lane >> 5)) ^ ((lane >> 1) & 7)) << 4); PIN(waddr[j]); } \
        lco = (lane ? CNT_Z1 + (tt >> 1) : ((b >> 3) & 15) * 16 + 8 + w) * CNT_STRIDE; lc = 0; PIN(lco); PIN(lc); \
        PIN(wvoff[0]); PIN(wvoff[1]); } while (0)
    if (w == 3) {
        const __amdgpu_buffer_rsrc_t kr = __builtin_amdgcn_make_buffer_rsrc((void*)(ws + WS_K), (short)0, 4u << 20, 0x00020000);
        const unsigned lo = lane * 16;
        unsigned kso = head * 65536 + 5 * 1024, kd = lds0 + 5 * 1024;
        if (!seen) { poll(CNT_QKV + (head >> 3) * 8 + ((b >> 3) & 7), 64, 1); lds_inc(lds0 + K2_MISC + SY_QKV); }
        SETUP();
#define KV4() do { BLDSC(kr, lo, kso, 0, kd, K2_K - 1024); BLDSC(kr, lo, kso, WS_V - WS_K, kd, K2_V); BLDSC(kr, lo, kso, 32768, kd, K2_K + 31744 - 1024); \
                   BLDSC(kr, lo, kso, WS_V - WS_K + 32768, kd, K2_V + 32768); kso += 1024; kd += 1024; } while (0)
#pragma unroll 1
        for (int t = 5; t < 20; ++t) KV4();
        WAITV(28);
        PRIO_HI(); lds_inc(lds0 + K2_MISC + SY_KVB); PRIO_LO();
        ISSUEW(0); ISSUEW(1); ISSUEW(2); ISSUEW(3); ISSUEW(4); ISSUEW(5); ISSUEW(6); ISSUEW(7);
#pragma unroll 1
        for (int t = 20; t < 32; ++t) { KV4(); WAITV(44); }
        PRIO_HI();
    } else {
        const int id1 = id1_of(b), g = grp_of(id1);
        const char* src = ws + WS_Z2 + (size_t)g * FH * 16;
        u32x4 zc[5], z0;
        const unsigned o0 = w * 1024 + lane * 16, o4 = o0 + (w < 2 ? 12288 : 9216);
        const unsigned zero = 0;
        if (w == 0) { if (!seen) { poll(CNT_Z2 + (g >> 4), 8, 1); lds_inc(lds0 + K2_MISC + SY_Z2); } } else lds_wait<1>(lds0 + K2_MISC + SY_Z2, 1);
        GLD16C(zc[0], o0, src, 0); GLD16C(zc[1], o0, src, 3072); GLD16C(zc[2], o0, src + 6144, 0); GLD16C(zc[3], o0, src + 6144, 3072); GLD16C(zc[4], o4, src, 0); GLD16C(z0, zero, src, 0);
        SETUP();
        ISSUEW(0); ISSUEW(1); ISSUEW(2); ISSUEW(3); ISSUEW(4); ISSUEW(5); ISSUEW(6); ISSUEW(7);
        asm volatile("s_waitcnt vmcnt(8)" : "+v"(zc[0]), "+v"(zc[1]), "+v"(zc[2]), "+v"(zc[3]), "+v"(zc[4]), "+v"(z0) :: "memory");
        PRIO_HI();
        char* dst = ws + WS_Z2N + (size_t)g * FH * 16;
        float sh[8], s1[8], s2[8];
#pragma unroll
        for (int e = 0; e < 4; ++e) { sh[2 * e] = bflo(z0[e]); sh[2 * e + 1] = bfhi(z0[e]); }
        const float m4 = w < 2 ? 1.f : 0.f;
#pragma unroll
        for (int e = 0; e < 8; ++e) {
            float v[5];
#pragma unroll
            for (int j = 0; j < 5; ++j) v[j] = ((e & 1) ? bfhi(zc[j][e >> 1]) : bflo(zc[j][e >> 1])) - sh[e];
            v[4] *= m4;
            s1[e] = ((v[0] + v[1]) + (v[2] + v[3])) + v[4];
            s2[e] = __builtin_fmaf(v[4], v[4], (v[0] * v[0] + v[1] * v[1]) + (v[2] * v[2] + v[3] * v[3]));
        }
        f32x4 q;
#pragma unroll
        for (int i = 0; i < 4; ++i) q[i] = rowsum16(fold16(fold32(s1[i], s2[i]), fold32(s1[i + 4], s2[i + 4])));
        LAS float* part = (LAS float*)(uintptr_t)(lds0 + K2_MISC + 256);
        *(LAS f32x4*)(part + w * 16 + (lane >> 4) * 4) = q;
        asm volatile("s_waitcnt lgkmcnt(0)" ::: "memory");
        lds_inc(lds0 + K2_MISC + SY_LN);
        lds_wait(lds0 + K2_MISC + SY_LN, 3);
        float rs[8], nm[8];
        {
            f32x4 t[4];
#pragma unroll
            for (int i = 0; i < 4; ++i) t[i] = (*(LAS f32x4*)(part + i * 4) + *(LAS f32x4*)(part + 16 + i * 4)) + *(LAS f32x4*)(part + 32 + i * 4);
#pragma unroll
            for (int e = 0; e < 8; ++e) {
                float mean = t[e >> 2][e & 3] * (1.0f / FH);
                float var = fmaxf(t[2 + (e >> 2)][e & 3] * (1.0f / FH) - mean * mean, 0.f);
                rs[e] = __builtin_amdgcn_rsqf(var + 1e-5f);
                nm[e] = -(mean + sh[e]) * rs[e];
            }
        }
#pragma unroll
        for (int j = 0; j < 5; ++j) {
            u32x4 v;
#pragma unroll
            for (int e = 0; e < 4; ++e) v[e] = pk2(__builtin_fmaf(bflo(zc[j][e]), rs[2 * e], nm[2 * e]), __builtin_fmaf(bfhi(zc[j][e]), rs[2 * e + 1], nm[2 * e + 1]));
            if (j < 4) GST16(o0, v, dst + j * 3072); else GST16(o4, v, dst);
        }
        WAITV(0);
        unsigned old = 0;
        if (lane == 0) old = __hip_atomic_fetch_add((LAS unsigned*)(uintptr_t)(lds0 + K2_MISC + SY_LNST), 1u, __ATOMIC_RELAXED, __HIP_MEMORY_SCOPE_WORKGROUP);
        old = __builtin_amdgcn_readfirstlane(old);
        if (old == 2) {
            add16(ws + WS_FLAG + (size_t)(id1 < 96 ? 8 + (id1 & 3) : id1 & 7) * CNT_STRIDE, 12);
            if (!active) { WAITV(0); lds_inc(lds0 + K2_MISC + SY_FLAG); }
        }
    }
    const unsigned l16 = lane * 16;
    auto reg = [&](int ww) { return lds0 + ((ww & 1) ? K2_V + (ww >> 1) * 32768 : K2_K + (ww >> 1) * 31744); };
    if (active) {
        const char* zb = ws + WS_Z2N + ((size_t)w * 64 * FH + ft * 128) * 16;
        u32x4 zf[4][4];
#define ZPTR(c, ks) (zb + (size_t)((c) * 4 + (ks)) * (2 * FH * 16))
        if (w == 0) { poll(((b >> 3) & 15) * 16 + lane, 20, 8); lds_inc(lds0 + K2_MISC + SY_FLAG); }
        else lds_wait<2>(lds0 + K2_MISC + SY_FLAG, 1);
        PRIO_LO();
#pragma unroll
        for (int ks = 0; ks < 4; ++ks)
#pragma unroll
            for (int j = 0; j < 4; ++j) GLD16S(zf[ks][j], zvoff[j], ZPTR(0, ks));
        if (w == 3) { WAITV(16); PRIO_HI(); lds_inc(lds0 + K2_MISC + SY_KVB); PRIO_LO(); }
#define ZWAIT(n, ks) asm volatile("s_waitcnt vmcnt(" #n ")" : "+v"(zf[ks][0]), "+v"(zf[ks][1]), "+v"(zf[ks][2]), "+v"(zf[ks][3]) :: "memory")
#define KPAIR(ks, j_, MORE) do { \
        acc[j_][0] = MFMA32(__builtin_bit_cast(bf16x8, zf[ks][j_]), wf[0][ks], acc[j_][0]); PIN(acc[j_][0]); SB(); \
        if (MORE && ks == 0) ISSUEW(2 * j_); \
        acc[j_][1] = MFMA32(__builtin_bit_cast(bf16x8, zf[ks][j_]), wf[1][ks], acc[j_][1]); PIN(acc[j_][1]); SB(); \
        if (MORE && ks == 0) ISSUEW(2 * j_ + 1); \
        if (MORE) GLD16S(zf[ks][j_], zvoff[j_], ZPTR(c + 1, ks)); } while (0)
#define KSTEP(ks, MORE) do { KPAIR(ks, 0, MORE); KPAIR(ks, 1, MORE); KPAIR(ks, 2, MORE); KPAIR(ks, 3, MORE); } while (0)
#define WREAD() _Pragma("unroll") for (int ks = 0; ks < 4; ++ks) { wf[0][ks] = ldsfrag(waddr[ks]); wf[1][ks] = ldsfrag(waddr[ks] + 4096); } \
        asm volatile("s_waitcnt lgkmcnt(0)" : "+v"(wf[0][0]), "+v"(wf[0][1]), "+v"(wf[0][2]), "+v"(wf[0][3]), "+v"(wf[1][0]), "+v"(wf[1][1]), "+v"(wf[1][2]), "+v"(wf[1][3]) :: "memory")
#pragma unroll 1
        for (int c = 0; c < 7; ++c) {
            bf16x8 wf[2][4];
            wso += 128;
            if (c == 2) asm volatile("s_mov_b64 exec, 3\n\tglobal_atomic_add %0, %1, %0, %2 sc0\n\ts_mov_b64 exec, -1" : "+v"(lc) : "v"(lco), "s"(ws + WS_FLAG) : "memory");
            ZWAIT(12, 0);
            if (c == 4) {
                asm volatile("" : "+v"(lc));
                if (__builtin_amdgcn_readlane(lc, 0) != 24 || __builtin_amdgcn_readlane(lc, 1) != 8) poll(lco / CNT_STRIDE, lane ? 8 : 24, 2);
            }
            WREAD();
            KSTEP(0, true);
            ZWAIT(20, 1); KSTEP(1, true);
            ZWAIT(20, 2); KSTEP(2, true);
            ZWAIT(20, 3); KSTEP(3, true);
        }
        const int fs0 = ft * 8 + w * 2;
        const char* z1p = ws + WS_Z1 + ((size_t)tt * 2 * 56 + fs0) * 1024;
        const char* bp = (const char*)bsp + tt * 128;
        const unsigned bo = (lane & 31) * 2;
        unsigned braw[2];
        auto blk = [&](int dstw, int j) { return (j < 3 ? reg(dstw) + (j - 1) * 8192 : lds0 + K2_W + dstw * 8192) + l16; };
        {
            const int c = 7; (void)c;
            bf16x8 wf[2][4];
            ZWAIT(12, 0);
            WREAD();
            PRIO_HI();
            lds_inc(lds0 + K2_MISC + SY_MAIN);
            lds_wait(lds0 + K2_MISC + SY_P16, 4);
            unsigned wa[3] = {blk(1 ^ w, 1), blk(2 ^ w, 2), blk(3 ^ w, 3)};
            PIN(wa[0]); PIN(wa[1]); PIN(wa[2]); SB();
            PRIO_LO();
            KSTEP(0, false);
            GLD16C(zf[0][0], l16, z1p, 0); GLD16C(zf[0][1], l16, z1p, 1024); GLD16C(zf[0][2], l16, z1p + 56 * 1024, 0); GLD16C(zf[0][3], l16, z1p + 56 * 1024, 1024);
            asm volatile("global_load_ushort %0, %1, %2" : "=v"(braw[0]) : "v"(bo), "s"(bp) : "memory");
            asm volatile("global_load_ushort %0, %1, %2 offset:64" : "=v"(braw[1]) : "v"(bo), "s"(bp) : "memory");
            ZWAIT(14, 1); KSTEP(1, false);
            ZWAIT(10, 2); ZWAIT(6, 3);
            sfor<16>(LAMBDA(N) {
                constexpr int n = N, j = ((n >> 2) + 1) & 3, ks = 2 + ((n >> 1) & 1), t = n & 1, jw = n >> 2;
                if (n == 12) { PRIO_HI(); lds_wait(lds0 + K2_MISC + SY_MAIN, 4); PRIO_LO(); }
                acc[j][t] = MFMA32(__builtin_bit_cast(bf16x8, zf[ks][j]), wf[t][ks], acc[j][t]); PIN(acc[j][t]); SB();
                if constexpr (jw > 0) {
#pragma unroll
                    for (int u = 0; u < 2; ++u) {
                        constexpr int tw = (n >> 1) & 1; const int q = (n & 1) * 2 + u;
                        f32x4 v = {acc[jw][tw][4 * q], acc[jw][tw][4 * q + 1], acc[jw][tw][4 * q + 2], acc[jw][tw][4 * q + 3]};
                        *(LAS f32x4*)(uintptr_t)(wa[jw - 1] + (tw * 4 + q) * 1024) = v;
                    }
                    SB();
                }
            });
        }
        asm volatile("s_waitcnt lgkmcnt(0)" ::: "memory");
        PRIO_HI();
        lds_inc(lds0 + K2_MISC + SY_RED);
        lds_wait(lds0 + K2_MISC + SY_RED, 4);
#pragma unroll
        for (int j = 1; j < 4; ++j) {
            const unsigned a = blk(w, j);
#pragma unroll
            for (int t = 0; t < 2; ++t)
#pragma unroll
                for (int q = 0; q < 4; ++q) {
                    f32x4 v = *(LAS f32x4*)(uintptr_t)(a + (t * 4 + q) * 1024);
#pragma unroll
                    for (int e = 0; e < 4; ++e) acc[0][t][4 * q + e] += v[e];
                }
        }
        asm volatile("s_waitcnt vmcnt(0)" : "+v"(zf[0][0]), "+v"(zf[0][1]), "+v"(zf[0][2]), "+v"(zf[0][3]), "+v"(braw[0]), "+v"(braw[1]) :: "memory");
        const float bias[2] = {bflo(braw[0]), bflo(braw[1])};
#pragma unroll
        for (int t = 0; t < 2; ++t)
#pragma unroll
            for (int s = 0; s < 2; ++s) {
                auto a0 = __builtin_amdgcn_permlane32_swap(zf[0][2 * t + s][0], zf[0][2 * t + s][2], false, false);
                auto a1 = __builtin_amdgcn_permlane32_swap(zf[0][2 * t + s][1], zf[0][2 * t + s][3], false, false);
                const unsigned zc[4] = {a0[0], a1[0], a0[1], a1[1]};
                unsigned p[4];
#pragma unroll
                for (int e = 0; e < 4; ++e)
                    p[e] = pk2(bflo(zc[e]) * (acc[0][t][8 * s + 2 * e] + bias[t]), bfhi(zc[e]) * (acc[0][t][8 * s + 2 * e + 1] + bias[t]));
                auto s0 = __builtin_amdgcn_permlane32_swap(p[0], p[2], false, false);
                auto s1 = __builtin_amdgcn_permlane32_swap(p[1], p[3], false, false);
                u32x4 v = {s0[0], s1[0], s0[1], s1[1]};
                GST16(l16, v, ws + WS_CAT + ((size_t)(tt * 2 + t) * CAT_KS + 32 + fs0 + s) * 1024);
            }
        publish(ws + WS_FLAG + (size_t)(CNT_REC + tt * 8 + (ft * 4 + w) / 7) * CNT_STRIDE);
    } else {
        const int id = (rr & 3) * 4 + w;
        if (w == 3) { WAITV(0); lds_inc(lds0 + K2_MISC + SY_KVB); }
        lds_wait<4>(lds0 + K2_MISC + SY_KVB, 2);
        lds_wait<4>(lds0 + K2_MISC + SY_FLAG, 1);
        PRIO_LO();
#pragma unroll 1
        for (int c = 1; c < 8; ++c) { wso += 128; ISSUEW(0); ISSUEW(1); ISSUEW(2); ISSUEW(3); ISSUEW(4); ISSUEW(5); ISSUEW(6); ISSUEW(7); WAITV(32); }
        lds_wait<32>(lds0 + K2_MISC + SY_P16, 4);
#pragma unroll 1
        for (int i = 0; i < 56; ++i) { glds16(wout + (size_t)(id * 56 + i) * 1024, l16, wring); if ((i & 7) == 7) WAITV(32); }
#pragma unroll 1
        for (int i = 0; i < 32; ++i) { glds16(wo + (size_t)(id * 32 + i) * 1024, l16, wring); if ((i & 7) == 7) WAITV(32); }
#pragma unroll 1
        for (int i = 0; i < 16; ++i) glds16(x + (size_t)(xc * 256 + id * 16 + i) * 1024, l16, wring);
        WAITV(0);
        PRIO_HI();
    }
    {
        const __amdgpu_buffer_rsrc_t hr = __builtin_amdgcn_make_buffer_rsrc((void*)wout, (short)0, FH * 1024, 0x00020000);
        const __amdgpu_buffer_rsrc_t orr = __builtin_amdgcn_make_buffer_rsrc((void*)wo, (short)0, D_ * 1024, 0x00020000);
        unsigned bvoff = ((lane >> 2) & 7) * 1024 + (lane >> 5) * 64 + (lane & 3) * 16;
        const unsigned hso = ft * 128 + w * 14 * 16384, oso = ft * 128 + w * 8 * 16384;
        const unsigned rg = reg(w);
#define FB(rs, so, s, base, off) do { BLDS(rs, bvoff, so, (s) * 16384, base, off); BLDS(rs, bvoff, so, (s) * 16384 + 8192, base, (off) + 1024); } while (0)
        const unsigned bl = (lane >> 5) * 1024 + (w & 1) * 512 + ((lane & 15) >> 2) * 64 + ((lane >> 4) & 1) * 32 + (lane & 3) * 8;
        unsigned br0 = rg + bl, br1 = rg + (bl ^ 512), bw0 = wring + bl, bw1 = wring + (bl ^ 512);
        unsigned xo = (lane & 31) * 1024 + (lane >> 5) * 16;
        PIN(bvoff); PIN(br0); PIN(br1); PIN(bw0); PIN(bw1); PIN(xo); SB();
        PRIO_LO();
        FB(hr, hso, 0, rg, 0); FB(hr, hso, 1, rg, 2048); FB(hr, hso, 2, rg, 4096); FB(hr, hso, 3, rg, 6144);
        FB(hr, hso, 4, rg, 8192); FB(hr, hso, 5, rg, 10240); FB(hr, hso, 6, rg, 12288); FB(hr, hso, 7, rg, 14336);
        FB(hr, hso, 8, wring, 0); FB(hr, hso, 9, wring, 2048); FB(hr, hso, 10, wring, 4096); FB(hr, hso, 11, wring, 6144);
        const size_t oo_ = (size_t)(tt * 64 + (w >> 1) * 32) * 1024 + (ft * 64 + (w & 1) * 32) * 2;
        u32x4 xv[2];
        GLD16(xv[0], xo, x + oo_, 0); GLD16(xv[1], xo, x + oo_, 32);
        PRIO_HI();
        lds_wait<8>(lds0 + K2_MISC + SY_P26, 4);
        FB(hr, hso, 12, rg, 16384); FB(hr, hso, 13, rg, 18432);
        const char* a0p = ws + WS_CAT + (size_t)(tt * 2 + (w >> 1)) * CAT_KS * 1024;
        const char* a1p = ws + WS_CAT + (size_t)(tt * 2 + ((w >> 1) ^ 1)) * CAT_KS * 1024;
        u32x4 af[14][2];
        f32x16 fc[2][2];
        bf16x8 bq[2][2];
#define SLOT0(s) ((s) < 8 ? br0 + (s) * 2048 : (s) < 12 ? bw0 + ((s) - 8) * 2048 : br0 + 16384 + ((s) - 12) * 2048)
#define SLOT1(s) ((s) < 8 ? br1 + (s) * 2048 : (s) < 12 ? bw1 + ((s) - 8) * 2048 : br1 + 16384 + ((s) - 12) * 2048)
#define BQ(s) do { bq[(s) & 1][0] = trfrag(SLOT0(s), SLOT0(s) + 256); bq[(s) & 1][1] = trfrag(SLOT1(s), SLOT1(s) + 256); } while (0)
#define AWAIT(n, s) asm volatile("s_waitcnt vmcnt(%c2)" : "+v"(af[s][0]), "+v"(af[s][1]) : "i"(n) : "memory")
#define FMM(s, FIRST) do { _Pragma("unroll") for (int i_ = 0; i_ < 2; ++i_) _Pragma("unroll") for (int j_ = 0; j_ < 2; ++j_) { \
            fc[i_][j_] = MFMA32(bq[(s) & 1][j_], __builtin_bit_cast(bf16x8, af[s][i_]), (FIRST) ? splat16(0.f) : fc[i_][j_]); PIN(fc[i_][j_]); } SB(); } while (0)
        poll(CNT_REC + tt * 8 + w, 7, 1);
        PRIO_LO();
#pragma unroll
        for (int s = 0; s < 14; ++s) { GLD16S(af[s][0], l16, a0p + (32 + w * 14 + s) * 1024); GLD16S(af[s][1], l16, a1p + (32 + w * 14 + s) * 1024); }
        BQ(0);
#define HST(s) do { if (s < 13) BQ(s + 1); AWAIT(2 * (13 - s) + (s > 11 ? 16 : 0), s); FMM(s, s == 0); } while (0)
        HST(0); HST(1); HST(2); HST(3); HST(4); HST(5); HST(6); HST(7); HST(8);
        HST(9); HST(10); HST(11);
        FB(orr, oso, 0, rg, 0); FB(orr, oso, 1, rg, 2048); FB(orr, oso, 2, rg, 4096); FB(orr, oso, 3, rg, 6144);
        FB(orr, oso, 4, rg, 8192); FB(orr, oso, 5, rg, 10240); FB(orr, oso, 6, rg, 12288); FB(orr, oso, 7, rg, 14336);
        HST(12); HST(13);
        PRIO_HI();
        poll(CNT_REC + tt * 8 + 4 + w, 8, 1);
#pragma unroll
        for (int s = 0; s < 8; ++s) { GLD16S(af[s][0], l16, a0p + (w * 8 + s) * 1024); GLD16S(af[s][1], l16, a1p + (w * 8 + s) * 1024); }
        BQ(0);
#define OST(s) do { if (s < 7) BQ(s + 1); AWAIT(2 * (7 - s), s); FMM(s, false); } while (0)
        OST(0); OST(1); OST(2); OST(3); OST(4); OST(5); OST(6); OST(7);
#pragma unroll
        for (int j = 1; j < 4; ++j)
#pragma unroll
            for (int q = 0; q < 4; ++q) {
                const f32x16& t = fc[j >> 1][j & 1];
                f32x4 v = {t[4 * q], t[4 * q + 1], t[4 * q + 2], t[4 * q + 3]};
                *(LAS f32x4*)(uintptr_t)(rg + (j * 4 + q) * 1024 + l16) = v;
            }
        asm volatile("s_waitcnt lgkmcnt(0)" ::: "memory");
        lds_inc(lds0 + K2_MISC + SY_FIN);
        lds_wait(lds0 + K2_MISC + SY_FIN, 4);
#pragma unroll
        for (int j = 1; j < 4; ++j)
#pragma unroll
            for (int q = 0; q < 4; ++q) {
                f32x4 v = *(LAS f32x4*)(uintptr_t)(reg(w ^ j) + (j * 4 + q) * 1024 + l16);
#pragma unroll
                for (int e = 0; e < 4; ++e) fc[0][0][4 * q + e] += v[e];
            }
        asm volatile("" : "+v"(xv[0]), "+v"(xv[1]));
#pragma unroll
        for (int s = 0; s < 2; ++s) {
            auto a0 = __builtin_amdgcn_permlane32_swap(xv[s][0], xv[s][2], false, false);
            auto a1 = __builtin_amdgcn_permlane32_swap(xv[s][1], xv[s][3], false, false);
            const unsigned xc4[4] = {a0[0], a1[0], a0[1], a1[1]};
            unsigned p[4];
#pragma unroll
            for (int e = 0; e < 4; ++e) p[e] = pk2(bflo(xc4[e]) + fc[0][0][8 * s + 2 * e], bfhi(xc4[e]) + fc[0][0][8 * s + 2 * e + 1]);
            auto s0 = __builtin_amdgcn_permlane32_swap(p[0], p[2], false, false);
            auto s1 = __builtin_amdgcn_permlane32_swap(p[1], p[3], false, false);
            u32x4 v = {s0[0], s1[0], s0[1], s1[1]};
            *(u32x4*)(out + oo_ + xo + s * 32) = v;
        }
    }
}

__global__ __launch_bounds__(512, 2) NOPK void k_block(const char* __restrict__ x, const char* __restrict__ wqkv, const char* __restrict__ wo, const char* __restrict__ win,
                                                       const char* __restrict__ wsp, const __hip_bfloat16* __restrict__ bsp, const char* __restrict__ wout,
                                                       char* __restrict__ ws, char* __restrict__ out) {
    extern __shared__ __attribute__((aligned(16))) char smem[];
    const unsigned lds0 = (unsigned)(uintptr_t)(LAS char*)smem;
    const bool seen = phase1(x, wqkv, win, ws, wsp, lds0);
    const int wid = __builtin_amdgcn_readfirstlane(threadIdx.x >> 6);
    const int b = blockIdx.x;
    const int head = (b & 7) * 4 + (b >> 6);
    if (wid < 4) att_role(head, (b >> 3) & 7, wid, ws, lds0);
    else spg_role(b, head, wid - 4, wsp, bsp, ws, lds0, x, wo, wout, out, seen);
}

extern "C" void kernel_launch(void* const* d_in, const int* in_sizes, int n_in, void* d_out, int out_size, void* d_ws, size_t ws_size,
                              hipStream_t stream) {
    (void)hipFuncSetAttribute((const void*)k_block, hipFuncAttributeMaxDynamicSharedMemorySize, K2_LDS);
    hipLaunchKernelGGL(k_block, dim3(256), dim3(512), K2_LDS, stream, (const char*)d_in[0], (const char*)d_in[1], (const char*)d_in[2], (const char*)d_in[4],
                       (const char*)d_in[5], (const __hip_bfloat16*)d_in[6], (const char*)d_in[7], (char*)d_ws, (char*)d_out);
}
```

```cpp
#include <hip/hip_runtime.h>
#include <hip/hip_bf16.h>
#include <stdint.h>
#include <utility>

typedef __attribute__((ext_vector_type(2))) __bf16 bf16x2;
typedef __attribute__((ext_vector_type(8))) __bf16 bf16x8;
typedef __attribute__((ext_vector_type(16))) __bf16 bf16x16;
typedef __attribute__((ext_vector_type(2))) float f32x2;
typedef __attribute__((ext_vector_type(4))) float f32x4;
typedef __attribute__((ext_vector_type(8))) float f32x8;
typedef __attribute__((ext_vector_type(16))) float f32x16;
typedef __attribute__((ext_vector_type(4))) unsigned u32x4;
typedef __attribute__((ext_vector_type(4))) short s16x4;
#define LAS __attribute__((address_space(3)))
#define DI __device__ __forceinline__
#if defined(__HIP_DEVICE_COMPILE__)
#define NOPK __attribute__((target("no-packed-fp32-ops")))
#else
#define NOPK
#endif
#define SB() __builtin_amdgcn_sched_barrier(0)
#define PIN(x) asm volatile("" : "+v"(x))
#define WAITV(n) asm volatile("s_waitcnt vmcnt(" #n ")" ::: "memory")
#define MFMA16(a, b, c) __builtin_amdgcn_mfma_f32_16x16x32_bf16(a, b, c, 0, 0, 0)
#define MFMA32(a, b, c) __builtin_amdgcn_mfma_f32_32x32x16_bf16(a, b, c, 0, 0, 0)

constexpr int S_ = 2048, D_ = 512, FH = 896;
constexpr size_t WS_Q = 0, WS_K = 2u << 20, WS_V = 4u << 20, WS_Z1 = 6u << 20, WS_Z2 = 10u << 20, WS_Z2N = 14u << 20,
                 WS_CAT = 18u << 20, WS_FLAG = 24u << 20;
constexpr int CNT_STRIDE = 256, CNT_REC = 256, CNT_QKV = 512, CNT_Z2 = 544, CNT_Z1 = 560, CNT_HB = 576;
__device__ __forceinline__ int id1_of(int b) {
    const int g = (b >> 2) & 1, p = b >> 6, c = p ? g * 3 + p - 1 : 6 + g;
    return ((b & 1) * 8 + c) * 16 + ((b >> 1) & 1) * 8 + ((b >> 3) & 7);
}
__device__ __forceinline__ int grp_of(int id1) { return (id1 & 3) * 64 + (id1 < 96 ? 40 + (id1 >> 2) : (id1 - 96) >> 2); }
constexpr int CAT_KS = 88;

#define GST16(voff, val, sbase) asm volatile("global_store_dwordx4 %0, %1, %2 sc1\n\ts_nop 1" :: "v"(voff), "v"(val), "s"(sbase) : "memory")

#define GLD16(dst, voff, sbase, IMM) asm volatile("global_load_dwordx4 %0, %1, %2 offset:" #IMM : "=v"(dst) : "v"(voff), "s"(sbase) : "memory")

DI void glds16(const void* sbase, unsigned voff, unsigned ldsdst) {
    unsigned keep;
    asm volatile("s_mov_b32 %0, m0\n\ts_mov_b32 m0, %3\n\ts_nop 0\n\tglobal_load_lds_dwordx4 %1, %2\n\ts_mov_b32 m0, %0"
                 : "=&s"(keep) : "v"(voff), "s"(sbase), "s"(ldsdst) : "memory");
}
DI unsigned pk2(float a, float b) { f32x2 v = {a, b}; return __builtin_bit_cast(unsigned, __builtin_convertvector(v, bf16x2)); }
DI float bflo(unsigned u) { return __uint_as_float(u << 16); }
DI float bfhi(unsigned u) { return __uint_as_float(u & 0xffff0000u); }
DI s16x4 trread(unsigned addr) { return __builtin_amdgcn_ds_read_tr16_b64_v4i16((LAS s16x4*)(uintptr_t)addr); }
DI bf16x8 trfrag(unsigned addr0, unsigned addr1) {
    s16x4 lo = trread(addr0), hi = trread(addr1);
    return __builtin_bit_cast(bf16x8, __builtin_shufflevector(lo, hi, 0, 1, 2, 3, 4, 5, 6, 7));
}
DI bf16x8 ldsfrag(unsigned addr) { return *(const LAS bf16x8*)(uintptr_t)addr; }
DI float gelu_t(float u) {
    const float k1 = -2.3022081985f;
    float w = u * __builtin_fmaf(u * u, k1 * 0.044715f, k1);
    float e = __builtin_amdgcn_exp2f(w);
    return u * __builtin_amdgcn_rcpf(1.0f + e);
}

constexpr int K2_K = 0, K2_V = 62 * 1024, K2_W = 126 * 1024, K2_ONES = K2_W + 32768, K2_MISC = K2_ONES + 1024, K2_LDS = K2_MISC + 1024;
enum { SY_KVA = 0, SY_KVB = 4, SY_P26 = 12, SY_LN = 16, SY_LNST = 20, SY_RED = 24, SY_FLAG = 28, SY_CMB = 36  , SY_P16 = 44, SY_MAIN = 48, SY_FIN = 52, SY_QKV = 56, SY_Z2 = 60, SY_HB = 128   };
constexpr int SPIN_MAX = 1 << 21, LSPIN_MAX = 1 << 24;

#define GLD16C(dst, voff, sbase, IMM) asm volatile("global_load_dwordx4 %0, %1, %2 offset:" #IMM " sc1" : "=v"(dst) : "v"(voff), "s"(sbase) : "memory")
DI void hb_sample(const char* p, unsigned ldsdst) {
    unsigned t;
    asm volatile("v_mov_b32 %0, 0\n\ts_mov_b32 m0, %2\n\ts_mov_b64 exec, 1\n\tglobal_load_lds_dword %0, %1 sc1\n\ts_mov_b64 exec, -1" : "=&v"(t) : "s"(p), "s"(ldsdst) : "memory");
}
DI void publish(char* p) {
    unsigned t0, t1;
    asm volatile("v_mov_b32 %0, 0\n\tv_mov_b32 %1, 1\n\ts_waitcnt vmcnt(0)\n\ts_mov_b64 exec, 1\n\tglobal_atomic_add %0, %1, %2\n\ts_mov_b64 exec, -1" : "=&v"(t0), "=&v"(t1) : "s"(p) : "memory");
}
DI void lds_inc(unsigned addr) {
    unsigned t0, t1;
    asm volatile("v_mov_b32 %0, %2\n\tv_mov_b32 %1, 1\n\ts_mov_b64 exec, 1\n\tds_add_u32 %0, %1\n\ts_mov_b64 exec, -1" : "=&v"(t0), "=&v"(t1) : "s"(addr) : "memory");
}
DI void addn(char* p, unsigned stride_shift, unsigned long long lanes) {
    unsigned t0, t1;
    asm volatile("v_mbcnt_lo_u32_b32 %0, -1, 0\n\tv_mov_b32 %1, 1\n\tv_lshlrev_b32 %0, %3, %0\n\ts_mov_b64 exec, %4\n\tglobal_atomic_add %0, %1, %2\n\ts_mov_b64 exec, -1"
                 : "=&v"(t0), "=&v"(t1) : "s"(p), "s"(stride_shift), "s"(lanes) : "memory");
}
DI void add16(char* p, unsigned stride_shift) { addn(p, stride_shift, 0xffff); }

constexpr int K1_MISC = 9 * 16384;
constexpr int K1_SUM = K1_MISC;
constexpr int K1_CS = K1_MISC + 1024;
constexpr int K1_PRIV = K1_MISC + 1024 + 2048;
constexpr int K1_JUNK = K1_PRIV + 8 * 256;
constexpr int K1_LDS = K1_JUNK + 1024;
static_assert(K1_LDS <= K2_ONES, "phase 2 words are initialised while phase 1 runs");
template <class F, int... I> DI void sfor_(F&& f, std::integer_sequence<int, I...>) { (f(std::integral_constant<int, I>{}), ...); }
template <int N, class F> DI void sfor(F&& f) { sfor_(f, std::make_integer_sequence<int, N>{}); }
#define LAMBDA(a) [&](auto a) __attribute__((always_inline))
DI unsigned word(bf16x8 v, int e) { const u32x4 u = __builtin_bit_cast(u32x4, v); return u[e]; }
DI f32x16 splat16(float v) { f32x16 r; for (int i = 0; i < 16; ++i) r[i] = v; return r; }

#define BLDS(rsrc, voff, soff, SIMM, m0b, MIMM) do { unsigned t_; \
    asm volatile("s_add_u32 m0, %4, %c6\n\ts_add_u32 %0, %3, %c5\n\tbuffer_load_dwordx4 %1, %2, %0 offen lds" \
                 : "=&s"(t_) : "v"(voff), "s"(rsrc), "s"(soff), "s"(m0b), "i"(SIMM), "i"(MIMM) : "scc", "memory"); } while (0)

#define BLDSC(rsrc, voff, soff, SIMM, m0b, MIMM) do { unsigned t_; \
    asm volatile("s_add_u32 m0, %4, %c6\n\ts_add_u32 %0, %3, %c5\n\tbuffer_load_dwordx4 %1, %2, %0 offen sc1 lds" \
                 : "=&s"(t_) : "v"(voff), "s"(rsrc), "s"(soff), "s"(m0b), "i"(SIMM), "i"(MIMM) : "scc", "memory"); } while (0)

DI void hb_check(const char* hbw, unsigned lds0) {
    const unsigned h0 = __builtin_amdgcn_readfirstlane(*(volatile LAS unsigned*)(uintptr_t)(lds0 + K2_MISC + SY_HB));
#pragma unroll 1
    for (int it = 0; it < SPIN_MAX && __builtin_amdgcn_readfirstlane(*(volatile LAS unsigned*)(uintptr_t)(lds0 + K2_MISC + SY_HB + 4)) == h0; ++it) {
        __builtin_amdgcn_s_sleep(8);
        hb_sample(hbw, lds0 + K2_MISC + SY_HB + 4);
        WAITV(0);
    }
}

template <bool ORI2>
DI bool k1_body(const char* __restrict__ x, const char* __restrict__ wqkv, const char* __restrict__ win, int c  , int rp,
                char* __restrict__ ws, unsigned lds0, const char* __restrict__ wsp) {
    constexpr int LDA = 3072, LDB = 3584, NA = ORI2 ? 1 : 2;
    const int tid = threadIdx.x, lane = tid & 63;
    const int wid = __builtin_amdgcn_readfirstlane(tid >> 6);
    const int wm = wid & 3, wn = wid >> 2;
    const int r = lane & 31, h = lane >> 5;
    const bool leader = blockIdx.x == 0 && wid == 1;
    char* hb = ws + WS_FLAG + CNT_HB * CNT_STRIDE;
    const char* hbw = hb + (blockIdx.x & 15) * CNT_STRIDE;
    const bool two = c < 6;
    const int zc = two ? c * 128 : 768 + (c - 6) * 64;
    const unsigned colA = ORI2 ? 2048 + c * 128 : c * 256, colB = ((ORI2 ? FH : 0) + zc) * 2;
    const __amdgpu_buffer_rsrc_t xr = __builtin_amdgcn_make_buffer_rsrc((void*)x, (short)0, S_ * D_ * 2, 0x00020000);
    const __amdgpu_buffer_rsrc_t ar = __builtin_amdgcn_make_buffer_rsrc((void*)wqkv, (short)0, D_ * LDA, 0x00020000);
    const __amdgpu_buffer_rsrc_t br = __builtin_amdgcn_make_buffer_rsrc((void*)win, (short)0, D_ * LDB, 0x00020000);
    const unsigned xso = rp * 131072 + wid * 16384, aso = colA + wid * 16 * LDA;
    unsigned bso = colB + wid * 16 * LDB;
    unsigned xvoff[2];
#pragma unroll
    for (int i = 0; i < 2; ++i) xvoff[i] = (i * 8 + (lane >> 3)) * 1024 + (((lane & 7) ^ ((4 * i + (lane >> 4)) & 7)) << 4);
    const unsigned avoff = ((lane >> 2) & 7) * LDA + (lane >> 5) * 64 + (lane & 3) * 16, bvoff = ((lane >> 2) & 7) * LDB + (lane >> 5) * 64 + (lane & 3) * 16;
    const unsigned sdst = lds0 + wid * 2048;
#define ISSX1(cc, slot, i) BLDS(xr, xvoff[i], xso, (cc) * 128, sdst, (slot) * 16384 + (i) * 1024)
#define ISSA1(pp, st, slot, i) BLDS(ar, avoff, aso, (pp) * 128 + ((st) * 128 + (i) * 8) * LDA, sdst, (slot) * 16384 + (i) * 1024)
#define ISSB1(st, base, off, i) BLDS(br, bvoff, bso, ((st) * 128 + (i) * 8) * LDB, base, (off) + (i) * 1024)
#define ISS11(st, slot, i) do { if (ORI2) ISSB1(st, sdst, (slot) * 16384, i); else ISSA1(1, st, slot, i); } while (0)
    ISSX1(0, 0, 0); ISSX1(0, 0, 1); ISSX1(1, 1, 0); ISSX1(1, 1, 1); ISSA1(0, 0, 2, 0); ISSA1(0, 0, 2, 1);
    ISSX1(2, 3, 0); ISSX1(2, 3, 1); ISSX1(3, 4, 0); ISSX1(3, 4, 1); ISSA1(0, 1, 5, 0); ISSA1(0, 1, 5, 1);
#define PERM(q) ((q) == 0 ? 0 : (q) == 1 ? 4 : (q) < 5 ? (q) - 1 : (q))
    unsigned xad[4], bad[4];
#pragma unroll
    for (int t = 0; t < 4; ++t) {
        xad[t] = lds0 + (wm * 32 + r) * 128 + (((2 * (t ^ wm) + h) ^ ((r >> 1) & 7)) << 4);
        bad[t] = lds0 + (2 * (t ^ wm) + h) * 1024 + wn * 512 + ((lane & 15) >> 2) * 64 + ((lane >> 4) & 1) * 32 + (lane & 3) * 8;
    }
    const unsigned selw = ((lane & 15) == ((lane >> 4) & 1)) ? 0x3f803f80u : 0u;
    u32x4 selu = {selw, selw, selw, selw};
    const bf16x8 sel = __builtin_bit_cast(bf16x8, selu);
    bf16x8 af[32];
    f32x16 accA, accB;
    float st0 = 0.f, st1 = 0.f;
    const unsigned one2 = 0x3f803f80u;
    f32x4 csum;
    const f32x4 z4 = {0, 0, 0, 0};
#define MM(bf, a, c) (ORI2 ? MFMA32(a, bf, c) : MFMA32(bf, a, c))
#define MM16(bf, a) asm volatile("v_mfma_f32_32x32x16_bf16 %0, %1, %2, %3" : "+v"(accB) : "v"(ORI2 ? (a) : (bf)), "v"(ORI2 ? (bf) : (a)), "v"(accA))
#define BFR(q, off) trfrag(bad[PERM(q) & 3] + (off) + (PERM(q) >> 2) * 8192, bad[PERM(q) & 3] + (off) + (PERM(q) >> 2) * 8192 + 256)
#define MAIN(i, bf) do { if ((i) == 0) accA = MM(bf, af[i], splat16(0.f)); else if ((i) < 16) accA = MM(bf, af[i], accA); else if ((i) == 16) MM16(bf, af[i]); else accB = MM(bf, af[i], accB); \
        if ((i) < 16) PIN(accA); else PIN(accB); \
        if ((i) == 18) asm volatile("" :: "v"(accA));        \
        if (((i) & 7) < 2) { csum = MFMA16(sel, bf, (i) == 0 ? z4 : csum); PIN(csum); } } while (0)
#define DA(k, l) "v_dot2c_f32_bf16 %0, %" #k ", %" #k "\n\tv_dot2c_f32_bf16 %1, %" #l ", %" #l "\n\t"
#define DB(k, l) "v_dot2c_f32_bf16 %0, %" #k ", %2\n\tv_dot2c_f32_bf16 %1, %" #l ", %2\n\t"
#define AW(i, e) "v"(word(af[i], e))
#define STATS4(i) asm volatile("s_cmp_lg_u32 %3, 0\n\ts_cbranch_scc1 1f\n\t" DA(4, 5) DA(6, 7) DA(8, 9) DA(10, 11) DA(12, 13) DA(14, 15) DA(16, 17) DA(18, 19) "s_branch 2f\n1:\n\t" \
            DB(4, 5) DB(6, 7) DB(8, 9) DB(10, 11) DB(12, 13) DB(14, 15) DB(16, 17) DB(18, 19) "2:" : "+v"(st0), "+v"(st1) : "v"(one2), "s"(wn), \
            AW(i, 0), AW(i, 1), AW(i, 2), AW(i, 3), AW(i + 1, 0), AW(i + 1, 1), AW(i + 1, 2), AW(i + 1, 3), AW(i + 2, 0), AW(i + 2, 1), AW(i + 2, 2), AW(i + 2, 3), \
            AW(i + 3, 0), AW(i + 3, 1), AW(i + 3, 2), AW(i + 3, 3) : "scc")
#define BAR(n) asm volatile("s_waitcnt vmcnt(" #n ") lgkmcnt(0)\n\ts_barrier" ::: "memory")
    asm volatile("" : "=v"(accB));
    const unsigned csw = lds0 + K1_CS + wn * 512 + (lane & 15) * 16 + wm * 4;
    const unsigned csr = lds0 + K1_CS + wn * 512 + r * 16;
    const unsigned priv = lds0 + K1_PRIV + wid * 256;
#define CSWRITE(par) do { if (lane < 16) { *(LAS float*)(uintptr_t)(csw + (par)) = csum[0]; *(LAS float*)(uintptr_t)(csw + (par) + 256) = csum[1]; } } while (0)
    bf16x8 bq[4];
    {
        const unsigned prow = (rp & 7) * 64 + wid * 8, pa = colA + prow * LDA, pb = colB + prow * LDB, junk = lds0 + K1_JUNK;
#pragma unroll
        for (int t = 0; t < 4; ++t) bad[t] += 2 * 16384;
        sfor<35>(LAMBDA(U) {
            constexpr int u = U, i = u - 3, st = u >> 3, q = u & 7, ip = i < 0 ? 0 : i, s4 = st & 3;
            if (q == 0) {
                if (st == 4) {
                    const float t = st0 + st1;
                    auto rr = __builtin_amdgcn_permlane32_swap(__float_as_uint(t), __float_as_uint(t), false, false);
                    *(LAS float*)(uintptr_t)(lds0 + K1_SUM + wn * 512 + (wm * 32 + r) * 4) = __uint_as_float(rr[0]) + __uint_as_float(rr[1]);
                    CSWRITE(0);
                }
                if (st == 0) BAR(6); else if (st < 3) BAR(7); else if (st == 3) BAR(6); else BAR(3);
                if (st > 0) {
#pragma unroll
                    for (int t = 0; t < 4; ++t) { if (st < 4) xad[t] += st == 3 ? -6 * 16384 : 3 * 16384; bad[t] += st == 3 ? -6 * 16384 : st == 4 ? 16384 : 3 * 16384; }
                }
                if (st == 4 && leader) { WAITV(0); add16(hb, 8); }
            }
            if constexpr (st < 4 && q < 4) { af[st * 8 + 2 * q] = ldsfrag(xad[PERM(2 * q) & 3] + (PERM(2 * q) >> 2) * 16384); af[st * 8 + 2 * q + 1] = ldsfrag(xad[PERM(2 * q + 1) & 3] + (PERM(2 * q + 1) >> 2) * 16384); }
            bq[u & 3] = BFR(q, 0);
            if (i >= 0) MAIN(ip, bq[ip & 3]);
            if (st == 0) {
                if (q < 2) ISSX1(4, 6, q & 1); else if (q < 4) ISSX1(5, 7, q & 1); else if (q < 6) ISSA1(0, 2, 8, q & 1);
                else if (q == 6) { if (ORI2) BLDS(br, bvoff, pb, 0, junk, 0); else BLDS(ar, avoff, pa, 128, junk, 0); }
            }
            if (st == 1) { if (q < 2) ISSX1(6, 0, q & 1); else if (q < 4) ISSX1(7, 1, q & 1); else if (q < 6) ISSA1(0, 3, 2, q & 1); }
            if (st == 2 && q < 6) ISS11(q >> 1, 3 + (q >> 1), q & 1);
            if (st == 3) {
                if (q < 2) ISS11(3, 6, q & 1);
                if (q == 2) BLDS(br, bvoff, pb, ORI2 ? 128 : 0, junk, 0);
                if (q == 3) BLDS(br, bvoff, pb, 128, junk, 0);
                if (q == 4) {
                    const __amdgpu_buffer_rsrc_t sr = __builtin_amdgcn_make_buffer_rsrc((void*)wsp, (short)0, S_ * S_ * 2, 0x00020000);
                    const unsigned so = ((blockIdx.x & 7) * 32 + (blockIdx.x >> 3)) * 32768 + wm * 1024 + wn * 128, so_v = (lane >> 3) * 4096 + (lane & 7) * 16;
                    BLDS(sr, so_v, so, 0, junk, 0);
                }
            }
            if constexpr (st < 4 && (q == 4 || q == 6)) STATS4(s4 * 8 + (q - 4) * 2);
            SB();
        });
    }
    float rs, nm, rsz, nmz;
    {
        const float ss = *(LAS float*)(uintptr_t)(lds0 + K1_SUM + (wm * 32 + r) * 4), sm = *(LAS float*)(uintptr_t)(lds0 + K1_SUM + 512 + (wm * 32 + r) * 4);
        const float mean = sm * (1.0f / 512.0f);
        const float var = fmaxf(ss * (1.0f / 512.0f) - mean * mean, 0.0f);
        rsz = __builtin_amdgcn_rsqf(var + 1e-5f);
        nmz = -mean * rsz;
        rs = rsz * (c < 4 ? 0.25f * 1.4426950408889634f : 1.0f);
        nm = -mean * rs;
        if (ORI2) *(LAS float*)(uintptr_t)(priv + h * 128 + r * 4) = h ? nmz : rsz;
    }
    const int rt = rp * 4 + wm;
    char* dst = ws + (ORI2 ? WS_V + ((size_t)(c * 4 + wn * 2) * 64 + rt) * 1024 : WS_Q + ((size_t)(c * 8 + wn * 2) * 64 + rt) * 1024);
    unsigned dvoff = ORI2 ? ((lane >> 4) & 1) * 65536 + (lane >> 5) * 256 + (lane & 15) * 16 : lane * 16;
    f32x4 ea[2], eb[2], c4; float cs; unsigned pk[4];
#define EPI_BEGIN0(par) c4 = *(LAS f32x4*)(uintptr_t)(csr + (par))
#define EPI_BEGIN1() do { cs = (c4[0] + c4[1]) + (c4[2] + c4[3]); PIN(cs); if (!ORI2) *(LAS float*)(uintptr_t)(priv + r * 4) = cs; } while (0)
#define EPI_LOAD(q) do { ea[(q) & 1] = *(LAS f32x4*)(uintptr_t)(priv + (q) * 32 + h * 16); if (ORI2) eb[(q) & 1] = *(LAS f32x4*)(uintptr_t)(priv + 128 + (q) * 32 + h * 16); } while (0)
#define EPI_Y(V, e) (ORI2 ? __builtin_fmaf(eb[((e) >> 2) & 1][(e) & 3], cs, (V) * ea[((e) >> 2) & 1][(e) & 3]) : __builtin_fmaf(GELU ? nmz : nm, ea[((e) >> 2) & 1][(e) & 3], (V) * (GELU ? rsz : rs)))
#define EPI_PAIR(V0, V1, e) do { float y0_ = EPI_Y(V0, e), y1_ = EPI_Y(V1, (e) + 1); if (GELU) { y0_ = gelu_t(y0_); y1_ = gelu_t(y1_); } \
        pk[((e) >> 1) & 3] = pk2(y0_, y1_); PIN(pk[((e) >> 1) & 3]); } while (0)
#define EPI_STORE(j) do { auto s0_ = __builtin_amdgcn_permlane32_swap(pk[0], pk[2], false, false); auto s1_ = __builtin_amdgcn_permlane32_swap(pk[1], pk[3], false, false); \
        u32x4 v_ = {s0_[0], s1_[0], s0_[1], s1_[1]}; GST16(dvoff, v_, dst + (j) * (GELU ? (ORI2 ? 2 * FH * 16 : 1024) : (ORI2 ? 512 : 65536))); } while (0)
    char* cntA = ws + WS_FLAG + (size_t)(CNT_QKV + (ORI2 ? c >> 1 : c & 3) * 8) * CNT_STRIDE;
#define PUBLISH_A() do { WAITV(0); if (wid == 0) hb_check(hbw, lds0); asm volatile("s_barrier" ::: "memory"); if (wid == 7) addn(cntA, 8, 0xff); } while (0)
    EPI_BEGIN0(0);
    unsigned par = 0, rslot = 3, wslot = 7;
    auto pass = [&](auto G, bool more, bool lastA) __attribute__((always_inline)) {
        constexpr bool GELU = G;
        unsigned wd[4];
#pragma unroll
        for (int s = 0; s < 4; ++s) { wd[s] = sdst + wslot * 16384; wslot = wslot == 8 ? 0 : wslot + 1; }
        float hi[8];
        SB();
        sfor<32>(LAMBDA(I) {
            constexpr int i = I;
            if (GELU && i == 6) PUBLISH_A();
            if (i == 29) {
                par ^= 1024;
                if (more) WAITV(2);
                asm volatile("s_waitcnt lgkmcnt(0)\n\ts_barrier" ::: "memory");
                EPI_BEGIN0(par);
                if (leader) add16(hb, 8);
                if (!GELU && wid == 0 && lastA) hb_sample(hbw, lds0 + K2_MISC + SY_HB + 4);
            }
            if (((i + 3) & 7) == 0) {
                const unsigned d = rslot == 8 ? (unsigned)(-8 * 16384) : 16384u;
                rslot = rslot == 8 ? 0 : rslot + 1;
#pragma unroll
                for (int t = 0; t < 4; ++t) bad[t] += d;
            }
            bq[(i + 3) & 3] = BFR((i + 3) & 7, 0);
            if constexpr (i >= 4 && i < 12) asm volatile("v_mov_b32 %0, %1" : "=&v"(hi[i - 4]) : "v"(accB[8 + i - 4]));
            if (i == 0) EPI_BEGIN1();
            if (i == 1 || i == 7 || i == 13 || i == 19) EPI_LOAD((i - 1) / 6);
            if constexpr (i >= 5 && i < 28 && (i - 5) % 3 == 0) { constexpr int e = ((i - 5) / 3) * 2; EPI_PAIR(e < 8 ? accB[e & 7] : hi[e & 7], e < 8 ? accB[(e + 1) & 7] : hi[(e + 1) & 7], e); }
            MAIN(i, bq[i & 3]);
            if constexpr (!GELU && i < 16 && !(i & 1)) ISSB1(i >> 2, wd[i >> 2], 0, (i >> 1) & 1);
            if (i == 15 || i == 27) EPI_STORE(i == 27);
            if (i == 27) CSWRITE(par ^ 1024);
            SB();
        });
    };
#pragma unroll 1
    for (int p = 1; p <= NA; ++p) {
        const bool lastA = p == NA, more = !lastA || two;
        if (lastA) bso = two ? bso + 128 : 0x40000000u;
        pass(std::false_type{}, more, lastA);
        dst += 4 * 65536;
    }
    dst = ws + (ORI2 ? WS_Z2 + ((size_t)rt * 4 * FH + zc + wn * 32) * 16 : WS_Z1 + ((size_t)rt * 56 + (zc >> 4) + wn * 2) * 1024);
    if (ORI2) dvoff = (lane >> 5) * (FH * 16) + (lane & 31) * 16;
    if (two) { pass(std::true_type{}, false, false); dst += ORI2 ? 1024 : 4096; }
    unsigned look = 0;
    const bool looks = two && (wid == 4 || wid == 7);
    if (looks) {
        const int bb = blockIdx.x, head = (bb & 7) * 4 + (bb >> 6);
        char* lp = ws + WS_FLAG + (size_t)(wid == 4 ? CNT_Z2 + (grp_of(id1_of(bb)) >> 4) : CNT_QKV + (head >> 3) * 8 + ((bb >> 3) & 7)) * CNT_STRIDE;
        asm volatile("s_mov_b64 exec, 1\n\tglobal_atomic_add %0, %0, %0, %1 sc0\n\ts_mov_b64 exec, -1" : "+v"(look) : "s"(lp) : "memory");
    }
    {
        constexpr bool GELU = true;
        EPI_BEGIN1();
        EPI_LOAD(0);
#pragma unroll
        for (int q = 0; q < 4; ++q) {
            if (q < 3) EPI_LOAD(q + 1);
#pragma unroll
            for (int e = 0; e < 4; e += 2) EPI_PAIR(accB[q * 4 + e], accB[q * 4 + e + 1], q * 4 + e);
            if (q == 1 && !two) PUBLISH_A();
            if (q & 1) EPI_STORE(q >> 1);
        }
    }
    asm volatile("s_waitcnt vmcnt(0)" : "+v"(look) :: "memory");
    const bool seen = looks && __builtin_amdgcn_readfirstlane(look) == (wid == 4 ? 8u : 64u);
    if (seen) lds_inc(lds0 + K2_MISC + (wid == 4 ? SY_Z2 : SY_QKV));
    asm volatile("s_waitcnt lgkmcnt(0)\n\ts_barrier" ::: "memory");
    if (wid == 7) addn(ws + WS_FLAG + (size_t)((ORI2 ? CNT_Z2 : CNT_Z1) + rp) * CNT_STRIDE, 8, 1);
    return seen;
}

DI bool phase1(const char* __restrict__ x, const char* __restrict__ wqkv, const char* __restrict__ win, char* __restrict__ ws, const char* __restrict__ wsp, unsigned lds0) {
    const int b = blockIdx.x, rp = id1_of(b) & 15, ct = id1_of(b) >> 4;
    const int lane = threadIdx.x & 63, wid = __builtin_amdgcn_readfirstlane(threadIdx.x >> 6);
    if (wid == 0) hb_sample(ws + WS_FLAG + (CNT_HB + (b & 15)) * CNT_STRIDE, lds0 + K2_MISC + SY_HB);
    if (b == 0 && wid == 1) {
#pragma unroll
        for (int i = 0; i < 9; ++i) __hip_atomic_store((unsigned*)(ws + WS_FLAG + (size_t)(i * 64 + lane) * CNT_STRIDE), 0u, __ATOMIC_RELAXED, __HIP_MEMORY_SCOPE_AGENT);
    }
    if (threadIdx.x < 256) *(LAS unsigned*)(uintptr_t)(lds0 + K2_ONES + threadIdx.x * 4) = ((threadIdx.x >> 2) & 11) == 0 ? 0x3f803f80u : 0u;
    else if (threadIdx.x < 256 + 32) *(LAS unsigned*)(uintptr_t)(lds0 + K2_MISC + (threadIdx.x - 256) * 4) = 0u;
    if (__builtin_expect(ct < 8, 1)) return k1_body<false>(x, wqkv, win, ct, rp, ws, lds0, wsp);
    return k1_body<true>(x, wqkv, win, ct - 8, rp, ws, lds0, wsp);
}

constexpr float REF_OFF = 96.0f;

template <int SLEEP = 0> DI void lds_wait(unsigned addr, unsigned n) {
    unsigned t0, t1, c, v;
    if (SLEEP == 0)
        asm volatile("v_mov_b32 %0, %4\n\ts_mov_b32 %2, %6\n1:\n\tds_read_b32 %1, %0\n\ts_sub_u32 %2, %2, 1\n\ts_waitcnt lgkmcnt(0)\n\tv_readfirstlane_b32 %3, %1\n\ts_nop 0\n\t"
                     "s_cselect_b32 %3, %5, %3\n\ts_cmp_lt_u32 %3, %5\n\ts_cbranch_scc1 1b"
                     : "=&v"(t0), "=&v"(t1), "=&s"(c), "=&s"(v) : "s"(addr), "s"(n), "i"(LSPIN_MAX) : "scc", "memory");
    else
        asm volatile("v_mov_b32 %0, %4\n\ts_mov_b32 %2, %6\n\ts_branch 2f\n1:\n\ts_sleep %c7\n2:\n\tds_read_b32 %1, %0\n\ts_sub_u32 %2, %2, 1\n\ts_waitcnt lgkmcnt(0)\n\tv_readfirstlane_b32 %3, %1\n\ts_nop 0\n\t"
                     "s_cselect_b32 %3, %5, %3\n\ts_cmp_lt_u32 %3, %5\n\ts_cbranch_scc1 1b"
                     : "=&v"(t0), "=&v"(t1), "=&s"(c), "=&s"(v) : "s"(addr), "s"(n), "i"(LSPIN_MAX), "i"(SLEEP) : "scc", "memory");
}
DI bf16x16 lo8(const f32x16& x) { f32x8 t = __builtin_shufflevector(x, x, 0, 1, 2, 3, 4, 5, 6, 7); return __builtin_bit_cast(bf16x16, t); }
DI bf16x16 hi8(const f32x16& x) { f32x8 t = __builtin_shufflevector(x, x, 8, 9, 10, 11, 12, 13, 14, 15); return __builtin_bit_cast(bf16x16, t); }
#define PRIO_HI() __builtin_amdgcn_s_setprio(2)
#define PRIO_LO() __builtin_amdgcn_s_setprio(0)
#define SMF(a, b, c) __builtin_amdgcn_smfmac_f32_32x32x32_bf16(a, b, c, sidx, 0, 0)
#define GLD16S(dst, voff, sbase) GLD16C(dst, voff, sbase, 0)

DI float max16(const f32x16& s) {
    float a = fmaxf(fmaxf(s[0], s[1]), s[2]), b = fmaxf(fmaxf(s[3], s[4]), s[5]);
    a = fmaxf(fmaxf(a, s[6]), s[7]); b = fmaxf(fmaxf(b, s[8]), s[9]);
    a = fmaxf(fmaxf(a, s[10]), s[11]); b = fmaxf(fmaxf(b, s[12]), s[13]);
    a = fmaxf(fmaxf(a, s[14]), s[15]);
    return fmaxf(a, b);
}
DI float xhalf_max(float m) {
    auto rr = __builtin_amdgcn_permlane32_swap(__float_as_uint(m), __float_as_uint(m), false, false);
    return fmaxf(__uint_as_float(rr[0]), __uint_as_float(rr[1]));
}

#define LD4C(d, p, voff, o0, o1, o2, o3) asm volatile("global_load_dwordx4 %0, %4, %5 offset:" #o0 " sc1\n\tglobal_load_dwordx4 %1, %4, %5 offset:" #o1 " sc1\n\t" \
        "global_load_dwordx4 %2, %4, %5 offset:" #o2 " sc1\n\tglobal_load_dwordx4 %3, %4, %5 offset:" #o3 " sc1\n\ts_waitcnt vmcnt(0)" \
        : "=&v"(d[0]), "=&v"(d[1]), "=&v"(d[2]), "=&v"(d[3]) : "v"(voff), "s"(p) : "memory")
DI void att_exact(const char* kg, const char* vg, bf16x8 q0, bf16x8 q1, f32x16& o0, f32x16& o1) {
    const unsigned lane = threadIdx.x & 63, l16 = lane * 16, vo = (lane >> 5) * 256 + (lane & 15) * 16;
    const int sidx = (int)0xDDDDDDDDu;
    const f32x16 z = splat16(0.f);
    float m0 = -INFINITY, m1 = -INFINITY;
#pragma unroll 1
    for (int t = 0; t < 64; t += 4) {
        bf16x8 kf[4];
        LD4C(kf, kg + t * 1024, l16, 0, 1024, 2048, 3072);
#pragma unroll
        for (int i = 0; i < 4; ++i) { m0 = fmaxf(m0, max16(MFMA32(kf[i], q0, z))); m1 = fmaxf(m1, max16(MFMA32(kf[i], q1, z))); }
    }
    const f32x16 c0 = splat16(-xhalf_max(m0)), c1 = splat16(-xhalf_max(m1));
    o0 = z; o1 = z;
    const u32x4 onesu = {0x3f803f80u, 0x3f803f80u, 0x3f803f80u, 0x3f803f80u};
#pragma unroll 1
    for (int t = 0; t < 64; t += 2) {
        bf16x8 kf[4]; u32x4 vf[4];
        LD4C(vf, vg + t * 1024, vo, 0, 512, 1024, 1536);
        LD4C(kf, kg + t * 1024, l16, 0, 1024, 0, 1024);
#pragma unroll
        for (int i = 0; i < 2; ++i) {
            const bf16x8 va = __builtin_bit_cast(bf16x8, (lane & 16) ? onesu : vf[2 * i]), vb = __builtin_bit_cast(bf16x8, (lane & 16) ? onesu : vf[2 * i + 1]);
            f32x16 s0 = MFMA32(kf[i], q0, c0), s1 = MFMA32(kf[i], q1, c1);
#pragma unroll
            for (int e = 0; e < 16; ++e) { s0[e] = __builtin_amdgcn_exp2f(s0[e]); s1[e] = __builtin_amdgcn_exp2f(s1[e]); }
            o0 = SMF(va, lo8(s0), o0); o0 = SMF(vb, hi8(s0), o0);
            o1 = SMF(va, lo8(s1), o1); o1 = SMF(vb, hi8(s1), o1);
        }
    }
}

DI void att_store(const f32x16& o, char* dst, unsigned l16) {
    const float rl = __builtin_amdgcn_rcpf(o[8]);
    unsigned p0 = pk2(o[0] * rl, o[1] * rl), p1 = pk2(o[2] * rl, o[3] * rl), p2 = pk2(o[4] * rl, o[5] * rl), p3 = pk2(o[6] * rl, o[7] * rl);
    auto s0 = __builtin_amdgcn_permlane32_swap(p0, p2, false, false);
    auto s1 = __builtin_amdgcn_permlane32_swap(p1, p3, false, false);
    u32x4 v = {s0[0], s1[0], s0[1], s1[1]};
    GST16(l16, v, dst);
}
DI float att_mag(const f32x16& o) {
    return ((fabsf(o[0]) + fabsf(o[1])) + (fabsf(o[2]) + fabsf(o[3]))) + ((fabsf(o[4]) + fabsf(o[5])) + (fabsf(o[6]) + fabsf(o[7]))) + o[8];
}

DI void att_role(int head, int qb, int w, char* __restrict__ ws, unsigned lds0) {
    const int lane = threadIdx.x & 63;
    const int qh = w & 1, kh = w >> 1;
    const char* kg = ws + WS_K + (size_t)head * 65536;
    const char* vg = ws + WS_V + (size_t)head * 65536;
    const char* qg = ws + WS_Q + (size_t)head * 65536 + (size_t)(qb * 8 + qh * 4) * 1024;
    const unsigned l16 = lane * 16;
    const unsigned vo = (lane >> 5) * 256 + (lane & 15) * 16;
    u32x4 qr[4], k0, ks;
    const unsigned qo = l16 + kh * 2048, qo2 = l16 + (kh ^ 1) * 2048;
    int sidx = (int)0xDDDDDDDDu; PIN(sidx);
    f32x16 oo[4] = {splat16(0.f), splat16(0.f), splat16(0.f), splat16(0.f)}, ss[4];
    ss[3] = splat16(0.f);
    u32x4 v0 = {0, 0, 0, 0}, v1 = {0, 0, 0, 0};
    asm volatile("" : "+{v[0:15]}"(oo[0]), "+{v[16:31]}"(oo[1]), "+{v[32:47]}"(oo[2]), "+{v[48:63]}"(oo[3]), "=&{v[144:159]}"(ss[1]), "=&{v[160:175]}"(ss[2]), "+{v[176:191]}"(ss[3]),
                      "+{v[212:215]}"(v0), "+{v[216:219]}"(v1));
    unsigned kaddr = lds0 + K2_K + kh * 31744 + l16;
    unsigned vaddr = (lane & 16) ? (lds0 + K2_ONES + vo) : (lds0 + K2_V + kh * 32768 + vo);
    unsigned vinc = (lane & 16) ? 0u : 1024u;
    u32x4 e0 = {0x3f80u, 0u, 0u, 0u};
    const unsigned cb = lds0 + (qh ? K2_V : K2_K) + 20 * 1024 + l16, cst = qh ? 32768 : 31744;
    unsigned cwa = cb + kh * cst, cra = cb + (kh ^ 1) * cst;
    char* dst = ws + WS_CAT + ((size_t)(qb * 8 + qh * 4 + kh * 2) * CAT_KS + head) * 1024;
    char* cnt = ws + WS_FLAG + (size_t)(CNT_REC + (qb * 4 + qh * 2 + kh) * 8 + 4 + (head >> 3)) * CNT_STRIDE;
    PIN(kaddr); PIN(vaddr); PIN(vinc); PIN(e0); PIN(cwa); PIN(cra);
    asm volatile("" : "+s"(dst), "+s"(cnt));
    lds_wait<1>(lds0 + K2_MISC + SY_QKV, 1);
    GLD16C(ks, l16, kg, 0);
    GLD16C(qr[0], qo, qg, 0); GLD16C(qr[1], qo, qg, 1024); GLD16C(qr[2], qo2, qg, 0); GLD16C(qr[3], qo2, qg, 1024);
    GLD16C(k0, l16, kg + kh * 32768, 0);
    {
        const __amdgpu_buffer_rsrc_t kr = __builtin_amdgcn_make_buffer_rsrc((void*)(ws + WS_K), (short)0, 4u << 20, 0x00020000);
        const unsigned so = head * 65536 + (qh ? (unsigned)(WS_V - WS_K) : 1024u) + kh * 32768, d = lds0 + (qh ? K2_V + kh * 32768 : K2_K + kh * 31744);
        BLDSC(kr, l16, so, 0, d, 0); BLDSC(kr, l16, so, 1024, d, 1024); BLDSC(kr, l16, so, 2048, d, 2048); BLDSC(kr, l16, so, 3072, d, 3072); BLDSC(kr, l16, so, 4096, d, 4096);
    }
    asm volatile("s_waitcnt vmcnt(5)" : "+v"(qr[0]), "+v"(qr[1]), "+v"(qr[2]), "+v"(qr[3]), "+v"(k0), "+v"(ks) :: "memory");
#define QF(i) __builtin_bit_cast(bf16x8, qr[i])
    f32x16 cc[4];
#pragma unroll
    for (int i = 0; i < 4; ++i) { cc[i] = MFMA32(__builtin_bit_cast(bf16x8, ks), QF(i), splat16(0.f)); PIN(cc[i]); SB(); }
#pragma unroll
    for (int i = 0; i < 4; ++i) {
        const float m = __builtin_fmaf(max16(cc[i]), -0.5f, -0.5f * REF_OFF);
        u32x4 mb = {__float_as_uint(m) >> 16, 0u, 0u, 0u};
        cc[i] = MFMA32(__builtin_bit_cast(bf16x8, e0), __builtin_bit_cast(bf16x8, mb), splat16(0.f));
        PIN(cc[i]);
    }
    u32x4 kf = k0;
    WAITV(0); lds_inc(lds0 + K2_MISC + SY_KVA);
    asm volatile("v_mfma_f32_32x32x16_bf16 v[128:143], v[208:211], v[192:195], v[64:79]" : "=&{v[128:143]}"(ss[0]) : "{v[208:211]}"(kf), "{v[192:195]}"(qr[0]), "{v[64:79]}"(cc[0]));
    lds_wait(lds0 + K2_MISC + SY_KVA, 4);
#pragma unroll 1
    for (int seg = 0; seg < 5; ++seg) {
        int n = 3;
        if (seg == 1) { n = 7; lds_wait(lds0 + K2_MISC + SY_KVB, 1); }
        else if (seg == 2) { n = 4; lds_wait(lds0 + K2_MISC + SY_KVB, 2); }
        else if (seg >= 3) { n = seg == 3 ? 9 : 4; lds_inc(lds0 + K2_MISC + (seg == 3 ? SY_P16 : SY_P26)); }
        asm volatile("s_nop 7\n\t.p2align 6\n1:\n\t"
            "v_mfma_f32_32x32x16_bf16 v[144:159], v[208:211], v[196:199], v[80:95]\n\tv_exp_f32 v128, v128\n\tv_exp_f32 v129, v129\n\tv_exp_f32 v130, v130\n\t"
            "v_exp_f32 v131, v131\n\tv_smfmac_f32_32x32x32_bf16 v[48:63], v[212:215], v[176:183], v220\n\tds_read_b128 v[212:215], v222\n\tv_exp_f32 v132, v132\n\t"
            "v_exp_f32 v133, v133\n\tv_exp_f32 v134, v134\n\tv_exp_f32 v135, v135\n\tv_smfmac_f32_32x32x32_bf16 v[48:63], v[216:219], v[184:191], v220\n\t"
            "ds_read_b128 v[216:219], v222 offset:512\n\tv_exp_f32 v136, v136\n\tv_exp_f32 v137, v137\n\tv_exp_f32 v138, v138\n\tv_exp_f32 v139, v139\n\t"
            "v_exp_f32 v140, v140\n\tv_exp_f32 v141, v141\n\tv_exp_f32 v142, v142\n\tv_exp_f32 v143, v143\n\t"
            "v_mfma_f32_32x32x16_bf16 v[160:175], v[208:211], v[200:203], v[96:111]\n\tv_exp_f32 v144, v144\n\tv_exp_f32 v145, v145\n\tv_exp_f32 v146, v146\n\t"
            "v_exp_f32 v147, v147\n\ts_waitcnt lgkmcnt(1)\n\tv_smfmac_f32_32x32x32_bf16 v[0:15], v[212:215], v[128:135], v220\n\tv_exp_f32 v148, v148\n\t"
            "v_exp_f32 v149, v149\n\tv_exp_f32 v150, v150\n\tv_exp_f32 v151, v151\n\ts_waitcnt lgkmcnt(0)\n\t"
            "v_smfmac_f32_32x32x32_bf16 v[0:15], v[216:219], v[136:143], v220\n\tv_add_u32 v222, v222, v223\n\tv_exp_f32 v152, v152\n\tv_exp_f32 v153, v153\n\t"
            "v_exp_f32 v154, v154\n\tv_exp_f32 v155, v155\n\tv_exp_f32 v156, v156\n\tv_exp_f32 v157, v157\n\tv_exp_f32 v158, v158\n\tv_exp_f32 v159, v159\n\t"
            "v_mfma_f32_32x32x16_bf16 v[176:191], v[208:211], v[204:207], v[112:127]\n\tds_read_b128 v[208:211], v221\n\tv_exp_f32 v160, v160\n\t"
            "v_exp_f32 v161, v161\n\tv_exp_f32 v162, v162\n\tv_exp_f32 v163, v163\n\tv_smfmac_f32_32x32x32_bf16 v[16:31], v[212:215], v[144:151], v220\n\t"
            "v_exp_f32 v164, v164\n\tv_exp_f32 v165, v165\n\tv_exp_f32 v166, v166\n\tv_exp_f32 v167, v167\n\t"
            "v_smfmac_f32_32x32x32_bf16 v[16:31], v[216:219], v[152:159], v220\n\tv_add_u32 v221, 0x400, v221\n\tv_exp_f32 v168, v168\n\tv_exp_f32 v169, v169\n\t"
            "v_exp_f32 v170, v170\n\tv_exp_f32 v171, v171\n\tv_exp_f32 v172, v172\n\tv_exp_f32 v173, v173\n\tv_exp_f32 v174, v174\n\tv_exp_f32 v175, v175\n\t"
            "s_waitcnt lgkmcnt(0)\n\tv_mfma_f32_32x32x16_bf16 v[128:143], v[208:211], v[192:195], v[64:79]\n\tv_exp_f32 v176, v176\n\tv_exp_f32 v177, v177\n\t"
            "v_exp_f32 v178, v178\n\tv_exp_f32 v179, v179\n\tv_smfmac_f32_32x32x32_bf16 v[32:47], v[212:215], v[160:167], v220\n\tv_exp_f32 v180, v180\n\t"
            "v_exp_f32 v181, v181\n\tv_exp_f32 v182, v182\n\tv_exp_f32 v183, v183\n\tv_smfmac_f32_32x32x32_bf16 v[32:47], v[216:219], v[168:175], v220\n\t"
            "v_exp_f32 v184, v184\n\tv_exp_f32 v185, v185\n\tv_exp_f32 v186, v186\n\tv_exp_f32 v187, v187\n\tv_exp_f32 v188, v188\n\tv_exp_f32 v189, v189\n\t"
            "s_sub_u32 %0, %0, 1\n\tv_exp_f32 v190, v190\n\tv_exp_f32 v191, v191\n\t"
            "s_cbranch_scc0 1b"
            : "+s"(n), "+{v[0:15]}"(oo[0]), "+{v[16:31]}"(oo[1]), "+{v[32:47]}"(oo[2]), "+{v[48:63]}"(oo[3]), "+{v[128:143]}"(ss[0]), "+{v[144:159]}"(ss[1]), "+{v[160:175]}"(ss[2]), "+{v[176:191]}"(ss[3]), "+{v[208:211]}"(kf), "+{v[212:215]}"(v0), "+{v[216:219]}"(v1), "+{v221}"(kaddr), "+{v222}"(vaddr)
            : "{v[64:79]}"(cc[0]), "{v[80:95]}"(cc[1]), "{v[96:111]}"(cc[2]), "{v[112:127]}"(cc[3]), "{v[192:195]}"(qr[0]), "{v[196:199]}"(qr[1]), "{v[200:203]}"(qr[2]), "{v[204:207]}"(qr[3]), "{v220}"(sidx), "{v223}"(vinc)
            : "scc", "memory");
    }
    asm volatile("v_smfmac_f32_32x32x32_bf16 v[48:63], v[212:215], v[176:183], v220\n\ts_nop 0\n\tv_smfmac_f32_32x32x32_bf16 v[48:63], v[216:219], v[184:191], v220\n\ts_nop 7\n\ts_nop 7"
            : "+{v[48:63]}"(oo[3]) : "{v[176:191]}"(ss[3]), "{v[212:215]}"(v0), "{v[216:219]}"(v1), "{v220}"(sidx));
    __builtin_amdgcn_s_setprio(3);
    lds_wait(lds0 + K2_MISC + SY_P26, 4);
#pragma unroll
    for (int i = 0; i < 2; ++i) {
        f32x4 a = {oo[2 + i][0], oo[2 + i][1], oo[2 + i][2], oo[2 + i][3]}, b = {oo[2 + i][4], oo[2 + i][5], oo[2 + i][6], oo[2 + i][7]};
        *(LAS f32x4*)(uintptr_t)(cwa + i * 2048) = a;
        *(LAS f32x4*)(uintptr_t)(cwa + i * 2048 + 1024) = b;
    }
    { f32x2 l2 = {oo[2][8], oo[3][8]}; *(LAS f32x2*)(uintptr_t)(cwa + 4096) = l2; }
    asm volatile("s_waitcnt lgkmcnt(0)" ::: "memory");
    lds_inc(lds0 + K2_MISC + SY_CMB + qh * 4);
    lds_wait(lds0 + K2_MISC + SY_CMB + qh * 4, 2);
    {
        const f32x2 l2 = *(LAS f32x2*)(uintptr_t)(cra + 4096);
#pragma unroll
        for (int i = 0; i < 2; ++i) {
            const f32x4 a = *(LAS f32x4*)(uintptr_t)(cra + i * 2048), b = *(LAS f32x4*)(uintptr_t)(cra + i * 2048 + 1024);
#pragma unroll
            for (int e = 0; e < 4; ++e) { oo[i][e] += a[e]; oo[i][4 + e] += b[e]; }
            oo[i][8] += l2[i];
            PIN(oo[i]);
        }
    }
    const bool ok = (att_mag(oo[0]) + att_mag(oo[1]) < 0x1p120f) & (fminf(oo[0][8], oo[1][8]) > 0x1p-100f);
    if (__builtin_expect(__builtin_amdgcn_ballot_w64(!ok) != 0, 0)) att_exact(kg, vg, QF(0), QF(1), oo[0], oo[1]);
    att_store(oo[0], dst, l16);
    att_store(oo[1], dst + CAT_KS * 1024, l16);
    publish(cnt);
}

template <int CTRL> DI float dppf(float x) { return __builtin_bit_cast(float, __builtin_amdgcn_mov_dpp(__builtin_bit_cast(int, x), CTRL, 0xf, 0xf, true)); }
DI float fold32(float a, float b) {
    auto r = __builtin_amdgcn_permlane32_swap(__float_as_uint(a), __float_as_uint(b), false, false);
    return __uint_as_float(r[0]) + __uint_as_float(r[1]);
}
DI float fold16(float a, float b) {
    auto r = __builtin_amdgcn_permlane16_swap(__float_as_uint(a), __float_as_uint(b), false, false);
    return __uint_as_float(r[0]) + __uint_as_float(r[1]);
}
DI float rowsum16(float v) { v += dppf<0xB1>(v); v += dppf<0x4E>(v); v += dppf<0x124>(v); v += dppf<0x128>(v); return v; }

DI void spg_role(int b, int head, int w, const char* __restrict__ wsp, const __hip_bfloat16* __restrict__ bsp, char* __restrict__ ws, unsigned lds0,
                 const char* __restrict__ x, const char* __restrict__ wo, const char* __restrict__ wout, char* __restrict__ out, bool seen) {
    const int lane = threadIdx.x & 63;
    const int xc = b & 7, rr = b >> 3;
    const int tt = xc * 4 + (rr & 3), ft = rr >> 2;
    const bool active = ft < 7;
    const unsigned wring = lds0 + K2_W + w * 8192;
    const __amdgpu_buffer_rsrc_t wr = __builtin_amdgcn_make_buffer_rsrc((void*)wsp, (short)0, S_ * S_ * 2, 0x00020000);
    unsigned wso = tt * 64 * 4096 + w * 1024;
    unsigned wvoff[2];
#pragma unroll
    for (int p = 0; p < 2; ++p) wvoff[p] = (lane >> 3) * 4096 + (((lane & 7) ^ ((4 * p + (lane >> 4)) & 7)) << 4);
#define ISSUEW(i) BLDS(wr, wvoff[(i) & 1], wso, (i) * 32768, wring, (i) * 1024)
    auto poll = [&](unsigned idx, unsigned cnt, int n) {
        const unsigned fo = idx * CNT_STRIDE;
        char* fl = ws + WS_FLAG;
#pragma unroll 1
        for (int it = 0; it < SPIN_MAX; ++it) {
            unsigned f = cnt;
            if (lane < n) { const unsigned z = 0; asm volatile("global_atomic_add %0, %1, %2, %3 sc0\n\ts_waitcnt vmcnt(0)" : "=&v"(f) : "v"(fo), "v"(z), "s"(fl) : "memory"); }
            if (__all(f == cnt)) break;
            if ((it & 63) == 63) add16(fl + CNT_HB * CNT_STRIDE, 8);
            __builtin_amdgcn_s_sleep(2);
        }
    };
    f32x16 acc[4][2];
    unsigned zvoff[4], waddr[4], lco, lc;
#define SETUP() do { _Pragma("unroll") for (int j = 0; j < 4; ++j) { \
            acc[j][0] = splat16(0.f); acc[j][1] = splat16(0.f); PIN(acc[j][0]); PIN(acc[j][1]); \
            zvoff[j] = (lane >> 5) * (FH * 16) + (j ^ w) * 512 + (lane & 31) * 16; PIN(zvoff[j]); \
            waddr[j] = wring + (lane & 31) * 128 + (((j * 2 + (lane >> 5)) ^ ((lane >> 1) & 7)) << 4); PIN(waddr[j]); } \
        lco = (lane ? CNT_Z1 + (tt >> 1) : ((b >> 3) & 15) * 16 + 8 + w) * CNT_STRIDE; lc = 0; PIN(lco); PIN(lc); \
        PIN(wvoff[0]); PIN(wvoff[1]); } while (0)
    PRIO_HI();
    if (w == 3) {
        const __amdgpu_buffer_rsrc_t kr = __builtin_amdgcn_make_buffer_rsrc((void*)(ws + WS_K), (short)0, 4u << 20, 0x00020000);
        const unsigned lo = lane * 16;
        unsigned kso = head * 65536 + 5 * 1024, kd = lds0 + 5 * 1024;
        if (!seen) { poll(CNT_QKV + (head >> 3) * 8 + ((b >> 3) & 7), 64, 1); lds_inc(lds0 + K2_MISC + SY_QKV); }
        SETUP();
        PRIO_LO();
#define KV4() do { BLDSC(kr, lo, kso, 0, kd, K2_K - 1024); BLDSC(kr, lo, kso, WS_V - WS_K, kd, K2_V); BLDSC(kr, lo, kso, 32768, kd, K2_K + 31744 - 1024); \
                   BLDSC(kr, lo, kso, WS_V - WS_K + 32768, kd, K2_V + 32768); kso += 1024; kd += 1024; } while (0)
#pragma unroll 1
        for (int t = 5; t < 20; ++t) KV4();
        WAITV(28);
        PRIO_HI(); lds_inc(lds0 + K2_MISC + SY_KVB); PRIO_LO();
        ISSUEW(0); ISSUEW(1); ISSUEW(2); ISSUEW(3); ISSUEW(4); ISSUEW(5); ISSUEW(6); ISSUEW(7);
#pragma unroll 1
        for (int t = 20; t < 32; ++t) { KV4(); WAITV(44); }
        PRIO_HI();
    } else {
        const int id1 = id1_of(b), g = grp_of(id1);
        const char* src = ws + WS_Z2 + (size_t)g * FH * 16;
        u32x4 zc[5];
        const unsigned o0 = w * 1024 + lane * 16, o4 = o0 + (w < 2 ? 12288 : 9216);
        if (w == 0) { if (!seen) { poll(CNT_Z2 + (g >> 4), 8, 1); lds_inc(lds0 + K2_MISC + SY_Z2); } } else lds_wait<1>(lds0 + K2_MISC + SY_Z2, 1);
        GLD16C(zc[0], o0, src, 0); GLD16C(zc[1], o0, src, 3072); GLD16C(zc[2], o0, src + 6144, 0); GLD16C(zc[3], o0, src + 6144, 3072); GLD16C(zc[4], o4, src, 0);
        SETUP();
        PRIO_LO();
        ISSUEW(0); ISSUEW(1); ISSUEW(2); ISSUEW(3); ISSUEW(4); ISSUEW(5); ISSUEW(6); ISSUEW(7);
        asm volatile("s_waitcnt vmcnt(8)" : "+v"(zc[0]), "+v"(zc[1]), "+v"(zc[2]), "+v"(zc[3]), "+v"(zc[4]) :: "memory");
        PRIO_HI();
        char* dst = ws + WS_Z2N + (size_t)g * FH * 16;
        if (w == 2) zc[4] = u32x4{0, 0, 0, 0};
        float s1[8], s2[8];
#pragma unroll
        for (int e = 0; e < 8; ++e) {
            float v[5];
#pragma unroll
            for (int j = 0; j < 5; ++j) v[j] = (e & 1) ? bfhi(zc[j][e >> 1]) : bflo(zc[j][e >> 1]);
            s1[e] = ((v[0] + v[1]) + (v[2] + v[3])) + v[4];
            s2[e] = __builtin_fmaf(v[4], v[4], __builtin_fmaf(v[3], v[3], __builtin_fmaf(v[2], v[2], __builtin_fmaf(v[1], v[1], v[0] * v[0]))));
        }
        f32x4 q;
#pragma unroll
        for (int i = 0; i < 4; ++i) q[i] = rowsum16(fold16(fold32(s1[i], s2[i]), fold32(s1[i + 4], s2[i + 4])));
        LAS float* part = (LAS float*)(uintptr_t)(lds0 + K2_MISC + 256);
        *(LAS f32x4*)(part + w * 16 + (lane >> 4) * 4) = q;
        asm volatile("s_waitcnt lgkmcnt(0)" ::: "memory");
        lds_inc(lds0 + K2_MISC + SY_LN);
        lds_wait(lds0 + K2_MISC + SY_LN, 3);
        f32x4 rn[4];
        {
            const int e = lane & 7;
            const float mean = ((part[e] + part[16 + e]) + part[32 + e]) * (1.0f / FH);
            const float var = fmaxf(((part[8 + e] + part[24 + e]) + part[40 + e]) * (1.0f / FH) - mean * mean, 0.f);
            const float r1 = __builtin_amdgcn_rsqf(var + 1e-5f);
            LAS float* bc = part + 64 + w * 16;
            bc[lane & 15] = (lane & 8) ? -mean * r1 : r1;
#pragma unroll
            for (int i = 0; i < 4; ++i) rn[i] = *(LAS f32x4*)(bc + i * 4);
        }
#pragma unroll
        for (int j = 0; j < 5; ++j) {
            u32x4 v;
#pragma unroll
            for (int e = 0; e < 4; ++e) v[e] = pk2(__builtin_fmaf(bflo(zc[j][e]), rn[e >> 1][(2 * e) & 3], rn[2 + (e >> 1)][(2 * e) & 3]), __builtin_fmaf(bfhi(zc[j][e]), rn[e >> 1][(2 * e + 1) & 3], rn[2 + (e >> 1)][(2 * e + 1) & 3]));
            if (j < 4) GST16(o0, v, dst + j * 3072); else if (w < 2) GST16(o4, v, dst);
        }
        WAITV(0);
        unsigned old = 0;
        if (lane == 0) old = __hip_atomic_fetch_add((LAS unsigned*)(uintptr_t)(lds0 + K2_MISC + SY_LNST), 1u, __ATOMIC_RELAXED, __HIP_MEMORY_SCOPE_WORKGROUP);
        old = __builtin_amdgcn_readfirstlane(old);
        if (old == 2) {
            add16(ws + WS_FLAG + (size_t)(id1 < 96 ? 8 + (id1 & 3) : id1 & 7) * CNT_STRIDE, 12);
            if (!active) { WAITV(0); lds_inc(lds0 + K2_MISC + SY_FLAG); }
        }
    }
    const unsigned l16 = lane * 16;
    auto reg = [&](int ww) { return lds0 + ((ww & 1) ? K2_V + (ww >> 1) * 32768 : K2_K + (ww >> 1) * 31744); };
    if (active) {
        const char* zb = ws + WS_Z2N + ((size_t)w * 64 * FH + ft * 128) * 16;
        u32x4 zf[4][4];
#define ZPTR(c, ks) (zb + (size_t)((c) * 4 + (ks)) * (2 * FH * 16))
        if (w == 0) { poll(((b >> 3) & 15) * 16 + lane, 20, 8); lds_inc(lds0 + K2_MISC + SY_FLAG); }
        else lds_wait<2>(lds0 + K2_MISC + SY_FLAG, 1);
        PRIO_LO();
#pragma unroll
        for (int ks = 0; ks < 4; ++ks)
#pragma unroll
            for (int j = 0; j < 4; ++j) GLD16S(zf[ks][j], zvoff[j], ZPTR(0, ks));
        if (w == 3) { WAITV(16); PRIO_HI(); lds_inc(lds0 + K2_MISC + SY_KVB); PRIO_LO(); }
#define ZWAIT(n, ks) asm volatile("s_waitcnt vmcnt(" #n ")" : "+v"(zf[ks][0]), "+v"(zf[ks][1]), "+v"(zf[ks][2]), "+v"(zf[ks][3]) :: "memory")
#define KPAIR(ks, j_, MORE) do { \
        acc[j_][0] = MFMA32(__builtin_bit_cast(bf16x8, zf[ks][j_]), wf[0][ks], acc[j_][0]); PIN(acc[j_][0]); SB(); \
        if (MORE && ks == 0) ISSUEW(2 * j_); \
        acc[j_][1] = MFMA32(__builtin_bit_cast(bf16x8, zf[ks][j_]), wf[1][ks], acc[j_][1]); PIN(acc[j_][1]); SB(); \
        if (MORE && ks == 0) ISSUEW(2 * j_ + 1); \
        if (MORE) GLD16S(zf[ks][j_], zvoff[j_], ZPTR(c + 1, ks)); } while (0)
#define KSTEP(ks, MORE) do { KPAIR(ks, 0, MORE); KPAIR(ks, 1, MORE); KPAIR(ks, 2, MORE); KPAIR(ks, 3, MORE); } while (0)
#define WREAD() _Pragma("unroll") for (int ks = 0; ks < 4; ++ks) { wf[0][ks] = ldsfrag(waddr[ks]); wf[1][ks] = ldsfrag(waddr[ks] + 4096); } \
        asm volatile("s_waitcnt lgkmcnt(0)" : "+v"(wf[0][0]), "+v"(wf[0][1]), "+v"(wf[0][2]), "+v"(wf[0][3]), "+v"(wf[1][0]), "+v"(wf[1][1]), "+v"(wf[1][2]), "+v"(wf[1][3]) :: "memory")
#pragma unroll 1
        for (int c = 0; c < 7; ++c) {
            bf16x8 wf[2][4];
            wso += 128;
            if (c == 2) asm volatile("s_mov_b64 exec, 3\n\tglobal_atomic_add %0, %1, %0, %2 sc0\n\ts_mov_b64 exec, -1" : "+v"(lc) : "v"(lco), "s"(ws + WS_FLAG) : "memory");
            ZWAIT(12, 0);
            if (c == 4) {
                asm volatile("" : "+v"(lc));
                if (__builtin_amdgcn_readlane(lc, 0) != 24 || __builtin_amdgcn_readlane(lc, 1) != 8) poll(lco / CNT_STRIDE, lane ? 8 : 24, 2);
            }
            WREAD();
            KSTEP(0, true);
            ZWAIT(20, 1); KSTEP(1, true);
            ZWAIT(20, 2); KSTEP(2, true);
            ZWAIT(20, 3); KSTEP(3, true);
        }
        const int fs0 = ft * 8 + w * 2;
        const char* z1p = ws + WS_Z1 + ((size_t)tt * 2 * 56 + fs0) * 1024;
        const char* bp = (const char*)bsp + tt * 128;
        const unsigned bo = (lane & 31) * 2;
        unsigned braw[2];
        auto blk = [&](int dstw, int j) { return (j < 3 ? reg(dstw) + (j - 1) * 8192 : lds0 + K2_W + dstw * 8192) + l16; };
        {
            const int c = 7; (void)c;
            bf16x8 wf[2][4];
            ZWAIT(12, 0);
            WREAD();
            PRIO_HI();
            lds_inc(lds0 + K2_MISC + SY_MAIN);
            lds_wait(lds0 + K2_MISC + SY_P16, 4);
            unsigned wa[3] = {blk(1 ^ w, 1), blk(2 ^ w, 2), blk(3 ^ w, 3)};
            PIN(wa[0]); PIN(wa[1]); PIN(wa[2]); SB();
            PRIO_LO();
            KSTEP(0, false);
            GLD16C(zf[0][0], l16, z1p, 0); GLD16C(zf[0][1], l16, z1p, 1024); GLD16C(zf[0][2], l16, z1p + 56 * 1024, 0); GLD16C(zf[0][3], l16, z1p + 56 * 1024, 1024);
            asm volatile("global_load_ushort %0, %1, %2" : "=v"(braw[0]) : "v"(bo), "s"(bp) : "memory");
            asm volatile("global_load_ushort %0, %1, %2 offset:64" : "=v"(braw[1]) : "v"(bo), "s"(bp) : "memory");
            ZWAIT(14, 1); KSTEP(1, false);
            ZWAIT(10, 2); ZWAIT(6, 3);
            sfor<16>(LAMBDA(N) {
                constexpr int n = N, j = ((n >> 2) + 1) & 3, ks = 2 + ((n >> 1) & 1), t = n & 1, jw = n >> 2;
                if (n == 12) { PRIO_HI(); lds_wait(lds0 + K2_MISC + SY_MAIN, 4); PRIO_LO(); }
                acc[j][t] = MFMA32(__builtin_bit_cast(bf16x8, zf[ks][j]), wf[t][ks], acc[j][t]); PIN(acc[j][t]); SB();
                if constexpr (jw > 0) {
#pragma unroll
                    for (int u = 0; u < 2; ++u) {
                        constexpr int tw = (n >> 1) & 1; const int q = (n & 1) * 2 + u;
                        f32x4 v = {acc[jw][tw][4 * q], acc[jw][tw][4 * q + 1], acc[jw][tw][4 * q + 2], acc[jw][tw][4 * q + 3]};
                        *(LAS f32x4*)(uintptr_t)(wa[jw - 1] + (tw * 4 + q) * 1024) = v;
                    }
                    SB();
                }
            });
        }
        asm volatile("s_waitcnt lgkmcnt(0)" ::: "memory");
        PRIO_HI();
        lds_inc(lds0 + K2_MISC + SY_RED);
        lds_wait(lds0 + K2_MISC + SY_RED, 4);
#pragma unroll
        for (int j = 1; j < 4; ++j) {
            const unsigned a = blk(w, j);
#pragma unroll
            for (int t = 0; t < 2; ++t)
#pragma unroll
                for (int q = 0; q < 4; ++q) {
                    f32x4 v = *(LAS f32x4*)(uintptr_t)(a + (t * 4 + q) * 1024);
#pragma unroll
                    for (int e = 0; e < 4; ++e) acc[0][t][4 * q + e] += v[e];
                }
        }
        asm volatile("s_waitcnt vmcnt(0)" : "+v"(zf[0][0]), "+v"(zf[0][1]), "+v"(zf[0][2]), "+v"(zf[0][3]), "+v"(braw[0]), "+v"(braw[1]) :: "memory");
        const float bias[2] = {bflo(braw[0]), bflo(braw[1])};
#pragma unroll
        for (int t = 0; t < 2; ++t)
#pragma unroll
            for (int s = 0; s < 2; ++s) {
                auto a0 = __builtin_amdgcn_permlane32_swap(zf[0][2 * t + s][0], zf[0][2 * t + s][2], false, false);
                auto a1 = __builtin_amdgcn_permlane32_swap(zf[0][2 * t + s][1], zf[0][2 * t + s][3], false, false);
                const unsigned zc[4] = {a0[0], a1[0], a0[1], a1[1]};
                unsigned p[4];
#pragma unroll
                for (int e = 0; e < 4; ++e)
                    p[e] = pk2(bflo(zc[e]) * (acc[0][t][8 * s + 2 * e] + bias[t]), bfhi(zc[e]) * (acc[0][t][8 * s + 2 * e + 1] + bias[t]));
                auto s0 = __builtin_amdgcn_permlane32_swap(p[0], p[2], false, false);
                auto s1 = __builtin_amdgcn_permlane32_swap(p[1], p[3], false, false);
                u32x4 v = {s0[0], s1[0], s0[1], s1[1]};
                GST16(l16, v, ws + WS_CAT + ((size_t)(tt * 2 + t) * CAT_KS + 32 + fs0 + s) * 1024);
            }
        publish(ws + WS_FLAG + (size_t)(CNT_REC + tt * 8 + (ft * 4 + w) / 7) * CNT_STRIDE);
    } else {
        const int id = (rr & 3) * 4 + w;
        if (w == 3) { WAITV(0); lds_inc(lds0 + K2_MISC + SY_KVB); }
        lds_wait<4>(lds0 + K2_MISC + SY_KVB, 2);
        lds_wait<4>(lds0 + K2_MISC + SY_FLAG, 1);
        PRIO_LO();
#pragma unroll 1
        for (int c = 1; c < 8; ++c) { wso += 128; ISSUEW(0); ISSUEW(1); ISSUEW(2); ISSUEW(3); ISSUEW(4); ISSUEW(5); ISSUEW(6); ISSUEW(7); WAITV(32); }
        lds_wait<32>(lds0 + K2_MISC + SY_P16, 4);
#pragma unroll 1
        for (int i = 0; i < 56; ++i) { glds16(wout + (size_t)(id * 56 + i) * 1024, l16, wring); if ((i & 7) == 7) WAITV(32); }
#pragma unroll 1
        for (int i = 0; i < 32; ++i) { glds16(wo + (size_t)(id * 32 + i) * 1024, l16, wring); if ((i & 7) == 7) WAITV(32); }
#pragma unroll 1
        for (int i = 0; i < 16; ++i) glds16(x + (size_t)(xc * 256 + id * 16 + i) * 1024, l16, wring);
        WAITV(0);
        PRIO_HI();
    }
    {
        const __amdgpu_buffer_rsrc_t hr = __builtin_amdgcn_make_buffer_rsrc((void*)wout, (short)0, FH * 1024, 0x00020000);
        const __amdgpu_buffer_rsrc_t orr = __builtin_amdgcn_make_buffer_rsrc((void*)wo, (short)0, D_ * 1024, 0x00020000);
        unsigned bvoff = ((lane >> 2) & 7) * 1024 + (lane >> 5) * 64 + (lane & 3) * 16;
        const unsigned hso = ft * 128 + w * 14 * 16384, oso = ft * 128 + w * 8 * 16384;
        const unsigned rg = reg(w);
#define FB(rs, so, s, base, off) do { BLDS(rs, bvoff, so, (s) * 16384, base, off); BLDS(rs, bvoff, so, (s) * 16384 + 8192, base, (off) + 1024); } while (0)
        const unsigned bl = (lane >> 5) * 1024 + (w & 1) * 512 + ((lane & 15) >> 2) * 64 + ((lane >> 4) & 1) * 32 + (lane & 3) * 8;
        unsigned br0 = rg + bl, br1 = rg + (bl ^ 512), bw0 = wring + bl, bw1 = wring + (bl ^ 512);
        unsigned xo = (lane & 31) * 1024 + (lane >> 5) * 16;
        PIN(bvoff); PIN(br0); PIN(br1); PIN(bw0); PIN(bw1); PIN(xo); SB();
        PRIO_LO();
        FB(hr, hso, 0, rg, 0); FB(hr, hso, 1, rg, 2048); FB(hr, hso, 2, rg, 4096); FB(hr, hso, 3, rg, 6144);
        FB(hr, hso, 4, rg, 8192); FB(hr, hso, 5, rg, 10240); FB(hr, hso, 6, rg, 12288); FB(hr, hso, 7, rg, 14336);
        FB(hr, hso, 8, wring, 0); FB(hr, hso, 9, wring, 2048); FB(hr, hso, 10, wring, 4096); FB(hr, hso, 11, wring, 6144);
        const size_t oo_ = (size_t)(tt * 64 + (w >> 1) * 32) * 1024 + (ft * 64 + (w & 1) * 32) * 2;
        u32x4 xv[2];
        GLD16(xv[0], xo, x + oo_, 0); GLD16(xv[1], xo, x + oo_, 32);
        PRIO_HI();
        lds_wait<8>(lds0 + K2_MISC + SY_P26, 4);
        FB(hr, hso, 12, rg, 16384); FB(hr, hso, 13, rg, 18432);
        const char* a0p = ws + WS_CAT + (size_t)(tt * 2 + (w >> 1)) * CAT_KS * 1024;
        const char* a1p = ws + WS_CAT + (size_t)(tt * 2 + ((w >> 1) ^ 1)) * CAT_KS * 1024;
        u32x4 af[14][2];
        f32x16 fc[2][2];
        bf16x8 bq[2][2];
#define SLOT0(s) ((s) < 8 ? br0 + (s) * 2048 : (s) < 12 ? bw0 + ((s) - 8) * 2048 : br0 + 16384 + ((s) - 12) * 2048)
#define SLOT1(s) ((s) < 8 ? br1 + (s) * 2048 : (s) < 12 ? bw1 + ((s) - 8) * 2048 : br1 + 16384 + ((s) - 12) * 2048)
#define BQ(s) do { bq[(s) & 1][0] = trfrag(SLOT0(s), SLOT0(s) + 256); bq[(s) & 1][1] = trfrag(SLOT1(s), SLOT1(s) + 256); } while (0)
#define AWAIT(n, s) asm volatile("s_waitcnt vmcnt(%c2)" : "+v"(af[s][0]), "+v"(af[s][1]) : "i"(n) : "memory")
#define FMM(s, FIRST) do { _Pragma("unroll") for (int i_ = 0; i_ < 2; ++i_) _Pragma("unroll") for (int j_ = 0; j_ < 2; ++j_) { \
            fc[i_][j_] = MFMA32(bq[(s) & 1][j_], __builtin_bit_cast(bf16x8, af[s][i_]), (FIRST) ? splat16(0.f) : fc[i_][j_]); PIN(fc[i_][j_]); } SB(); } while (0)
        poll(CNT_REC + tt * 8 + w, 7, 1);
        PRIO_LO();
#pragma unroll
        for (int s = 0; s < 14; ++s) { GLD16S(af[s][0], l16, a0p + (32 + w * 14 + s) * 1024); GLD16S(af[s][1], l16, a1p + (32 + w * 14 + s) * 1024); }
        BQ(0);
#define HST(s) do { if (s < 13) BQ(s + 1); AWAIT(2 * (13 - s) + (s > 11 ? 16 : 0), s); FMM(s, s == 0); } while (0)
        HST(0); HST(1); HST(2); HST(3); HST(4); HST(5); HST(6); HST(7); HST(8);
        HST(9); HST(10); HST(11);
        FB(orr, oso, 0, rg, 0); FB(orr, oso, 1, rg, 2048); FB(orr, oso, 2, rg, 4096); FB(orr, oso, 3, rg, 6144);
        FB(orr, oso, 4, rg, 8192); FB(orr, oso, 5, rg, 10240); FB(orr, oso, 6, rg, 12288); FB(orr, oso, 7, rg, 14336);
        HST(12); HST(13);
        PRIO_HI();
        poll(CNT_REC + tt * 8 + 4 + w, 8, 1);
#pragma unroll
        for (int s = 0; s < 8; ++s) { GLD16S(af[s][0], l16, a0p + (w * 8 + s) * 1024); GLD16S(af[s][1], l16, a1p + (w * 8 + s) * 1024); }
        BQ(0);
#define OST(s) do { if (s < 7) BQ(s + 1); AWAIT(2 * (7 - s), s); FMM(s, false); } while (0)
        OST(0); OST(1); OST(2); OST(3); OST(4); OST(5); OST(6); OST(7);
#pragma unroll
        for (int j = 1; j < 4; ++j)
#pragma unroll
            for (int q = 0; q < 4; ++q) {
                const f32x16& t = fc[j >> 1][j & 1];
                f32x4 v = {t[4 * q], t[4 * q + 1], t[4 * q + 2], t[4 * q + 3]};
                *(LAS f32x4*)(uintptr_t)(rg + (j * 4 + q) * 1024 + l16) = v;
            }
        asm volatile("s_waitcnt lgkmcnt(0)" ::: "memory");
        lds_inc(lds0 + K2_MISC + SY_FIN);
        lds_wait(lds0 + K2_MISC + SY_FIN, 4);
#pragma unroll
        for (int j = 1; j < 4; ++j)
#pragma unroll
            for (int q = 0; q < 4; ++q) {
                f32x4 v = *(LAS f32x4*)(uintptr_t)(reg(w ^ j) + (j * 4 + q) * 1024 + l16);
#pragma unroll
                for (int e = 0; e < 4; ++e) fc[0][0][4 * q + e] += v[e];
            }
        asm volatile("" : "+v"(xv[0]), "+v"(xv[1]));
#pragma unroll
        for (int s = 0; s < 2; ++s) {
            auto a0 = __builtin_amdgcn_permlane32_swap(xv[s][0], xv[s][2], false, false);
            auto a1 = __builtin_amdgcn_permlane32_swap(xv[s][1], xv[s][3], false, false);
            const unsigned xc4[4] = {a0[0], a1[0], a0[1], a1[1]};
            unsigned p[4];
#pragma unroll
            for (int e = 0; e < 4; ++e) p[e] = pk2(bflo(xc4[e]) + fc[0][0][8 * s + 2 * e], bfhi(xc4[e]) + fc[0][0][8 * s + 2 * e + 1]);
            auto s0 = __builtin_amdgcn_permlane32_swap(p[0], p[2], false, false);
            auto s1 = __builtin_amdgcn_permlane32_swap(p[1], p[3], false, false);
            u32x4 v = {s0[0], s1[0], s0[1], s1[1]};
            *(u32x4*)(out + oo_ + xo + s * 32) = v;
        }
    }
}

__global__ __launch_bounds__(512, 2) NOPK void k_block(const char* __restrict__ x, const char* __restrict__ wqkv, const char* __restrict__ wo, const char* __restrict__ win,
                                                       const char* __restrict__ wsp, const __hip_bfloat16* __restrict__ bsp, const char* __restrict__ wout,
                                                       char* __restrict__ ws, char* __restrict__ out) {
    extern __shared__ __attribute__((aligned(16))) char smem[];
    const unsigned lds0 = (unsigned)(uintptr_t)(LAS char*)smem;
    const bool seen = phase1(x, wqkv, win, ws, wsp, lds0);
    const int wid = __builtin_amdgcn_readfirstlane(threadIdx.x >> 6);
    const int b = blockIdx.x;
    const int head = (b & 7) * 4 + (b >> 6);
    if (wid < 4) att_role(head, (b >> 3) & 7, wid, ws, lds0);
    else spg_role(b, head, wid - 4, wsp, bsp, ws, lds0, x, wo, wout, out, seen);
}

extern "C" void kernel_launch(void* const* d_in, const int* in_sizes, int n_in, void* d_out, int out_size, void* d_ws, size_t ws_size,
                              hipStream_t stream) {
    (void)hipFuncSetAttribute((const void*)k_block, hipFuncAttributeMaxDynamicSharedMemorySize, K2_LDS);
    hipLaunchKernelGGL(k_block, dim3(256), dim3(512), K2_LDS, stream, (const char*)d_in[0], (const char*)d_in[1], (const char*)d_in[2], (const char*)d_in[4],
                       (const char*)d_in[5], (const __hip_bfloat16*)d_in[6], (const char*)d_in[7], (char*)d_ws, (char*)d_out);
}
```
